```python
import torch

def _softmax(x, axis=-1):
    return torch.softmax(x, dim=axis)
def _gelu(x):
    return torch.nn.functional.gelu(x, approximate="tanh")
def _silu(x):
    return torch.nn.functional.silu(x)
def _sigmoid(x):
    return torch.sigmoid(x)
def _layernorm(x, axis=-1, eps=1e-5):
    m = x.mean(dim=axis, keepdim=True)
    v = x.var(dim=axis, keepdim=True, unbiased=False)
    return (x - m) / torch.sqrt(v + eps)
def _scatter_add(tgt, idx, src, axis=0):
    return tgt.clone().index_add_(axis, idx, src)
def _segment_sum(src, seg, n):
    out = torch.zeros((n,) + src.shape[1:], dtype=src.dtype)
    return out.index_add_(0, seg, src)
def _pad(x, pw):
    flat = tuple(v for pair in reversed(pw) for v in pair)
    return torch.nn.functional.pad(x, flat)

kernel_name = 'parallel_sum__sliding_block__dense_activation_gelu__prompt'

def setup_inputs():

    _w = (lambda a: (lambda t: t.to(torch.bfloat16) if t.is_floating_point() else t)(torch.as_tensor(a)))
    def _A_setup():
        B, S, D = 1, 2048, 1024
        return {
            "x": _w((lambda *s: torch.randn(*s, dtype=torch.bfloat16) * 0.05)(B, S, D)),
            "wqkv": _w((lambda *s: torch.randn(*s, dtype=torch.bfloat16) * 0.05)(D, 3 * D)),
            "wo": _w((lambda *s: torch.randn(*s, dtype=torch.bfloat16) * 0.05)(D, D)),
        }
    _A_in = _A_setup()

    def _B_setup():
        B, S, D, F = 1, 2048, 1024, 3584
        return {
            "x": _w((lambda *s: torch.randn(*s, dtype=torch.bfloat16) * 0.05)(B, S, D)),
            "w1": _w((lambda *s: torch.randn(*s, dtype=torch.bfloat16) * 3.2)(D, F) * (D ** -0.5)),
            "b1": _w((lambda *s: torch.randn(*s, dtype=torch.bfloat16) * 3.2)(F) * 0.01),
            "w2": _w((lambda *s: torch.randn(*s, dtype=torch.bfloat16) * 0.4)(F, D) * (F ** -0.5)),
            "b2": _w((lambda *s: torch.randn(*s, dtype=torch.bfloat16) * 0.4)(D) * 0.01),
        }
    _B_in = _B_setup()

    return {
        "A_x": _A_in["x"],
        "A_wqkv": _A_in["wqkv"],
        "A_wo": _A_in["wo"],
        "B_x": _B_in["x"],
        "B_w1": _B_in["w1"],
        "B_b1": _B_in["b1"],
        "B_w2": _B_in["w2"],
        "B_b2": _B_in["b2"],
    }

def reference(A_x, A_wqkv, A_wo, B_x, B_w1, B_b1, B_w2, B_b2):
    def _A_ref(x, wqkv, wo):
        B, S, D = x.shape
        H = 32; Dh = D // H; W = 256
        qkv = (lambda x, *d: x.permute(*d))((x @ wqkv).reshape(B, S, 3, H, Dh), 2, 0, 3, 1, 4)
        q, k, v = qkv[0], qkv[1], qkv[2]
        scores = torch.einsum("bhqd,bhkd->bhqk", q, k) * (Dh ** -0.5)
        idx = torch.arange(S)
        mask = (idx[:, None] - idx[None, :] >= 0) & (idx[:, None] - idx[None, :] < W)
        scores = torch.where(mask[None, None], scores, -1e9)
        p = _softmax(scores, axis=-1)
        o = torch.einsum("bhqk,bhkd->bhqd", p, v)
        return (lambda x, *d: x.permute(*d))(o, 0, 2, 1, 3).reshape(B, S, D) @ wo

    def _B_ref(x, w1, b1, w2, b2):
        h = torch.einsum("bsd,df->bsf", x, w1) + b1
        h = _gelu(h)
        return torch.einsum("bsf,fd->bsd", h, w2) + b2

    x = A_x
    xn = _layernorm(x, axis=-1)
    return x + _A_ref(xn, A_wqkv, A_wo) + _B_ref(xn, B_w1, B_b1, B_w2, B_b2)
```

```cpp
#include <hip/hip_runtime.h>
#include <hip/hip_bf16.h>
typedef unsigned short u16;
__device__ __forceinline__ float bf2f(u16 v){ return __uint_as_float(((unsigned)v)<<16); }
__device__ __forceinline__ u16 f2bf(float f){ unsigned u=__float_as_uint(f); u = (u + 0x7FFF + ((u>>16)&1))>>16; return (u16)u; }
constexpr int S=2048, D=1024, F=3584, H=32, DH=32, W=256;

__global__ void ln_k(const u16* x, u16* xn){
  int row=blockIdx.x; int t=threadIdx.x;
  __shared__ float red[256];
  float v[4]; float s=0;
  for(int i=0;i<4;i++){ v[i]=bf2f(x[row*D+t*4+i]); s+=v[i]; }
  red[t]=s; __syncthreads();
  for(int o=128;o>0;o>>=1){ if(t<o) red[t]+=red[t+o]; __syncthreads(); }
  float mean=red[0]/D; __syncthreads();
  float q=0; for(int i=0;i<4;i++){ float d=v[i]-mean; q+=d*d; }
  red[t]=q; __syncthreads();
  for(int o=128;o>0;o>>=1){ if(t<o) red[t]+=red[t+o]; __syncthreads(); }
  float var=red[0]/D;
  float rs=1.0f/sqrtf(var+1e-5f);
  for(int i=0;i<4;i++) xn[row*D+t*4+i]=f2bf((v[i]-mean)*rs);
}
__global__ void gemm_k(const u16* A,const u16* B,int N,int K,int mode,const u16* bias,u16* Cb,float* Cf,const u16* x){
  __shared__ float As[16][17], Bs[16][17];
  int tx=threadIdx.x, ty=threadIdx.y; int r=blockIdx.y*16+ty, c=blockIdx.x*16+tx;
  float acc=0;
  for(int k0=0;k0<K;k0+=16){
    As[ty][tx]=bf2f(A[(size_t)r*K+k0+tx]); Bs[ty][tx]=bf2f(B[(size_t)(k0+ty)*N+c]); __syncthreads();
    for(int k=0;k<16;k++) acc+=As[ty][k]*Bs[k][tx];
    __syncthreads();
  }
  if(mode==0) Cb[(size_t)r*N+c]=f2bf(acc);
  else if(mode==1){ float h=acc+bf2f(bias[c]); float u=0.7978845608028654f*(h+0.044715f*h*h*h); float g=0.5f*h*(1.f+tanhf(u)); Cb[(size_t)r*N+c]=f2bf(g);}
  else if(mode==2) Cf[(size_t)r*N+c]=acc;
  else Cb[(size_t)r*N+c]=f2bf(acc+bf2f(bias[c])+Cf[(size_t)r*N+c]+bf2f(x[(size_t)r*N+c]));
}
__global__ void attn_k(const u16* qkv,u16* o){
  int q=blockIdx.x, h=blockIdx.y, t=threadIdx.x;
  __shared__ float p[256]; __shared__ float red[256];
  int key=q-t; float s=-1e30f;
  if(key>=0){ s=0; for(int d=0;d<DH;d++) s+=bf2f(qkv[(size_t)q*3*D+h*DH+d])*bf2f(qkv[(size_t)key*3*D+D+h*DH+d]); s*=0.17677669529663687f; }
  red[t]=s; __syncthreads();
  for(int o2=128;o2>0;o2>>=1){ if(t<o2) red[t]=fmaxf(red[t],red[t+o2]); __syncthreads(); }
  float m=red[0]; __syncthreads();
  float e= key>=0? __expf(s-m):0.f; p[t]=e; red[t]=e; __syncthreads();
  for(int o2=128;o2>0;o2>>=1){ if(t<o2) red[t]+=red[t+o2]; __syncthreads(); }
  float l=red[0];
  if(t<DH){ float a=0; for(int j=0;j<256;j++){ int kk=q-j; if(kk>=0) a+=p[j]*bf2f(qkv[(size_t)kk*3*D+2*D+h*DH+t]); } o[(size_t)q*D+h*DH+t]=f2bf(a/l); }
}
extern "C" void kernel_launch(void* const* d_in, const int* in_sizes, int n_in, void* d_out, int out_size, void* d_ws, size_t ws_size, hipStream_t stream){
  const u16* x=(const u16*)d_in[0]; const u16* wqkv=(const u16*)d_in[1]; const u16* wo=(const u16*)d_in[2];
  const u16* w1=(const u16*)d_in[4]; const u16* b1=(const u16*)d_in[5]; const u16* w2=(const u16*)d_in[6]; const u16* b2=(const u16*)d_in[7];
  char* ws=(char*)d_ws; u16* xn=(u16*)ws; u16* qkv=(u16*)(ws+(8<<20)); u16* h=(u16*)(ws+(32<<20)); u16* o=(u16*)(ws+(64<<20)); float* cf=(float*)(ws+(80<<20));
  ln_k<<<S,256,0,stream>>>(x,xn);
  gemm_k<<<dim3(3*D/16,S/16),dim3(16,16),0,stream>>>(xn,wqkv,3*D,D,0,nullptr,qkv,nullptr,nullptr);
  gemm_k<<<dim3(F/16,S/16),dim3(16,16),0,stream>>>(xn,w1,F,D,1,b1,h,nullptr,nullptr);
  attn_k<<<dim3(S,H),256,0,stream>>>(qkv,o);
  gemm_k<<<dim3(D/16,S/16),dim3(16,16),0,stream>>>(o,wo,D,D,2,nullptr,nullptr,cf,nullptr);
  gemm_k<<<dim3(D/16,S/16),dim3(16,16),0,stream>>>(h,w2,D,F,3,b2,(u16*)d_out,cf,x);
}
```

```cpp
#include <hip/hip_runtime.h>
#include <hip/hip_bf16.h>

__global__ __launch_bounds__(256, 1) __attribute__((aligned(4096))) void fused_block_kernel(
    const void* x, const void* wqkv, const void* wo, const void* w1, const void* b1, const void* w2, const void* b2, void* out, void* ws) {
  unsigned tid = threadIdx.x;
  asm volatile(
"s_nop 0\n"
"s_lshr_b32 s90, s54, 3\n"
"s_lshl_b32 s90, s90, 2\n"
"s_mul_i32 s92, s90, 0x7f40\n"
"s_add_u32 s92, s92, 0x800\n"
"s_load_dword s33, s[36:37], s92\n"
"s_mul_i32 s92, s90, 0xbf40\n"
"s_add_u32 s92, s92, 0x800\n"
"s_load_dword s33, s[38:39], s92\n"
"s_mul_i32 s92, s90, 0xdf40\n"
"s_add_u32 s92, s92, 0x800\n"
"s_load_dword s33, s[42:43], s92\n"
"s_mul_i32 s92, s90, 0x7f40\n"
"s_add_u32 s92, s92, 0x800\n"
"s_load_dword s33, s[52:53], s92\n"
"s_mul_i32 s92, s90, 0\n"
"s_load_dword s33, s[44:45], s92\n"
"s_mul_i32 s92, s90, 0\n"
"s_add_u32 s92, s92, 0x2031000\n"
"s_load_dword s33, s[52:53], s92\n"
"s_mul_i32 s92, s90, 16\n"
"s_load_dword s33, s[48:49], s92\n"
"v_and_b32 v236, 63, v255\n"
"v_lshrrev_b32 v254, 6, v255\n"
"s_lshr_b32 s76, s54, 5\n"
"v_readfirstlane_b32 s78, v254\n"
"s_and_b32 s90, s54, 7\n"
"s_lshl_b32 s90, s90, 2\n"
"s_bfe_u32 s91, s54, 0x20003\n"
"s_add_u32 s77, s90, s91\n"
"s_mov_b32 s68, s36\n"
"s_and_b32 s69, s37, 0xffff\n"
"s_mov_b32 s70, 0x400000\n"
"s_mov_b32 s71, 0x20000\n"
"v_and_b32 v240, 7, v236\n"
"v_lshrrev_b32 v241, 3, v236\n"
"v_xor_b32 v243, v240, v241\n"
"v_lshlrev_b32 v243, 4, v243\n"
"v_lshl_add_u32 v242, v241, 11, v243\n"
"s_lshl_b32 s90, s76, 5\n"
"s_add_u32 s99, s90, s77\n"
"s_lshl_b32 s90, s99, 14\n"
"s_lshl_b32 s91, s78, 7\n"
"s_add_u32 s90, s90, s91\n"
"s_lshl_b32 s91, s78, 12\n"
"s_add_u32 s91, s91, 0x23400\n"
"s_add_u32 m0, s91, 0\n"
"s_nop 0\n"
"buffer_load_dwordx4 v242, s[68:71], s90 offen lds\n"
"s_add_u32 s90, s90, 0x200\n"
"s_add_u32 m0, s91, 0x400\n"
"s_nop 0\n"
"buffer_load_dwordx4 v242, s[68:71], s90 offen lds\n"
"s_add_u32 s90, s90, 0x200\n"
"s_add_u32 m0, s91, 0x800\n"
"s_nop 0\n"
"buffer_load_dwordx4 v242, s[68:71], s90 offen lds\n"
"s_add_u32 s90, s90, 0x200\n"
"s_add_u32 m0, s91, 0xc00\n"
"s_nop 0\n"
"buffer_load_dwordx4 v242, s[68:71], s90 offen lds\n"
"v_lshl_add_u32 v254, v240, 7, v243\n"
"v_add_u32 v254, s91, v254\n"
"s_mov_b32 s64, s52\n"
"s_and_b32 s65, s53, 0xffff\n"
"s_mov_b32 s66, 0x3800000\n"
"s_mov_b32 s67, 0x20000\n"
"v_mov_b32 v91, 0x2030000\n"
"s_lshl_b32 s91, s99, 7\n"
"s_add_u32 s91, s91, 0x2080000\n"
"v_writelane_b32 v91, s91, 63\n"
"s_lshr_b32 s90, s54, 3\n"
"s_lshl_b32 s90, s90, 2\n"
"s_add_u32 s90, s90, s78\n"
"s_lshl_b32 s90, s90, 7\n"
"v_and_b32 v92, 3, v236\n"
"v_lshlrev_b32 v92, 14, v92\n"
"v_add_u32 v92, s90, v92\n"
"v_min_u32 v92, 0xe000, v92\n"
"s_getpc_b64 s[24:25]\n"
"s_mul_i32 s91, s77, 0xe0\n"
"v_lshrrev_b32 v90, 4, v236\n"
"v_lshlrev_b32 v90, 3, v90\n"
"v_add_u32 v90, s91, v90\n"
"buffer_load_dwordx2 v[228:229], v91, s[64:67], 0 offen sc1\n"
"s_mov_b32 s56, s38\n"
"s_and_b32 s57, s39, 0xffff\n"
"s_mov_b32 s58, 0x600000\n"
"s_mov_b32 s59, 0x20000\n"
"global_load_dword a166, v92, s[24:25]\n"
"s_mov_b32 s60, s42\n"
"s_and_b32 s61, s43, 0xffff\n"
"s_mov_b32 s62, 0x700000\n"
"s_mov_b32 s63, 0x20000\n"
"global_load_dwordx2 v[96:97], v90, s[44:45] offset:0\n"
"s_lshl_b32 s90, s77, 6\n"
"s_mul_i32 s91, s77, 0xe0\n"
"v_add_u32 v243, 0, v236\n"
"v_mul_u32_u24 v244, 0x5556, v243\n"
"global_load_dwordx2 v[112:113], v90, s[44:45] offset:32\n"
"v_lshrrev_b32 v244, 18, v244\n"
"v_mul_u32_u24 v245, 12, v244\n"
"v_sub_u32 v245, v243, v245\n"
"v_lshrrev_b32 v246, 1, v244\n"
"global_load_dwordx2 v[128:129], v90, s[44:45] offset:64\n"
"v_and_b32 v246, 2, v246\n"
"v_xor_b32 v245, v245, v246\n"
"v_and_b32 v246, 12, v244\n"
"v_add_u32 v244, v244, v246\n"
"global_load_dwordx2 v[144:145], v90, s[44:45] offset:96\n"
"v_mul_u32_u24 v244, 0x1800, v244\n"
"v_lshrrev_b32 v246, 2, v245\n"
"v_lshl_add_u32 v244, v246, 11, v244\n"
"v_and_b32 v245, 3, v245\n"
"global_load_dwordx2 v[160:161], v90, s[44:45] offset:128\n"
"v_lshl_add_u32 v244, v245, 4, v244\n"
"v_add_u32 v217, s90, v244\n"
"v_add_u32 v243, 64, v236\n"
"v_mul_u32_u24 v244, 0x5556, v243\n"
"global_load_dwordx2 v[176:177], v90, s[44:45] offset:160\n"
"v_lshrrev_b32 v244, 18, v244\n"
"v_mul_u32_u24 v245, 12, v244\n"
"v_sub_u32 v245, v243, v245\n"
"v_lshrrev_b32 v246, 1, v244\n"
"global_load_dwordx2 v[192:193], v90, s[44:45] offset:192\n"
"v_and_b32 v246, 2, v246\n"
"v_xor_b32 v245, v245, v246\n"
"v_and_b32 v246, 12, v244\n"
"v_add_u32 v244, v244, v246\n"
"v_mul_u32_u24 v244, 0x1800, v244\n"
"v_lshrrev_b32 v246, 2, v245\n"
"v_lshl_add_u32 v244, v246, 11, v244\n"
"v_and_b32 v245, 3, v245\n"
"v_lshl_add_u32 v244, v245, 4, v244\n"
"v_add_u32 v218, s90, v244\n"
"v_add_u32 v243, 0x80, v236\n"
"v_mul_u32_u24 v244, 0x5556, v243\n"
"v_lshrrev_b32 v244, 18, v244\n"
"v_mul_u32_u24 v245, 12, v244\n"
"v_sub_u32 v245, v243, v245\n"
"v_lshrrev_b32 v246, 1, v244\n"
"v_and_b32 v246, 2, v246\n"
"v_xor_b32 v245, v245, v246\n"
"v_and_b32 v246, 12, v244\n"
"v_add_u32 v244, v244, v246\n"
"v_mul_u32_u24 v244, 0x1800, v244\n"
"v_lshrrev_b32 v246, 2, v245\n"
"v_lshl_add_u32 v244, v246, 11, v244\n"
"v_and_b32 v245, 3, v245\n"
"v_lshl_add_u32 v244, v245, 4, v244\n"
"v_add_u32 v219, s90, v244\n"
"s_lshr_b32 s92, s78, 1\n"
"s_lshl_b32 s92, s92, 5\n"
"s_and_b32 s93, s78, 1\n"
"s_lshl_b32 s93, s93, 2\n"
"s_add_u32 s92, s92, s93\n"
"s_mul_i32 s79, s92, 0x1800\n"
"s_mul_i32 s80, s92, 0x1c00\n"
"s_mul_i32 s82, s78, 0xc00\n"
"s_lshl_b32 s83, s78, 12\n"
"s_add_u32 s83, s83, 0x3000\n"
"v_subrev_u32 v243, 0, v236\n"
"v_and_b32 v245, 63, v243\n"
"v_mul_u32_u24 v244, 0x4925, v245\n"
"v_lshrrev_b32 v244, 18, v244\n"
"v_mul_u32_u24 v246, 14, v244\n"
"v_sub_u32 v245, v245, v246\n"
"v_mul_u32_u24 v244, 0x1c00, v244\n"
"v_lshl_add_u32 v244, v245, 4, v244\n"
"v_add_u32 v244, s91, v244\n"
"v_cmp_gt_u32 vcc, 56, v243\n"
"v_mov_b32 v246, 0x40000000\n"
"v_add_u32 v247, 0, v244\n"
"v_cndmask_b32 v220, v246, v247, vcc\n"
"v_add_u32 v247, 0x1c000, v244\n"
"v_cndmask_b32 v222, v246, v247, vcc\n"
"v_subrev_u32 v243, 8, v236\n"
"v_and_b32 v245, 63, v243\n"
"v_mul_u32_u24 v244, 0x4925, v245\n"
"v_lshrrev_b32 v244, 18, v244\n"
"v_mul_u32_u24 v246, 14, v244\n"
"v_sub_u32 v245, v245, v246\n"
"v_mul_u32_u24 v244, 0x1c00, v244\n"
"v_lshl_add_u32 v244, v245, 4, v244\n"
"v_add_u32 v244, s91, v244\n"
"v_cmp_gt_u32 vcc, 56, v243\n"
"v_mov_b32 v246, 0x40000000\n"
"v_add_u32 v247, 0xe000, v244\n"
"v_cndmask_b32 v221, v246, v247, vcc\n"
"v_add_u32 v247, 0x2a000, v244\n"
"v_cndmask_b32 v223, v246, v247, vcc\n"
"v_lshrrev_b32 v243, 4, v236\n"
"v_lshlrev_b32 v224, 4, v236\n"
"v_lshlrev_b32 v226, 3, v236\n"
"v_cmp_eq_u32 vcc, 0, v236\n"
"v_mov_b32 v246, 0x40000000\n"
"v_mov_b32 v247, 0\n"
"v_cndmask_b32 v225, v246, v247, vcc\n"
"v_lshrrev_b32 v245, 2, v236\n"
"v_and_b32 v246, 3, v236\n"
"v_lshlrev_b32 v246, 3, v246\n"
"v_and_b32 v247, 1, v243\n"
"v_mul_u32_u24 v248, 0xc0, v245\n"
"v_add_u32 v248, v248, v246\n"
"v_lshl_add_u32 v208, v247, 5, v248\n"
"v_xor_b32 v249, 1, v247\n"
"v_lshl_add_u32 v209, v249, 5, v248\n"
"v_and_b32 v245, 3, v245\n"
"v_mul_u32_u24 v245, 0xe0, v245\n"
"v_add_u32 v245, v245, v246\n"
"v_lshl_add_u32 v245, v243, 10, v245\n"
"v_lshl_add_u32 v210, v247, 7, v245\n"
"v_add_u32 v211, 0xe000, v208\n"
"v_add_u32 v212, 0xe000, v209\n"
"v_add_u32 v213, 0xe000, v210\n"
"v_add_u32 v214, 0x1c000, v208\n"
"v_add_u32 v215, 0x1c000, v209\n"
"v_add_u32 v216, 0x1c000, v210\n"
"v_and_b32 v245, 15, v236\n"
"v_lshrrev_b32 v246, 1, v243\n"
"v_lshl_add_u32 v245, v246, 4, v245\n"
"v_lshlrev_b32 v245, 4, v245\n"
"v_lshl_add_u32 v227, v247, 3, v245\n"
"s_lshl_b32 s90, s78, 12\n"
"v_add_u32 v237, s90, v224\n"
"v_mov_b32 v232, 0xc0135761\n"
"v_mov_b32 v233, 0xc0135761\n"
"s_mov_b32 s94, 0x30d40\n"
"v_lshrrev_b32 v245, 2, v241\n"
"v_and_b32 v246, 3, v241\n"
"v_lshlrev_b32 v250, 4, v240\n"
"v_lshl_add_u32 v250, v245, 12, v250\n"
"v_lshl_add_u32 v250, v246, 8, v250\n"
"s_lshr_b32 s90, s99, 3\n"
"s_lshl_b32 s90, s90, 17\n"
"s_bfe_u32 s91, s77, 0x20001\n"
"s_lshl_b32 s91, s91, 10\n"
"s_add_u32 s90, s90, s91\n"
"s_and_b32 s91, s77, 1\n"
"s_lshl_b32 s91, s91, 7\n"
"s_add_u32 s90, s90, s91\n"
"s_lshl_b32 s91, s78, 13\n"
"s_add_u32 s35, s90, s91\n"
"v_lshlrev_b32 v251, 3, v240\n"
"v_add_u32 v251, 0x23000, v251\n"
"s_lshl_b32 s90, s78, 6\n"
"v_add_u32 v252, s90, v251\n"
"s_lshl_b32 s90, s99, 2\n"
"s_add_u32 s99, s90, s78\n"
"s_lshl_b32 s90, s76, 2\n"
"s_add_u32 s90, s90, s78\n"
"s_lshl_b32 s81, s90, 17\n"
"s_mul_i32 s95, s90, 0x90000\n"
"s_add_u32 s95, s95, 0x400000\n"
"s_mul_i32 s90, s77, 7\n"
"s_add_u32 s90, s90, 64\n"
"s_lshr_b32 s91, s90, 1\n"
"s_lshl_b32 s91, s91, 12\n"
"s_add_u32 s98, s95, s91\n"
"s_and_b32 s90, s90, 1\n"
"s_lshl_b32 s91, s90, 9\n"
"s_add_u32 s98, s98, s91\n"
"s_mul_i32 s96, s90, 0xc00\n"
"s_add_u32 s96, s96, 0x200\n"
"s_sub_u32 s97, 0x1000, s96\n"
"s_lshr_b32 s90, s99, 2\n"
"s_lshl_b32 s90, s90, 7\n"
"s_add_u32 s90, s90, 0x2080000\n"
"s_cmp_eq_u32 s78, 0\n"
"s_cselect_b32 s90, s90, 0x40000000\n"
"s_lshl_b32 s91, s76, 2\n"
"s_add_u32 s91, s91, s78\n"
"s_lshl_b32 s91, s91, 10\n"
"s_add_u32 s91, s91, 0x2080000\n"
"v_and_b32 v244, 7, v236\n"
"v_lshlrev_b32 v244, 7, v244\n"
"s_lshr_b32 s28, s54, 3\n"
"s_lshl_b32 s28, s28, 2\n"
"s_add_u32 s28, s28, s78\n"
"s_mul_i32 s28, s28, 2\n"
"s_add_u32 s28, s28, 0\n"
"s_and_b32 s31, s54, 7\n"
"s_mul_i32 s29, s28, 0x1800\n"
"s_mul_i32 s30, s28, 0x1c00\n"
"s_mul_i32 s28, s31, 0x380\n"
"s_add_u32 s30, s30, s28\n"
"s_lshl_b32 s31, s31, 8\n"
"s_add_u32 s29, s29, s31\n"
"v_min_u32 v247, 11, v236\n"
"v_lshrrev_b32 v248, 1, v247\n"
"v_and_b32 v246, 1, v247\n"
"v_lshlrev_b32 v246, 7, v246\n"
"v_lshl_add_u32 v249, v248, 11, v246\n"
"v_min_u32 v247, 13, v236\n"
"v_mul_u32_u24 v248, 0x925, v247\n"
"v_lshrrev_b32 v248, 14, v248\n"
"v_mul_u32_u24 v248, 0x1880, v248\n"
"v_lshl_add_u32 v253, v247, 7, v248\n"
"buffer_load_dword v238, v249, s[56:59], s29 offen\n"
"buffer_load_dword v239, v253, s[60:63], s30 offen\n"
"s_waitcnt vmcnt(11)\n"
"ds_read_b128 v[0:3], v254\n"
"ds_read_b128 v[4:7], v254 offset:1024\n"
"ds_read_b128 v[8:11], v254 offset:2048\n"
"ds_read_b128 v[12:15], v254 offset:3072\n"
"s_waitcnt lgkmcnt(3)\n"
"v_lshlrev_b32 v16, 16, v0\n"
"v_and_b32 v17, 0xffff0000, v0\n"
"v_lshlrev_b32 v18, 16, v1\n"
"v_and_b32 v19, 0xffff0000, v1\n"
"v_lshlrev_b32 v20, 16, v2\n"
"v_and_b32 v21, 0xffff0000, v2\n"
"v_lshlrev_b32 v22, 16, v3\n"
"v_and_b32 v23, 0xffff0000, v3\n"
"s_waitcnt lgkmcnt(2)\n"
"v_lshlrev_b32 v24, 16, v4\n"
"v_and_b32 v25, 0xffff0000, v4\n"
"v_lshlrev_b32 v26, 16, v5\n"
"v_and_b32 v27, 0xffff0000, v5\n"
"v_lshlrev_b32 v28, 16, v6\n"
"v_and_b32 v29, 0xffff0000, v6\n"
"v_lshlrev_b32 v30, 16, v7\n"
"v_and_b32 v31, 0xffff0000, v7\n"
"s_waitcnt lgkmcnt(1)\n"
"v_lshlrev_b32 v32, 16, v8\n"
"v_and_b32 v33, 0xffff0000, v8\n"
"v_lshlrev_b32 v34, 16, v9\n"
"v_and_b32 v35, 0xffff0000, v9\n"
"v_lshlrev_b32 v36, 16, v10\n"
"v_and_b32 v37, 0xffff0000, v10\n"
"v_lshlrev_b32 v38, 16, v11\n"
"v_and_b32 v39, 0xffff0000, v11\n"
"s_waitcnt lgkmcnt(0)\n"
"v_lshlrev_b32 v40, 16, v12\n"
"v_and_b32 v41, 0xffff0000, v12\n"
"v_lshlrev_b32 v42, 16, v13\n"
"v_and_b32 v43, 0xffff0000, v13\n"
"v_lshlrev_b32 v44, 16, v14\n"
"v_and_b32 v45, 0xffff0000, v14\n"
"v_lshlrev_b32 v46, 16, v15\n"
"v_and_b32 v47, 0xffff0000, v15\n"
"v_pk_add_f32 v[48:49], v[16:17], v[24:25]\n"
"v_pk_add_f32 v[50:51], v[18:19], v[26:27]\n"
"v_pk_add_f32 v[52:53], v[20:21], v[28:29]\n"
"v_pk_add_f32 v[54:55], v[22:23], v[30:31]\n"
"v_pk_add_f32 v[48:49], v[48:49], v[32:33]\n"
"v_pk_add_f32 v[50:51], v[50:51], v[34:35]\n"
"v_pk_add_f32 v[52:53], v[52:53], v[36:37]\n"
"v_pk_add_f32 v[54:55], v[54:55], v[38:39]\n"
"v_pk_add_f32 v[48:49], v[48:49], v[40:41]\n"
"v_pk_add_f32 v[50:51], v[50:51], v[42:43]\n"
"v_pk_add_f32 v[52:53], v[52:53], v[44:45]\n"
"v_pk_add_f32 v[54:55], v[54:55], v[46:47]\n"
"v_pk_add_f32 v[48:49], v[48:49], v[50:51]\n"
"v_pk_add_f32 v[52:53], v[52:53], v[54:55]\n"
"v_pk_add_f32 v[48:49], v[48:49], v[52:53]\n"
"v_add_f32 v56, v48, v49\n"
"s_nop 1\n"
"v_add_f32_dpp v56, v56, v56 row_ror:8 row_mask:0xf bank_mask:0xf\n"
"v_mov_b32 v60, v56\n"
"s_nop 1\n"
"v_permlane16_swap_b32 v60, v56\n"
"v_add_f32 v56, v56, v60\n"
"v_mov_b32 v60, v56\n"
"s_nop 1\n"
"v_permlane32_swap_b32 v60, v56\n"
"v_add_f32 v56, v56, v60\n"
"v_mul_f32 v56, 0x3b800000, v56\n"
"v_pk_add_f32 v[64:65], v[16:17], v[56:57] op_sel_hi:[1,0] neg_lo:[0,1] neg_hi:[0,1]\n"
"v_pk_add_f32 v[66:67], v[18:19], v[56:57] op_sel_hi:[1,0] neg_lo:[0,1] neg_hi:[0,1]\n"
"v_pk_add_f32 v[68:69], v[20:21], v[56:57] op_sel_hi:[1,0] neg_lo:[0,1] neg_hi:[0,1]\n"
"v_pk_add_f32 v[70:71], v[22:23], v[56:57] op_sel_hi:[1,0] neg_lo:[0,1] neg_hi:[0,1]\n"
"v_pk_add_f32 v[72:73], v[24:25], v[56:57] op_sel_hi:[1,0] neg_lo:[0,1] neg_hi:[0,1]\n"
"v_pk_add_f32 v[74:75], v[26:27], v[56:57] op_sel_hi:[1,0] neg_lo:[0,1] neg_hi:[0,1]\n"
"v_pk_add_f32 v[76:77], v[28:29], v[56:57] op_sel_hi:[1,0] neg_lo:[0,1] neg_hi:[0,1]\n"
"v_pk_add_f32 v[78:79], v[30:31], v[56:57] op_sel_hi:[1,0] neg_lo:[0,1] neg_hi:[0,1]\n"
"v_pk_add_f32 v[80:81], v[32:33], v[56:57] op_sel_hi:[1,0] neg_lo:[0,1] neg_hi:[0,1]\n"
"v_pk_add_f32 v[82:83], v[34:35], v[56:57] op_sel_hi:[1,0] neg_lo:[0,1] neg_hi:[0,1]\n"
"v_pk_add_f32 v[84:85], v[36:37], v[56:57] op_sel_hi:[1,0] neg_lo:[0,1] neg_hi:[0,1]\n"
"v_pk_add_f32 v[86:87], v[38:39], v[56:57] op_sel_hi:[1,0] neg_lo:[0,1] neg_hi:[0,1]\n"
"v_pk_add_f32 v[88:89], v[40:41], v[56:57] op_sel_hi:[1,0] neg_lo:[0,1] neg_hi:[0,1]\n"
"v_pk_add_f32 v[90:91], v[42:43], v[56:57] op_sel_hi:[1,0] neg_lo:[0,1] neg_hi:[0,1]\n"
"v_pk_add_f32 v[92:93], v[44:45], v[56:57] op_sel_hi:[1,0] neg_lo:[0,1] neg_hi:[0,1]\n"
"v_pk_add_f32 v[94:95], v[46:47], v[56:57] op_sel_hi:[1,0] neg_lo:[0,1] neg_hi:[0,1]\n"
"v_pk_mul_f32 v[48:49], v[64:65], v[64:65]\n"
"v_pk_mul_f32 v[50:51], v[66:67], v[66:67]\n"
"v_pk_mul_f32 v[52:53], v[68:69], v[68:69]\n"
"v_pk_mul_f32 v[54:55], v[70:71], v[70:71]\n"
"v_pk_fma_f32 v[48:49], v[72:73], v[72:73], v[48:49]\n"
"v_pk_fma_f32 v[50:51], v[74:75], v[74:75], v[50:51]\n"
"v_pk_fma_f32 v[52:53], v[76:77], v[76:77], v[52:53]\n"
"v_pk_fma_f32 v[54:55], v[78:79], v[78:79], v[54:55]\n"
"v_pk_fma_f32 v[48:49], v[80:81], v[80:81], v[48:49]\n"
"v_pk_fma_f32 v[50:51], v[82:83], v[82:83], v[50:51]\n"
"v_pk_fma_f32 v[52:53], v[84:85], v[84:85], v[52:53]\n"
"v_pk_fma_f32 v[54:55], v[86:87], v[86:87], v[54:55]\n"
"v_pk_fma_f32 v[48:49], v[88:89], v[88:89], v[48:49]\n"
"v_pk_fma_f32 v[50:51], v[90:91], v[90:91], v[50:51]\n"
"v_pk_fma_f32 v[52:53], v[92:93], v[92:93], v[52:53]\n"
"v_pk_fma_f32 v[54:55], v[94:95], v[94:95], v[54:55]\n"
"v_pk_add_f32 v[48:49], v[48:49], v[50:51]\n"
"v_pk_add_f32 v[52:53], v[52:53], v[54:55]\n"
"v_pk_add_f32 v[48:49], v[48:49], v[52:53]\n"
"v_add_f32 v57, v48, v49\n"
"s_nop 1\n"
"v_add_f32_dpp v57, v57, v57 row_ror:8 row_mask:0xf bank_mask:0xf\n"
"v_mov_b32 v60, v57\n"
"s_nop 1\n"
"v_permlane16_swap_b32 v60, v57\n"
"v_add_f32 v57, v57, v60\n"
"v_mov_b32 v60, v57\n"
"s_nop 1\n"
"v_permlane32_swap_b32 v60, v57\n"
"v_add_f32 v57, v57, v60\n"
"ds_write_b64 v252, v[56:57]\n"
"s_waitcnt lgkmcnt(0)\n"
"s_barrier\n"
"ds_read_b64 v[48:49], v251\n"
"ds_read_b64 v[50:51], v251 offset:64\n"
"ds_read_b64 v[52:53], v251 offset:128\n"
"ds_read_b64 v[54:55], v251 offset:192\n"
"s_waitcnt lgkmcnt(2)\n"
"v_add_f32 v56, v48, v50\n"
"s_waitcnt lgkmcnt(0)\n"
"v_add_f32 v57, v52, v54\n"
"v_add_f32 v58, v49, v51\n"
"v_add_f32 v59, v53, v55\n"
"v_add_f32 v56, v56, v57\n"
"v_add_f32 v58, v58, v59\n"
"v_mul_f32 v56, 0x3e800000, v56\n"
"v_sub_f32 v48, v48, v56\n"
"v_sub_f32 v50, v50, v56\n"
"v_sub_f32 v52, v52, v56\n"
"v_sub_f32 v54, v54, v56\n"
"v_mul_f32 v48, v48, v48\n"
"v_fmac_f32 v48, v50, v50\n"
"v_fmac_f32 v48, v52, v52\n"
"v_fmac_f32 v48, v54, v54\n"
"v_mul_f32 v58, 0x3a800000, v58\n"
"v_fmac_f32 v58, 0x3e800000, v48\n"
"v_cvt_pk_bf16_f32 v59, v58, v56\n"
"v_and_b32 v56, 0xffff0000, v59\n"
"v_lshlrev_b32 v58, 16, v59\n"
"v_add_f32 v58, 0x3727c5ac, v58\n"
"v_pk_add_f32 v[16:17], v[16:17], v[56:57] op_sel_hi:[1,0] neg_lo:[0,1] neg_hi:[0,1]\n"
"v_pk_add_f32 v[18:19], v[18:19], v[56:57] op_sel_hi:[1,0] neg_lo:[0,1] neg_hi:[0,1]\n"
"v_cvt_pk_bf16_f32 v58, v58, v58\n"
"v_pk_add_f32 v[20:21], v[20:21], v[56:57] op_sel_hi:[1,0] neg_lo:[0,1] neg_hi:[0,1]\n"
"v_pk_add_f32 v[22:23], v[22:23], v[56:57] op_sel_hi:[1,0] neg_lo:[0,1] neg_hi:[0,1]\n"
"v_lshlrev_b32 v58, 16, v58\n"
"v_cvt_pk_bf16_f32 v0, v16, v17\n"
"v_cvt_pk_bf16_f32 v1, v18, v19\n"
"v_sqrt_f32 v58, v58\n"
"v_cvt_pk_bf16_f32 v2, v20, v21\n"
"v_cvt_pk_bf16_f32 v3, v22, v23\n"
"v_cvt_pk_bf16_f32 v58, v58, v58\n"
"v_lshlrev_b32 v16, 16, v0\n"
"v_and_b32 v17, 0xffff0000, v0\n"
"v_lshlrev_b32 v58, 16, v58\n"
"v_lshlrev_b32 v18, 16, v1\n"
"v_and_b32 v19, 0xffff0000, v1\n"
"v_rcp_f32 v58, v58\n"
"v_lshlrev_b32 v20, 16, v2\n"
"v_and_b32 v21, 0xffff0000, v2\n"
"v_lshlrev_b32 v22, 16, v3\n"
"v_and_b32 v23, 0xffff0000, v3\n"
"v_pk_mul_f32 v[16:17], v[16:17], v[58:59] op_sel_hi:[1,0]\n"
"v_pk_mul_f32 v[18:19], v[18:19], v[58:59] op_sel_hi:[1,0]\n"
"v_pk_mul_f32 v[20:21], v[20:21], v[58:59] op_sel_hi:[1,0]\n"
"v_pk_mul_f32 v[22:23], v[22:23], v[58:59] op_sel_hi:[1,0]\n"
"v_cvt_pk_bf16_f32 v0, v16, v17\n"
"v_cvt_pk_bf16_f32 v1, v18, v19\n"
"v_cvt_pk_bf16_f32 v2, v20, v21\n"
"v_cvt_pk_bf16_f32 v3, v22, v23\n"
"buffer_store_dwordx4 v[0:3], v250, s[64:67], s35 offen sc1\n"
"s_add_u32 s35, s35, 0x8000\n"
"v_pk_add_f32 v[24:25], v[24:25], v[56:57] op_sel_hi:[1,0] neg_lo:[0,1] neg_hi:[0,1]\n"
"v_pk_add_f32 v[26:27], v[26:27], v[56:57] op_sel_hi:[1,0] neg_lo:[0,1] neg_hi:[0,1]\n"
"v_pk_add_f32 v[28:29], v[28:29], v[56:57] op_sel_hi:[1,0] neg_lo:[0,1] neg_hi:[0,1]\n"
"v_pk_add_f32 v[30:31], v[30:31], v[56:57] op_sel_hi:[1,0] neg_lo:[0,1] neg_hi:[0,1]\n"
"v_cvt_pk_bf16_f32 v4, v24, v25\n"
"v_cvt_pk_bf16_f32 v5, v26, v27\n"
"v_cvt_pk_bf16_f32 v6, v28, v29\n"
"v_cvt_pk_bf16_f32 v7, v30, v31\n"
"v_lshlrev_b32 v24, 16, v4\n"
"v_and_b32 v25, 0xffff0000, v4\n"
"v_lshlrev_b32 v26, 16, v5\n"
"v_and_b32 v27, 0xffff0000, v5\n"
"v_lshlrev_b32 v28, 16, v6\n"
"v_and_b32 v29, 0xffff0000, v6\n"
"v_lshlrev_b32 v30, 16, v7\n"
"v_and_b32 v31, 0xffff0000, v7\n"
"v_pk_mul_f32 v[24:25], v[24:25], v[58:59] op_sel_hi:[1,0]\n"
"v_pk_mul_f32 v[26:27], v[26:27], v[58:59] op_sel_hi:[1,0]\n"
"v_pk_mul_f32 v[28:29], v[28:29], v[58:59] op_sel_hi:[1,0]\n"
"v_pk_mul_f32 v[30:31], v[30:31], v[58:59] op_sel_hi:[1,0]\n"
"v_cvt_pk_bf16_f32 v4, v24, v25\n"
"v_cvt_pk_bf16_f32 v5, v26, v27\n"
"v_cvt_pk_bf16_f32 v6, v28, v29\n"
"v_cvt_pk_bf16_f32 v7, v30, v31\n"
"buffer_store_dwordx4 v[4:7], v250, s[64:67], s35 offen sc1\n"
"s_add_u32 s35, s35, 0x8000\n"
"v_pk_add_f32 v[32:33], v[32:33], v[56:57] op_sel_hi:[1,0] neg_lo:[0,1] neg_hi:[0,1]\n"
"v_pk_add_f32 v[34:35], v[34:35], v[56:57] op_sel_hi:[1,0] neg_lo:[0,1] neg_hi:[0,1]\n"
"v_pk_add_f32 v[36:37], v[36:37], v[56:57] op_sel_hi:[1,0] neg_lo:[0,1] neg_hi:[0,1]\n"
"v_pk_add_f32 v[38:39], v[38:39], v[56:57] op_sel_hi:[1,0] neg_lo:[0,1] neg_hi:[0,1]\n"
"v_cvt_pk_bf16_f32 v8, v32, v33\n"
"v_cvt_pk_bf16_f32 v9, v34, v35\n"
"v_cvt_pk_bf16_f32 v10, v36, v37\n"
"v_cvt_pk_bf16_f32 v11, v38, v39\n"
"v_lshlrev_b32 v32, 16, v8\n"
"v_and_b32 v33, 0xffff0000, v8\n"
"v_lshlrev_b32 v34, 16, v9\n"
"v_and_b32 v35, 0xffff0000, v9\n"
"v_lshlrev_b32 v36, 16, v10\n"
"v_and_b32 v37, 0xffff0000, v10\n"
"v_lshlrev_b32 v38, 16, v11\n"
"v_and_b32 v39, 0xffff0000, v11\n"
"v_pk_mul_f32 v[32:33], v[32:33], v[58:59] op_sel_hi:[1,0]\n"
"v_pk_mul_f32 v[34:35], v[34:35], v[58:59] op_sel_hi:[1,0]\n"
"v_pk_mul_f32 v[36:37], v[36:37], v[58:59] op_sel_hi:[1,0]\n"
"v_pk_mul_f32 v[38:39], v[38:39], v[58:59] op_sel_hi:[1,0]\n"
"v_cvt_pk_bf16_f32 v8, v32, v33\n"
"v_cvt_pk_bf16_f32 v9, v34, v35\n"
"v_cvt_pk_bf16_f32 v10, v36, v37\n"
"v_cvt_pk_bf16_f32 v11, v38, v39\n"
"buffer_store_dwordx4 v[8:11], v250, s[64:67], s35 offen sc1\n"
"s_add_u32 s35, s35, 0x8000\n"
"v_pk_add_f32 v[40:41], v[40:41], v[56:57] op_sel_hi:[1,0] neg_lo:[0,1] neg_hi:[0,1]\n"
"v_pk_add_f32 v[42:43], v[42:43], v[56:57] op_sel_hi:[1,0] neg_lo:[0,1] neg_hi:[0,1]\n"
"v_pk_add_f32 v[44:45], v[44:45], v[56:57] op_sel_hi:[1,0] neg_lo:[0,1] neg_hi:[0,1]\n"
"v_pk_add_f32 v[46:47], v[46:47], v[56:57] op_sel_hi:[1,0] neg_lo:[0,1] neg_hi:[0,1]\n"
"v_cvt_pk_bf16_f32 v12, v40, v41\n"
"v_cvt_pk_bf16_f32 v13, v42, v43\n"
"v_cvt_pk_bf16_f32 v14, v44, v45\n"
"v_cvt_pk_bf16_f32 v15, v46, v47\n"
"v_lshlrev_b32 v40, 16, v12\n"
"v_and_b32 v41, 0xffff0000, v12\n"
"v_lshlrev_b32 v42, 16, v13\n"
"v_and_b32 v43, 0xffff0000, v13\n"
"v_lshlrev_b32 v44, 16, v14\n"
"v_and_b32 v45, 0xffff0000, v14\n"
"v_lshlrev_b32 v46, 16, v15\n"
"v_and_b32 v47, 0xffff0000, v15\n"
"v_pk_mul_f32 v[40:41], v[40:41], v[58:59] op_sel_hi:[1,0]\n"
"v_pk_mul_f32 v[42:43], v[42:43], v[58:59] op_sel_hi:[1,0]\n"
"v_pk_mul_f32 v[44:45], v[44:45], v[58:59] op_sel_hi:[1,0]\n"
"v_pk_mul_f32 v[46:47], v[46:47], v[58:59] op_sel_hi:[1,0]\n"
"v_cvt_pk_bf16_f32 v12, v40, v41\n"
"v_cvt_pk_bf16_f32 v13, v42, v43\n"
"v_cvt_pk_bf16_f32 v14, v44, v45\n"
"v_cvt_pk_bf16_f32 v15, v46, v47\n"
"buffer_store_dwordx4 v[12:15], v250, s[64:67], s35 offen sc1\n"
"s_waitcnt vmcnt(14)\n"
"v_readfirstlane_b32 s84, v228\n"
"v_readfirstlane_b32 s85, v229\n"
"s_nop 0\n"
"s_mul_i32 s84, s84, 0x9e3779b1\n"
"s_add_u32 s84, s84, 0x7f4a7c15\n"
"s_mul_i32 s85, s85, 0x85ebca6d\n"
"s_add_u32 s85, s85, 0x1b873593\n"
"v_mov_b32 v228, s84\n"
"v_mov_b32 v229, s85\n"
"s_waitcnt vmcnt(3)\n"
"s_barrier\n"
"buffer_store_dwordx2 v[228:229], v225, s[64:67], s90 offen sc1\n"
"s_add_u32 m0, s82, 0x1c000\n"
"s_lshr_b32 s28, s54, 3\n"
"s_lshl_b32 s28, s28, 2\n"
"s_add_u32 s28, s28, s78\n"
"s_mul_i32 s28, s28, 2\n"
"s_add_u32 s28, s28, 0x100\n"
"s_and_b32 s31, s54, 7\n"
"s_mul_i32 s29, s28, 0x1800\n"
"s_mul_i32 s30, s28, 0x1c00\n"
"buffer_load_dwordx4 v217, s[56:59], s79 offen lds\n"
"s_add_u32 m0, s82, 0x1c400\n"
"s_mul_i32 s28, s31, 0x380\n"
"s_add_u32 s30, s30, s28\n"
"s_lshl_b32 s31, s31, 8\n"
"s_add_u32 s29, s29, s31\n"
"v_min_u32 v247, 11, v236\n"
"v_lshrrev_b32 v248, 1, v247\n"
"v_and_b32 v246, 1, v247\n"
"v_lshlrev_b32 v246, 7, v246\n"
"buffer_load_dwordx4 v218, s[56:59], s79 offen lds\n"
"s_add_u32 m0, s82, 0x1c800\n"
"v_lshl_add_u32 v249, v248, 11, v246\n"
"v_min_u32 v247, 13, v236\n"
"v_mul_u32_u24 v248, 0x925, v247\n"
"v_lshrrev_b32 v248, 14, v248\n"
"v_mul_u32_u24 v248, 0x1880, v248\n"
"v_lshl_add_u32 v253, v247, 7, v248\n"
"v_lshlrev_b32 v108, 16, v96\n"
"v_and_b32 v109, 0xffff0000, v96\n"
"buffer_load_dwordx4 v219, s[56:59], s79 offen lds\n"
"s_add_u32 m0, s83, 0x1c000\n"
"v_lshlrev_b32 v110, 16, v97\n"
"v_and_b32 v111, 0xffff0000, v97\n"
"v_lshlrev_b32 v124, 16, v112\n"
"v_and_b32 v125, 0xffff0000, v112\n"
"v_lshlrev_b32 v126, 16, v113\n"
"v_and_b32 v127, 0xffff0000, v113\n"
"v_lshlrev_b32 v140, 16, v128\n"
"v_and_b32 v141, 0xffff0000, v128\n"
"buffer_load_dwordx4 v220, s[60:63], s80 offen lds\n"
"s_add_u32 m0, s83, 0x1c400\n"
"v_lshlrev_b32 v142, 16, v129\n"
"v_and_b32 v143, 0xffff0000, v129\n"
"v_lshlrev_b32 v156, 16, v144\n"
"v_and_b32 v157, 0xffff0000, v144\n"
"v_lshlrev_b32 v158, 16, v145\n"
"v_and_b32 v159, 0xffff0000, v145\n"
"v_lshlrev_b32 v172, 16, v160\n"
"v_and_b32 v173, 0xffff0000, v160\n"
"buffer_load_dwordx4 v221, s[60:63], s80 offen lds\n"
"s_add_u32 m0, s83, 0x1c800\n"
"v_lshlrev_b32 v174, 16, v161\n"
"v_and_b32 v175, 0xffff0000, v161\n"
"v_lshlrev_b32 v188, 16, v176\n"
"v_and_b32 v189, 0xffff0000, v176\n"
"v_lshlrev_b32 v190, 16, v177\n"
"v_and_b32 v191, 0xffff0000, v177\n"
"v_lshlrev_b32 v204, 16, v192\n"
"v_and_b32 v205, 0xffff0000, v192\n"
"buffer_load_dwordx4 v222, s[60:63], s80 offen lds\n"
"s_add_u32 m0, s83, 0x1cc00\n"
"v_lshlrev_b32 v206, 16, v193\n"
"v_and_b32 v207, 0xffff0000, v193\n"
"buffer_load_dwordx4 v223, s[60:63], s80 offen lds\n"
"s_add_u32 s79, s79, 0x60000\n"
"s_add_u32 s80, s80, 0x70000\n"
"buffer_load_dwordx2 v[234:235], v244, s[64:67], s91 offen sc1\n"
"s_mov_b64 exec, 0\n"
"s_cmp_eq_u32 s78, 0\n"
"s_cbranch_scc1 .Lhop0_%=\n"
"s_cmp_eq_u32 s78, 1\n"
"s_cbranch_scc1 .Lhop1_%=\n"
"s_cmp_eq_u32 s78, 2\n"
"s_cbranch_scc1 .Lhop2_%=\n"
"s_branch .Lhop3_%=\n"
".Lback0_%=:\n"
"s_mov_b64 exec, -1\n"
"s_waitcnt vmcnt(0)\n"
".Lchk0_%=:\n"
"v_cmp_ne_u64 vcc, s[84:85], v[234:235]\n"
"s_nop 1\n"
"s_cmp_eq_u64 vcc, 0\n"
"s_cbranch_scc1 .Lok0_%=\n"
"s_cmp_eq_u32 s94, 0\n"
"s_cbranch_scc1 .Lok0_%=\n"
"s_sub_u32 s94, s94, 1\n"
"s_sleep 1\n"
"buffer_load_dwordx2 v[234:235], v244, s[64:67], s91 offen sc1\n"
"s_waitcnt vmcnt(0)\n"
"s_branch .Lchk0_%=\n"
".Lok0_%=:\n"
"buffer_load_dwordx4 a[0:3], v224, s[64:67], s81 offen sc1\n"
"buffer_load_dwordx4 a[4:7], v224, s[64:67], s81 offen offset:1024 sc1\n"
"buffer_load_dwordx4 a[8:11], v224, s[64:67], s81 offen offset:2048 sc1\n"
"buffer_load_dwordx4 a[12:15], v224, s[64:67], s81 offen offset:3072 sc1\n"
"s_add_u32 s81, s81, 0x1000\n"
"s_add_u32 m0, s82, 0\n"
"s_nop 0\n"
"buffer_load_dwordx4 v217, s[56:59], s79 offen lds\n"
"s_add_u32 m0, s82, 0x400\n"
"s_nop 0\n"
"buffer_load_dwordx4 v218, s[56:59], s79 offen lds\n"
"s_add_u32 m0, s82, 0x800\n"
"s_nop 0\n"
"buffer_load_dwordx4 v219, s[56:59], s79 offen lds\n"
"s_add_u32 m0, s83, 0\n"
"s_nop 0\n"
"buffer_load_dwordx4 v220, s[60:63], s80 offen lds\n"
".Lhop0_%=:\n"
".Lpad0_%=:\n"
"s_cbranch_execz .Lpad4_%=\n"
"s_add_u32 m0, s83, 0x400\n"
"s_nop 0\n"
"buffer_load_dwordx4 v221, s[60:63], s80 offen lds\n"
"s_add_u32 m0, s83, 0x800\n"
"s_nop 0\n"
"buffer_load_dwordx4 v222, s[60:63], s80 offen lds\n"
"s_add_u32 m0, s83, 0xc00\n"
"s_nop 0\n"
"buffer_load_dwordx4 v223, s[60:63], s80 offen lds\n"
"s_add_u32 s79, s79, 0x60000\n"
"s_add_u32 s80, s80, 0x70000\n"
"s_barrier\n"
"buffer_store_dwordx2 v[228:229], v225, s[64:67], s90 offen offset:64 sc1\n"
"s_add_u32 s91, s91, 64\n"
"ds_read_b64_tr_b16 a[128:129], v214\n"
"ds_read_b64_tr_b16 a[130:131], v214 offset:3072\n"
"ds_read_b64_tr_b16 a[132:133], v215\n"
"ds_read_b64_tr_b16 a[134:135], v215 offset:3072\n"
"ds_read_b64_tr_b16 a[136:137], v214 offset:64\n"
"ds_read_b64_tr_b16 a[138:139], v214 offset:3136\n"
"ds_read_b64_tr_b16 a[140:141], v215 offset:64\n"
"ds_read_b64_tr_b16 a[142:143], v215 offset:3136\n"
"s_waitcnt vmcnt(8) lgkmcnt(6)\n"
"v_mfma_f32_16x16x32_bf16 v[0:3], a[128:131], a[0:3], 0\n"
"ds_read_b64_tr_b16 a[144:145], v214 offset:128\n"
"v_mfma_f32_16x16x32_bf16 v[4:7], a[128:131], a[4:7], 0\n"
"ds_read_b64_tr_b16 a[146:147], v214 offset:3200\n"
"v_mfma_f32_16x16x32_bf16 v[8:11], a[128:131], a[8:11], 0\n"
"buffer_load_dwordx4 a[16:19], v224, s[64:67], s81 offen sc1\n"
"v_mfma_f32_16x16x32_bf16 v[12:15], a[128:131], a[12:15], 0\n"
"s_waitcnt lgkmcnt(6)\n"
"v_mfma_f32_16x16x32_bf16 v[16:19], a[132:135], a[0:3], 0\n"
"ds_read_b64_tr_b16 a[148:149], v215 offset:128\n"
"v_mfma_f32_16x16x32_bf16 v[20:23], a[132:135], a[4:7], 0\n"
"ds_read_b64_tr_b16 a[150:151], v215 offset:3200\n"
"v_mfma_f32_16x16x32_bf16 v[24:27], a[132:135], a[8:11], 0\n"
"buffer_load_dwordx4 a[20:23], v224, s[64:67], s81 offen offset:1024 sc1\n"
"v_mfma_f32_16x16x32_bf16 v[28:31], a[132:135], a[12:15], 0\n"
"s_waitcnt lgkmcnt(6)\n"
"v_mfma_f32_16x16x32_bf16 v[32:35], a[136:139], a[0:3], 0\n"
"ds_read_b64_tr_b16 a[152:153], v216 offset:12288\n"
"v_mfma_f32_16x16x32_bf16 v[36:39], a[136:139], a[4:7], 0\n"
"ds_read_b64_tr_b16 a[154:155], v216 offset:16384\n"
"v_mfma_f32_16x16x32_bf16 v[40:43], a[136:139], a[8:11], 0\n"
"buffer_load_dwordx4 a[24:27], v224, s[64:67], s81 offen offset:2048 sc1\n"
"v_mfma_f32_16x16x32_bf16 v[44:47], a[136:139], a[12:15], 0\n"
"s_waitcnt lgkmcnt(6)\n"
"v_mfma_f32_16x16x32_bf16 v[48:51], a[140:143], a[0:3], 0\n"
"ds_read_b64_tr_b16 a[156:157], v216 offset:12320\n"
"v_mfma_f32_16x16x32_bf16 v[52:55], a[140:143], a[4:7], 0\n"
"ds_read_b64_tr_b16 a[158:159], v216 offset:16416\n"
"v_mfma_f32_16x16x32_bf16 v[56:59], a[140:143], a[8:11], 0\n"
"buffer_load_dwordx4 a[28:31], v224, s[64:67], s81 offen offset:3072 sc1\n"
"v_mfma_f32_16x16x32_bf16 v[60:63], a[140:143], a[12:15], 0\n"
"s_add_u32 s81, s81, 0x1000\n"
"s_waitcnt lgkmcnt(6)\n"
"v_mfma_f32_16x16x32_bf16 v[64:67], a[0:3], a[144:147], 0\n"
"ds_read_b64_tr_b16 a[128:129], v216 offset:12352\n"
"v_mfma_f32_16x16x32_bf16 v[68:71], a[4:7], a[144:147], 0\n"
"ds_read_b64_tr_b16 a[130:131], v216 offset:16448\n"
"v_mfma_f32_16x16x32_bf16 v[72:75], a[8:11], a[144:147], 0\n"
"buffer_load_dwordx4 a[32:35], v224, s[64:67], s81 offen sc1\n"
"v_mfma_f32_16x16x32_bf16 v[76:79], a[12:15], a[144:147], 0\n"
"s_waitcnt lgkmcnt(6)\n"
"v_mfma_f32_16x16x32_bf16 v[80:83], a[0:3], a[148:151], 0\n"
"ds_read_b64_tr_b16 a[132:133], v216 offset:12384\n"
"v_mfma_f32_16x16x32_bf16 v[84:87], a[4:7], a[148:151], 0\n"
"ds_read_b64_tr_b16 a[134:135], v216 offset:16480\n"
"v_mfma_f32_16x16x32_bf16 v[88:91], a[8:11], a[148:151], 0\n"
"buffer_load_dwordx4 a[36:39], v224, s[64:67], s81 offen offset:1024 sc1\n"
"v_mfma_f32_16x16x32_bf16 v[92:95], a[12:15], a[148:151], 0\n"
"s_waitcnt lgkmcnt(6)\n"
"v_mfma_f32_16x16x32_bf16 v[96:99], a[152:155], a[0:3], v[108:111]\n"
"ds_read_b64_tr_b16 a[136:137], v216 offset:12416\n"
"v_mfma_f32_16x16x32_bf16 v[100:103], a[152:155], a[4:7], v[108:111]\n"
"ds_read_b64_tr_b16 a[138:139], v216 offset:16512\n"
"v_mfma_f32_16x16x32_bf16 v[104:107], a[152:155], a[8:11], v[108:111]\n"
"buffer_load_dwordx4 a[40:43], v224, s[64:67], s81 offen offset:2048 sc1\n"
"v_mfma_f32_16x16x32_bf16 v[108:111], a[152:155], a[12:15], v[108:111]\n"
"s_waitcnt lgkmcnt(6)\n"
"v_mfma_f32_16x16x32_bf16 v[112:115], a[156:159], a[0:3], v[124:127]\n"
"ds_read_b64_tr_b16 a[140:141], v216 offset:12448\n"
"v_mfma_f32_16x16x32_bf16 v[116:119], a[156:159], a[4:7], v[124:127]\n"
"ds_read_b64_tr_b16 a[142:143], v216 offset:16544\n"
"v_mfma_f32_16x16x32_bf16 v[120:123], a[156:159], a[8:11], v[124:127]\n"
"buffer_load_dwordx4 a[44:47], v224, s[64:67], s81 offen offset:3072 sc1\n"
"v_mfma_f32_16x16x32_bf16 v[124:127], a[156:159], a[12:15], v[124:127]\n"
"s_add_u32 s81, s81, 0x1000\n"
"s_waitcnt lgkmcnt(6)\n"
"v_mfma_f32_16x16x32_bf16 v[128:131], a[128:131], a[0:3], v[140:143]\n"
"ds_read_b64_tr_b16 a[144:145], v216 offset:12480\n"
"v_mfma_f32_16x16x32_bf16 v[132:135], a[128:131], a[4:7], v[140:143]\n"
"ds_read_b64_tr_b16 a[146:147], v216 offset:16576\n"
"s_add_u32 m0, s82, 0x7000\n"
"v_mfma_f32_16x16x32_bf16 v[136:139], a[128:131], a[8:11], v[140:143]\n"
"buffer_load_dwordx4 v217, s[56:59], s79 offen lds\n"
"v_mfma_f32_16x16x32_bf16 v[140:143], a[128:131], a[12:15], v[140:143]\n"
"s_waitcnt lgkmcnt(6)\n"
"v_mfma_f32_16x16x32_bf16 v[144:147], a[132:135], a[0:3], v[156:159]\n"
"ds_read_b64_tr_b16 a[148:149], v214 offset:6144\n"
"v_mfma_f32_16x16x32_bf16 v[148:151], a[132:135], a[4:7], v[156:159]\n"
"ds_read_b64_tr_b16 a[150:151], v214 offset:9216\n"
"v_mfma_f32_16x16x32_bf16 v[152:155], a[132:135], a[8:11], v[156:159]\n"
"s_mov_b32 s72, s40\n"
"v_mfma_f32_16x16x32_bf16 v[156:159], a[132:135], a[12:15], v[156:159]\n"
"s_and_b32 s73, s41, 0xffff\n"
"s_waitcnt lgkmcnt(6)\n"
"v_mfma_f32_16x16x32_bf16 v[160:163], a[136:139], a[0:3], v[172:175]\n"
"ds_read_b64_tr_b16 a[152:153], v215 offset:6144\n"
"v_mfma_f32_16x16x32_bf16 v[164:167], a[136:139], a[4:7], v[172:175]\n"
"ds_read_b64_tr_b16 a[154:155], v215 offset:9216\n"
"s_add_u32 m0, s82, 0x7400\n"
"v_mfma_f32_16x16x32_bf16 v[168:171], a[136:139], a[8:11], v[172:175]\n"
"buffer_load_dwordx4 v218, s[56:59], s79 offen lds\n"
"v_mfma_f32_16x16x32_bf16 v[172:175], a[136:139], a[12:15], v[172:175]\n"
"s_waitcnt lgkmcnt(6)\n"
"v_mfma_f32_16x16x32_bf16 v[176:179], a[140:143], a[0:3], v[188:191]\n"
"ds_read_b64_tr_b16 a[156:157], v214 offset:6208\n"
"v_mfma_f32_16x16x32_bf16 v[180:183], a[140:143], a[4:7], v[188:191]\n"
"ds_read_b64_tr_b16 a[158:159], v214 offset:9280\n"
"v_mfma_f32_16x16x32_bf16 v[184:187], a[140:143], a[8:11], v[188:191]\n"
"s_mov_b32 s74, 0x200000\n"
"v_mfma_f32_16x16x32_bf16 v[188:191], a[140:143], a[12:15], v[188:191]\n"
"s_mov_b32 s75, 0x20000\n"
"s_waitcnt lgkmcnt(6)\n"
"v_mfma_f32_16x16x32_bf16 v[192:195], a[144:147], a[0:3], v[204:207]\n"
"ds_read_b64_tr_b16 a[128:129], v215 offset:6208\n"
"v_mfma_f32_16x16x32_bf16 v[196:199], a[144:147], a[4:7], v[204:207]\n"
"ds_read_b64_tr_b16 a[130:131], v215 offset:9280\n"
"v_mfma_f32_16x16x32_bf16 v[200:203], a[144:147], a[8:11], v[204:207]\n"
"s_lshl_b32 s86, s54, 2\n"
"v_mfma_f32_16x16x32_bf16 v[204:207], a[144:147], a[12:15], v[204:207]\n"
"s_add_u32 s86, s86, s78\n"
"s_waitcnt vmcnt(6) lgkmcnt(6)\n"
"v_mfma_f32_16x16x32_bf16 v[0:3], a[148:151], a[16:19], v[0:3]\n"
"ds_read_b64_tr_b16 a[132:133], v214 offset:6272\n"
"v_mfma_f32_16x16x32_bf16 v[4:7], a[148:151], a[20:23], v[4:7]\n"
"ds_read_b64_tr_b16 a[134:135], v214 offset:9344\n"
"s_add_u32 m0, s82, 0x7800\n"
"v_mfma_f32_16x16x32_bf16 v[8:11], a[148:151], a[24:27], v[8:11]\n"
"buffer_load_dwordx4 v219, s[56:59], s79 offen lds\n"
"v_mfma_f32_16x16x32_bf16 v[12:15], a[148:151], a[28:31], v[12:15]\n"
"s_add_u32 s79, s79, 0x60000\n"
"s_waitcnt lgkmcnt(6)\n"
"v_mfma_f32_16x16x32_bf16 v[28:31], a[152:155], a[28:31], v[28:31]\n"
"ds_read_b64_tr_b16 a[136:137], v215 offset:6272\n"
"v_mfma_f32_16x16x32_bf16 v[24:27], a[152:155], a[24:27], v[24:27]\n"
"ds_read_b64_tr_b16 a[138:139], v215 offset:9344\n"
"s_add_u32 m0, s83, 0x7000\n"
"v_mfma_f32_16x16x32_bf16 v[20:23], a[152:155], a[20:23], v[20:23]\n"
"buffer_load_dwordx4 v220, s[60:63], s80 offen lds\n"
"v_mfma_f32_16x16x32_bf16 v[16:19], a[152:155], a[16:19], v[16:19]\n"
"s_waitcnt lgkmcnt(6)\n"
"v_mfma_f32_16x16x32_bf16 v[32:35], a[156:159], a[16:19], v[32:35]\n"
"ds_read_b64_tr_b16 a[140:141], v216 offset:20480\n"
"v_mfma_f32_16x16x32_bf16 v[36:39], a[156:159], a[20:23], v[36:39]\n"
"ds_read_b64_tr_b16 a[142:143], v216 offset:24576\n"
"v_mfma_f32_16x16x32_bf16 v[40:43], a[156:159], a[24:27], v[40:43]\n"
"s_lshr_b32 s90, s54, 3\n"
"v_mfma_f32_16x16x32_bf16 v[44:47], a[156:159], a[28:31], v[44:47]\n"
"s_lshl_b32 s90, s90, 2\n"
"s_waitcnt lgkmcnt(6)\n"
"v_mfma_f32_16x16x32_bf16 v[60:63], a[128:131], a[28:31], v[60:63]\n"
"ds_read_b64_tr_b16 a[144:145], v216 offset:20512\n"
"v_mfma_f32_16x16x32_bf16 v[56:59], a[128:131], a[24:27], v[56:59]\n"
"ds_read_b64_tr_b16 a[146:147], v216 offset:24608\n"
"s_add_u32 m0, s83, 0x7400\n"
"v_mfma_f32_16x16x32_bf16 v[52:55], a[128:131], a[20:23], v[52:55]\n"
"buffer_load_dwordx4 v221, s[60:63], s80 offen lds\n"
"v_mfma_f32_16x16x32_bf16 v[48:51], a[128:131], a[16:19], v[48:51]\n"
"s_waitcnt lgkmcnt(6)\n"
"v_mfma_f32_16x16x32_bf16 v[64:67], a[16:19], a[132:135], v[64:67]\n"
"ds_read_b64_tr_b16 a[148:149], v216 offset:20544\n"
"v_mfma_f32_16x16x32_bf16 v[68:71], a[20:23], a[132:135], v[68:71]\n"
"ds_read_b64_tr_b16 a[150:151], v216 offset:24640\n"
"s_add_u32 m0, s83, 0x7800\n"
"v_mfma_f32_16x16x32_bf16 v[72:75], a[24:27], a[132:135], v[72:75]\n"
"buffer_load_dwordx4 v222, s[60:63], s80 offen lds\n"
"v_mfma_f32_16x16x32_bf16 v[76:79], a[28:31], a[132:135], v[76:79]\n"
"s_waitcnt lgkmcnt(6)\n"
"v_mfma_f32_16x16x32_bf16 v[92:95], a[28:31], a[136:139], v[92:95]\n"
"ds_read_b64_tr_b16 a[152:153], v216 offset:20576\n"
"v_mfma_f32_16x16x32_bf16 v[88:91], a[24:27], a[136:139], v[88:91]\n"
"ds_read_b64_tr_b16 a[154:155], v216 offset:24672\n"
"v_mfma_f32_16x16x32_bf16 v[84:87], a[20:23], a[136:139], v[84:87]\n"
"s_add_u32 s90, s90, s78\n"
"v_mfma_f32_16x16x32_bf16 v[80:83], a[16:19], a[136:139], v[80:83]\n"
"s_bfe_u32 s92, s54, 0x20001\n"
"s_waitcnt lgkmcnt(6)\n"
"v_mfma_f32_16x16x32_bf16 v[96:99], a[140:143], a[16:19], v[96:99]\n"
"ds_read_b64_tr_b16 a[156:157], v216 offset:20608\n"
"v_mfma_f32_16x16x32_bf16 v[100:103], a[140:143], a[20:23], v[100:103]\n"
"ds_read_b64_tr_b16 a[158:159], v216 offset:24704\n"
"s_add_u32 m0, s83, 0x7c00\n"
"v_mfma_f32_16x16x32_bf16 v[104:107], a[140:143], a[24:27], v[104:107]\n"
"buffer_load_dwordx4 v223, s[60:63], s80 offen lds\n"
"v_mfma_f32_16x16x32_bf16 v[108:111], a[140:143], a[28:31], v[108:111]\n"
"s_add_u32 s80, s80, 0x70000\n"
"s_waitcnt lgkmcnt(6)\n"
"v_mfma_f32_16x16x32_bf16 v[124:127], a[144:147], a[28:31], v[124:127]\n"
"ds_read_b64_tr_b16 a[128:129], v216 offset:20640\n"
"v_mfma_f32_16x16x32_bf16 v[120:123], a[144:147], a[24:27], v[120:123]\n"
"ds_read_b64_tr_b16 a[130:131], v216 offset:24736\n"
"v_mfma_f32_16x16x32_bf16 v[116:119], a[144:147], a[20:23], v[116:119]\n"
"buffer_load_dwordx4 a[48:51], v224, s[64:67], s81 offen sc1\n"
"v_mfma_f32_16x16x32_bf16 v[112:115], a[144:147], a[16:19], v[112:115]\n"
"s_waitcnt lgkmcnt(6)\n"
"v_mfma_f32_16x16x32_bf16 v[128:131], a[148:151], a[16:19], v[128:131]\n"
"ds_read_b64_tr_b16 a[132:133], v216 offset:20672\n"
"v_mfma_f32_16x16x32_bf16 v[132:135], a[148:151], a[20:23], v[132:135]\n"
"ds_read_b64_tr_b16 a[134:135], v216 offset:24768\n"
"v_mfma_f32_16x16x32_bf16 v[136:139], a[148:151], a[24:27], v[136:139]\n"
"buffer_load_dwordx4 a[52:55], v224, s[64:67], s81 offen offset:1024 sc1\n"
"v_mfma_f32_16x16x32_bf16 v[140:143], a[148:151], a[28:31], v[140:143]\n"
"s_barrier\n"
"s_waitcnt lgkmcnt(6)\n"
"v_mfma_f32_16x16x32_bf16 v[156:159], a[152:155], a[28:31], v[156:159]\n"
"ds_read_b64_tr_b16 a[136:137], v208\n"
"v_mfma_f32_16x16x32_bf16 v[152:155], a[152:155], a[24:27], v[152:155]\n"
"ds_read_b64_tr_b16 a[138:139], v208 offset:3072\n"
"v_mfma_f32_16x16x32_bf16 v[148:151], a[152:155], a[20:23], v[148:151]\n"
"buffer_load_dwordx4 a[56:59], v224, s[64:67], s81 offen offset:2048 sc1\n"
"v_mfma_f32_16x16x32_bf16 v[144:147], a[152:155], a[16:19], v[144:147]\n"
"s_waitcnt lgkmcnt(6)\n"
"v_mfma_f32_16x16x32_bf16 v[160:163], a[156:159], a[16:19], v[160:163]\n"
"ds_read_b64_tr_b16 a[140:141], v209\n"
"v_mfma_f32_16x16x32_bf16 v[164:167], a[156:159], a[20:23], v[164:167]\n"
"ds_read_b64_tr_b16 a[142:143], v209 offset:3072\n"
"v_mfma_f32_16x16x32_bf16 v[168:171], a[156:159], a[24:27], v[168:171]\n"
"buffer_load_dwordx4 a[60:63], v224, s[64:67], s81 offen offset:3072 sc1\n"
"v_mfma_f32_16x16x32_bf16 v[172:175], a[156:159], a[28:31], v[172:175]\n"
"s_add_u32 s81, s81, 0x1000\n"
"s_waitcnt lgkmcnt(6)\n"
"v_mfma_f32_16x16x32_bf16 v[188:191], a[128:131], a[28:31], v[188:191]\n"
"ds_read_b64_tr_b16 a[144:145], v208 offset:64\n"
"v_mfma_f32_16x16x32_bf16 v[184:187], a[128:131], a[24:27], v[184:187]\n"
"ds_read_b64_tr_b16 a[146:147], v208 offset:3136\n"
"v_mfma_f32_16x16x32_bf16 v[180:183], a[128:131], a[20:23], v[180:183]\n"
"buffer_load_dwordx4 a[64:67], v224, s[64:67], s81 offen sc1\n"
"v_mfma_f32_16x16x32_bf16 v[176:179], a[128:131], a[16:19], v[176:179]\n"
"s_waitcnt lgkmcnt(6)\n"
"v_mfma_f32_16x16x32_bf16 v[192:195], a[132:135], a[16:19], v[192:195]\n"
"ds_read_b64_tr_b16 a[148:149], v209 offset:64\n"
"v_mfma_f32_16x16x32_bf16 v[196:199], a[132:135], a[20:23], v[196:199]\n"
"ds_read_b64_tr_b16 a[150:151], v209 offset:3136\n"
"v_mfma_f32_16x16x32_bf16 v[200:203], a[132:135], a[24:27], v[200:203]\n"
"buffer_load_dwordx4 a[68:71], v224, s[64:67], s81 offen offset:1024 sc1\n"
"v_mfma_f32_16x16x32_bf16 v[204:207], a[132:135], a[28:31], v[204:207]\n"
"s_waitcnt vmcnt(13) lgkmcnt(6)\n"
"v_mfma_f32_16x16x32_bf16 v[0:3], a[136:139], a[32:35], v[0:3]\n"
"ds_read_b64_tr_b16 a[152:153], v208 offset:128\n"
"v_mfma_f32_16x16x32_bf16 v[4:7], a[136:139], a[36:39], v[4:7]\n"
"ds_read_b64_tr_b16 a[154:155], v208 offset:3200\n"
"v_mfma_f32_16x16x32_bf16 v[8:11], a[136:139], a[40:43], v[8:11]\n"
"s_and_b32 s93, s54, 1\n"
"v_mfma_f32_16x16x32_bf16 v[12:15], a[136:139], a[44:47], v[12:15]\n"
"s_lshl_b32 s93, s93, 10\n"
"s_waitcnt lgkmcnt(6)\n"
"v_mfma_f32_16x16x32_bf16 v[28:31], a[140:143], a[44:47], v[28:31]\n"
"ds_read_b64_tr_b16 a[156:157], v209 offset:128\n"
"v_mfma_f32_16x16x32_bf16 v[24:27], a[140:143], a[40:43], v[24:27]\n"
"ds_read_b64_tr_b16 a[158:159], v209 offset:3200\n"
"v_mfma_f32_16x16x32_bf16 v[20:23], a[140:143], a[36:39], v[20:23]\n"
"buffer_load_dwordx4 a[72:75], v224, s[64:67], s81 offen offset:2048 sc1\n"
"v_mfma_f32_16x16x32_bf16 v[16:19], a[140:143], a[32:35], v[16:19]\n"
"s_waitcnt lgkmcnt(6)\n"
"v_mfma_f32_16x16x32_bf16 v[32:35], a[144:147], a[32:35], v[32:35]\n"
"ds_read_b64_tr_b16 a[128:129], v210 offset:12288\n"
"v_mfma_f32_16x16x32_bf16 v[36:39], a[144:147], a[36:39], v[36:39]\n"
"ds_read_b64_tr_b16 a[130:131], v210 offset:16384\n"
"v_mfma_f32_16x16x32_bf16 v[40:43], a[144:147], a[40:43], v[40:43]\n"
"buffer_load_dwordx4 a[76:79], v224, s[64:67], s81 offen offset:3072 sc1\n"
"v_mfma_f32_16x16x32_bf16 v[44:47], a[144:147], a[44:47], v[44:47]\n"
"s_add_u32 s81, s81, 0x1000\n"
"s_waitcnt lgkmcnt(6)\n"
"v_mfma_f32_16x16x32_bf16 v[60:63], a[148:151], a[44:47], v[60:63]\n"
"ds_read_b64_tr_b16 a[132:133], v210 offset:12320\n"
"v_mfma_f32_16x16x32_bf16 v[56:59], a[148:151], a[40:43], v[56:59]\n"
"ds_read_b64_tr_b16 a[134:135], v210 offset:16416\n"
"v_mfma_f32_16x16x32_bf16 v[52:55], a[148:151], a[36:39], v[52:55]\n"
"s_mul_i32 s26, s92, 0x380\n"
"v_mfma_f32_16x16x32_bf16 v[48:51], a[148:151], a[32:35], v[48:51]\n"
"s_mul_i32 s27, s90, 7\n"
"s_waitcnt lgkmcnt(6)\n"
"v_mfma_f32_16x16x32_bf16 v[64:67], a[32:35], a[152:155], v[64:67]\n"
"ds_read_b64_tr_b16 a[136:137], v210 offset:12352\n"
"v_mfma_f32_16x16x32_bf16 v[68:71], a[36:39], a[152:155], v[68:71]\n"
"ds_read_b64_tr_b16 a[138:139], v210 offset:16448\n"
"v_mfma_f32_16x16x32_bf16 v[72:75], a[40:43], a[152:155], v[72:75]\n"
"buffer_load_dwordx2 v[234:235], v244, s[64:67], s91 offen sc1\n"
"v_mfma_f32_16x16x32_bf16 v[76:79], a[44:47], a[152:155], v[76:79]\n"
"s_waitcnt lgkmcnt(6)\n"
"v_mfma_f32_16x16x32_bf16 v[92:95], a[44:47], a[156:159], v[92:95]\n"
"ds_read_b64_tr_b16 a[140:141], v210 offset:12384\n"
"v_mfma_f32_16x16x32_bf16 v[88:91], a[40:43], a[156:159], v[88:91]\n"
"ds_read_b64_tr_b16 a[142:143], v210 offset:16480\n"
"s_add_u32 m0, s82, 0xe000\n"
"v_mfma_f32_16x16x32_bf16 v[84:87], a[36:39], a[156:159], v[84:87]\n"
"buffer_load_dwordx4 v217, s[56:59], s79 offen lds\n"
"v_mfma_f32_16x16x32_bf16 v[80:83], a[32:35], a[156:159], v[80:83]\n"
"s_waitcnt lgkmcnt(6)\n"
"v_mfma_f32_16x16x32_bf16 v[96:99], a[128:131], a[32:35], v[96:99]\n"
"ds_read_b64_tr_b16 a[144:145], v210 offset:12416\n"
"v_mfma_f32_16x16x32_bf16 v[100:103], a[128:131], a[36:39], v[100:103]\n"
"ds_read_b64_tr_b16 a[146:147], v210 offset:16512\n"
"v_mfma_f32_16x16x32_bf16 v[104:107], a[128:131], a[40:43], v[104:107]\n"
"s_add_u32 s26, s26, s27\n"
"v_mfma_f32_16x16x32_bf16 v[108:111], a[128:131], a[44:47], v[108:111]\n"
"s_lshl_b32 s26, s26, 11\n"
"s_waitcnt lgkmcnt(6)\n"
"v_mfma_f32_16x16x32_bf16 v[124:127], a[132:135], a[44:47], v[124:127]\n"
"ds_read_b64_tr_b16 a[148:149], v210 offset:12448\n"
"v_mfma_f32_16x16x32_bf16 v[120:123], a[132:135], a[40:43], v[120:123]\n"
"ds_read_b64_tr_b16 a[150:151], v210 offset:16544\n"
"s_add_u32 m0, s82, 0xe400\n"
"v_mfma_f32_16x16x32_bf16 v[116:119], a[132:135], a[36:39], v[116:119]\n"
"buffer_load_dwordx4 v218, s[56:59], s79 offen lds\n"
"v_mfma_f32_16x16x32_bf16 v[112:115], a[132:135], a[32:35], v[112:115]\n"
"s_waitcnt lgkmcnt(6)\n"
"v_mfma_f32_16x16x32_bf16 v[128:131], a[136:139], a[32:35], v[128:131]\n"
"ds_read_b64_tr_b16 a[152:153], v210 offset:12480\n"
"v_mfma_f32_16x16x32_bf16 v[132:135], a[136:139], a[36:39], v[132:135]\n"
"ds_read_b64_tr_b16 a[154:155], v210 offset:16576\n"
"v_mfma_f32_16x16x32_bf16 v[136:139], a[136:139], a[40:43], v[136:139]\n"
"buffer_load_dword a164, v249, s[56:59], s29 offen\n"
"v_mfma_f32_16x16x32_bf16 v[140:143], a[136:139], a[44:47], v[140:143]\n"
"s_add_u32 s29, s29, 0x180000\n"
"s_waitcnt lgkmcnt(6)\n"
"v_mfma_f32_16x16x32_bf16 v[156:159], a[140:143], a[44:47], v[156:159]\n"
"ds_read_b64_tr_b16 a[156:157], v208 offset:6144\n"
"v_mfma_f32_16x16x32_bf16 v[152:155], a[140:143], a[40:43], v[152:155]\n"
"ds_read_b64_tr_b16 a[158:159], v208 offset:9216\n"
"v_mfma_f32_16x16x32_bf16 v[148:151], a[140:143], a[36:39], v[148:151]\n"
"buffer_load_dword a165, v253, s[60:63], s30 offen\n"
"v_mfma_f32_16x16x32_bf16 v[144:147], a[140:143], a[32:35], v[144:147]\n"
"s_add_u32 s30, s30, 0x1c0000\n"
"s_waitcnt lgkmcnt(6)\n"
"v_mfma_f32_16x16x32_bf16 v[160:163], a[144:147], a[32:35], v[160:163]\n"
"ds_read_b64_tr_b16 a[128:129], v209 offset:6144\n"
"v_mfma_f32_16x16x32_bf16 v[164:167], a[144:147], a[36:39], v[164:167]\n"
"ds_read_b64_tr_b16 a[130:131], v209 offset:9216\n"
"v_mfma_f32_16x16x32_bf16 v[168:171], a[144:147], a[40:43], v[168:171]\n"
"s_add_u32 s26, s26, s93\n"
"v_mfma_f32_16x16x32_bf16 v[172:175], a[144:147], a[44:47], v[172:175]\n"
"s_and_b32 s27, s92, 1\n"
"s_waitcnt lgkmcnt(6)\n"
"v_mfma_f32_16x16x32_bf16 v[188:191], a[148:151], a[44:47], v[188:191]\n"
"ds_read_b64_tr_b16 a[132:133], v208 offset:6208\n"
"v_mfma_f32_16x16x32_bf16 v[184:187], a[148:151], a[40:43], v[184:187]\n"
"ds_read_b64_tr_b16 a[134:135], v208 offset:9280\n"
"v_mfma_f32_16x16x32_bf16 v[180:183], a[148:151], a[36:39], v[180:183]\n"
"s_lshl_b32 s27, s27, 7\n"
"v_mfma_f32_16x16x32_bf16 v[176:179], a[148:151], a[32:35], v[176:179]\n"
"s_add_u32 s90, s90, s27\n"
"s_waitcnt lgkmcnt(6)\n"
"v_mfma_f32_16x16x32_bf16 v[192:195], a[152:155], a[32:35], v[192:195]\n"
"ds_read_b64_tr_b16 a[136:137], v209 offset:6208\n"
"v_mfma_f32_16x16x32_bf16 v[196:199], a[152:155], a[36:39], v[196:199]\n"
"ds_read_b64_tr_b16 a[138:139], v209 offset:9280\n"
"v_mfma_f32_16x16x32_bf16 v[200:203], a[152:155], a[40:43], v[200:203]\n"
"s_lshl_b32 s27, s90, 13\n"
"v_mfma_f32_16x16x32_bf16 v[204:207], a[152:155], a[44:47], v[204:207]\n"
"s_add_u32 s27, s27, s93\n"
"s_waitcnt vmcnt(9) lgkmcnt(6)\n"
"v_mfma_f32_16x16x32_bf16 v[0:3], a[156:159], a[48:51], v[0:3]\n"
"ds_read_b64_tr_b16 a[140:141], v208 offset:6272\n"
"v_mfma_f32_16x16x32_bf16 v[4:7], a[156:159], a[52:55], v[4:7]\n"
"ds_read_b64_tr_b16 a[142:143], v208 offset:9344\n"
"v_mfma_f32_16x16x32_bf16 v[8:11], a[156:159], a[56:59], v[8:11]\n"
"s_lshr_b32 s92, s92, 1\n"
"v_mfma_f32_16x16x32_bf16 v[12:15], a[156:159], a[60:63], v[12:15]\n"
"s_lshl_b32 s92, s92, 7\n"
"s_waitcnt lgkmcnt(6)\n"
"v_mfma_f32_16x16x32_bf16 v[28:31], a[128:131], a[60:63], v[28:31]\n"
"ds_read_b64_tr_b16 a[144:145], v209 offset:6272\n"
"v_mfma_f32_16x16x32_bf16 v[24:27], a[128:131], a[56:59], v[24:27]\n"
"ds_read_b64_tr_b16 a[146:147], v209 offset:9344\n"
"s_add_u32 m0, s82, 0xe800\n"
"v_mfma_f32_16x16x32_bf16 v[20:23], a[128:131], a[52:55], v[20:23]\n"
"buffer_load_dwordx4 v219, s[56:59], s79 offen lds\n"
"v_mfma_f32_16x16x32_bf16 v[16:19], a[128:131], a[48:51], v[16:19]\n"
"s_add_u32 s79, s79, 0x60000\n"
"s_waitcnt lgkmcnt(6)\n"
"v_mfma_f32_16x16x32_bf16 v[32:35], a[132:135], a[48:51], v[32:35]\n"
"ds_read_b64_tr_b16 a[148:149], v210 offset:20480\n"
"v_mfma_f32_16x16x32_bf16 v[36:39], a[132:135], a[52:55], v[36:39]\n"
"ds_read_b64_tr_b16 a[150:151], v210 offset:24576\n"
"s_add_u32 m0, s83, 0xe000\n"
"v_mfma_f32_16x16x32_bf16 v[40:43], a[132:135], a[56:59], v[40:43]\n"
"buffer_load_dwordx4 v220, s[60:63], s80 offen lds\n"
"v_mfma_f32_16x16x32_bf16 v[44:47], a[132:135], a[60:63], v[44:47]\n"
"s_waitcnt lgkmcnt(6)\n"
"v_mfma_f32_16x16x32_bf16 v[60:63], a[136:139], a[60:63], v[60:63]\n"
"ds_read_b64_tr_b16 a[152:153], v210 offset:20512\n"
"v_mfma_f32_16x16x32_bf16 v[56:59], a[136:139], a[56:59], v[56:59]\n"
"ds_read_b64_tr_b16 a[154:155], v210 offset:24608\n"
"s_add_u32 m0, s83, 0xe400\n"
"v_mfma_f32_16x16x32_bf16 v[52:55], a[136:139], a[52:55], v[52:55]\n"
"buffer_load_dwordx4 v221, s[60:63], s80 offen lds\n"
"v_mfma_f32_16x16x32_bf16 v[48:51], a[136:139], a[48:51], v[48:51]\n"
"s_waitcnt lgkmcnt(6)\n"
"v_mfma_f32_16x16x32_bf16 v[64:67], a[48:51], a[140:143], v[64:67]\n"
"ds_read_b64_tr_b16 a[156:157], v210 offset:20544\n"
"v_mfma_f32_16x16x32_bf16 v[68:71], a[52:55], a[140:143], v[68:71]\n"
"ds_read_b64_tr_b16 a[158:159], v210 offset:24640\n"
"v_mfma_f32_16x16x32_bf16 v[72:75], a[56:59], a[140:143], v[72:75]\n"
"s_add_u32 s27, s27, s92\n"
"v_mfma_f32_16x16x32_bf16 v[76:79], a[60:63], a[140:143], v[76:79]\n"
"s_waitcnt lgkmcnt(6)\n"
"v_min_u32 v240, 31, v236\n"
"v_mfma_f32_16x16x32_bf16 v[92:95], a[60:63], a[144:147], v[92:95]\n"
"ds_read_b64_tr_b16 a[128:129], v210 offset:20576\n"
"v_mfma_f32_16x16x32_bf16 v[88:91], a[56:59], a[144:147], v[88:91]\n"
"ds_read_b64_tr_b16 a[130:131], v210 offset:24672\n"
"s_add_u32 m0, s83, 0xe800\n"
"v_mfma_f32_16x16x32_bf16 v[84:87], a[52:55], a[144:147], v[84:87]\n"
"buffer_load_dwordx4 v222, s[60:63], s80 offen lds\n"
"v_mfma_f32_16x16x32_bf16 v[80:83], a[48:51], a[144:147], v[80:83]\n"
"s_waitcnt lgkmcnt(6)\n"
"v_mfma_f32_16x16x32_bf16 v[96:99], a[148:151], a[48:51], v[96:99]\n"
"ds_read_b64_tr_b16 a[132:133], v210 offset:20608\n"
"v_mfma_f32_16x16x32_bf16 v[100:103], a[148:151], a[52:55], v[100:103]\n"
"ds_read_b64_tr_b16 a[134:135], v210 offset:24704\n"
"v_mfma_f32_16x16x32_bf16 v[104:107], a[148:151], a[56:59], v[104:107]\n"
"v_lshrrev_b32 v242, 3, v240\n"
"v_mfma_f32_16x16x32_bf16 v[108:111], a[148:151], a[60:63], v[108:111]\n"
"s_waitcnt lgkmcnt(6)\n"
"v_and_b32 v240, 7, v240\n"
"v_mfma_f32_16x16x32_bf16 v[124:127], a[152:155], a[60:63], v[124:127]\n"
"ds_read_b64_tr_b16 a[136:137], v210 offset:20640\n"
"v_mfma_f32_16x16x32_bf16 v[120:123], a[152:155], a[56:59], v[120:123]\n"
"ds_read_b64_tr_b16 a[138:139], v210 offset:24736\n"
"s_add_u32 m0, s83, 0xec00\n"
"v_mfma_f32_16x16x32_bf16 v[116:119], a[152:155], a[52:55], v[116:119]\n"
"buffer_load_dwordx4 v223, s[60:63], s80 offen lds\n"
"v_mfma_f32_16x16x32_bf16 v[112:115], a[152:155], a[48:51], v[112:115]\n"
"s_add_u32 s80, s80, 0x70000\n"
"s_waitcnt lgkmcnt(6)\n"
"v_mfma_f32_16x16x32_bf16 v[128:131], a[156:159], a[48:51], v[128:131]\n"
"ds_read_b64_tr_b16 a[140:141], v210 offset:20672\n"
"v_mfma_f32_16x16x32_bf16 v[132:135], a[156:159], a[52:55], v[132:135]\n"
"ds_read_b64_tr_b16 a[142:143], v210 offset:24768\n"
"v_mfma_f32_16x16x32_bf16 v[136:139], a[156:159], a[56:59], v[136:139]\n"
"buffer_load_dwordx4 a[80:83], v224, s[64:67], s81 offen sc1\n"
"v_mfma_f32_16x16x32_bf16 v[140:143], a[156:159], a[60:63], v[140:143]\n"
"s_barrier\n"
"s_waitcnt lgkmcnt(6)\n"
"v_mfma_f32_16x16x32_bf16 v[156:159], a[128:131], a[60:63], v[156:159]\n"
"ds_read_b64_tr_b16 a[144:145], v208 offset:28672\n"
"v_mfma_f32_16x16x32_bf16 v[152:155], a[128:131], a[56:59], v[152:155]\n"
"ds_read_b64_tr_b16 a[146:147], v208 offset:31744\n"
"v_mfma_f32_16x16x32_bf16 v[148:151], a[128:131], a[52:55], v[148:151]\n"
"buffer_load_dwordx4 a[84:87], v224, s[64:67], s81 offen offset:1024 sc1\n"
"v_mfma_f32_16x16x32_bf16 v[144:147], a[128:131], a[48:51], v[144:147]\n"
"s_waitcnt lgkmcnt(6)\n"
"v_mfma_f32_16x16x32_bf16 v[160:163], a[132:135], a[48:51], v[160:163]\n"
"ds_read_b64_tr_b16 a[148:149], v209 offset:28672\n"
"v_mfma_f32_16x16x32_bf16 v[164:167], a[132:135], a[52:55], v[164:167]\n"
"ds_read_b64_tr_b16 a[150:151], v209 offset:31744\n"
"v_mfma_f32_16x16x32_bf16 v[168:171], a[132:135], a[56:59], v[168:171]\n"
"buffer_load_dwordx4 a[88:91], v224, s[64:67], s81 offen offset:2048 sc1\n"
"v_mfma_f32_16x16x32_bf16 v[172:175], a[132:135], a[60:63], v[172:175]\n"
"s_waitcnt lgkmcnt(6)\n"
"v_mfma_f32_16x16x32_bf16 v[188:191], a[136:139], a[60:63], v[188:191]\n"
"ds_read_b64_tr_b16 a[152:153], v208 offset:28736\n"
"v_mfma_f32_16x16x32_bf16 v[184:187], a[136:139], a[56:59], v[184:187]\n"
"ds_read_b64_tr_b16 a[154:155], v208 offset:31808\n"
"v_mfma_f32_16x16x32_bf16 v[180:183], a[136:139], a[52:55], v[180:183]\n"
"buffer_load_dwordx4 a[92:95], v224, s[64:67], s81 offen offset:3072 sc1\n"
"v_mfma_f32_16x16x32_bf16 v[176:179], a[136:139], a[48:51], v[176:179]\n"
"s_add_u32 s81, s81, 0x1000\n"
"s_waitcnt lgkmcnt(6)\n"
"v_mfma_f32_16x16x32_bf16 v[192:195], a[140:143], a[48:51], v[192:195]\n"
"ds_read_b64_tr_b16 a[156:157], v209 offset:28736\n"
"v_mfma_f32_16x16x32_bf16 v[196:199], a[140:143], a[52:55], v[196:199]\n"
"ds_read_b64_tr_b16 a[158:159], v209 offset:31808\n"
"v_mfma_f32_16x16x32_bf16 v[200:203], a[140:143], a[56:59], v[200:203]\n"
"buffer_load_dwordx4 a[96:99], v224, s[64:67], s81 offen sc1\n"
"v_mfma_f32_16x16x32_bf16 v[204:207], a[140:143], a[60:63], v[204:207]\n"
"s_waitcnt vmcnt(15) lgkmcnt(6)\n"
"v_mfma_f32_16x16x32_bf16 v[0:3], a[144:147], a[64:67], v[0:3]\n"
"ds_read_b64_tr_b16 a[128:129], v208 offset:28800\n"
"v_mfma_f32_16x16x32_bf16 v[4:7], a[144:147], a[68:71], v[4:7]\n"
"ds_read_b64_tr_b16 a[130:131], v208 offset:31872\n"
"v_mfma_f32_16x16x32_bf16 v[8:11], a[144:147], a[72:75], v[8:11]\n"
"s_waitcnt vmcnt(14)\n"
".Lchk1_%=:\n"
"v_cmp_ne_u64 vcc, s[84:85], v[234:235]\n"
"s_nop 1\n"
"s_cmp_eq_u64 vcc, 0\n"
"s_cbranch_scc1 .Lok1_%=\n"
"s_cmp_eq_u32 s94, 0\n"
"s_cbranch_scc1 .Lok1_%=\n"
"s_sub_u32 s94, s94, 1\n"
"s_sleep 1\n"
"buffer_load_dwordx2 v[234:235], v244, s[64:67], s91 offen sc1\n"
"s_waitcnt vmcnt(0)\n"
"s_branch .Lchk1_%=\n"
".Lok1_%=:\n"
"v_mfma_f32_16x16x32_bf16 v[12:15], a[144:147], a[76:79], v[12:15]\n"
"s_waitcnt lgkmcnt(6)\n"
"v_mfma_f32_16x16x32_bf16 v[28:31], a[148:151], a[76:79], v[28:31]\n"
"ds_read_b64_tr_b16 a[132:133], v209 offset:28800\n"
"v_mfma_f32_16x16x32_bf16 v[24:27], a[148:151], a[72:75], v[24:27]\n"
"ds_read_b64_tr_b16 a[134:135], v209 offset:31872\n"
"v_mfma_f32_16x16x32_bf16 v[20:23], a[148:151], a[68:71], v[20:23]\n"
"buffer_load_dwordx4 a[100:103], v224, s[64:67], s81 offen offset:1024 sc1\n"
"v_mfma_f32_16x16x32_bf16 v[16:19], a[148:151], a[64:67], v[16:19]\n"
"s_waitcnt lgkmcnt(6)\n"
"v_mfma_f32_16x16x32_bf16 v[32:35], a[152:155], a[64:67], v[32:35]\n"
"ds_read_b64_tr_b16 a[136:137], v210 offset:40960\n"
"v_mfma_f32_16x16x32_bf16 v[36:39], a[152:155], a[68:71], v[36:39]\n"
"ds_read_b64_tr_b16 a[138:139], v210 offset:45056\n"
"v_mfma_f32_16x16x32_bf16 v[40:43], a[152:155], a[72:75], v[40:43]\n"
"buffer_load_dwordx4 a[104:107], v224, s[64:67], s81 offen offset:2048 sc1\n"
"v_mfma_f32_16x16x32_bf16 v[44:47], a[152:155], a[76:79], v[44:47]\n"
"s_waitcnt lgkmcnt(6)\n"
"v_mfma_f32_16x16x32_bf16 v[60:63], a[156:159], a[76:79], v[60:63]\n"
"ds_read_b64_tr_b16 a[140:141], v210 offset:40992\n"
"v_mfma_f32_16x16x32_bf16 v[56:59], a[156:159], a[72:75], v[56:59]\n"
"ds_read_b64_tr_b16 a[142:143], v210 offset:45088\n"
"v_mfma_f32_16x16x32_bf16 v[52:55], a[156:159], a[68:71], v[52:55]\n"
"buffer_load_dwordx4 a[108:111], v224, s[64:67], s81 offen offset:3072 sc1\n"
"v_mfma_f32_16x16x32_bf16 v[48:51], a[156:159], a[64:67], v[48:51]\n"
"s_add_u32 s81, s81, 0x1000\n"
"s_waitcnt lgkmcnt(6)\n"
"v_mfma_f32_16x16x32_bf16 v[64:67], a[64:67], a[128:131], v[64:67]\n"
"ds_read_b64_tr_b16 a[144:145], v210 offset:41024\n"
"v_mfma_f32_16x16x32_bf16 v[68:71], a[68:71], a[128:131], v[68:71]\n"
"ds_read_b64_tr_b16 a[146:147], v210 offset:45120\n"
"v_mfma_f32_16x16x32_bf16 v[72:75], a[72:75], a[128:131], v[72:75]\n"
"v_lshlrev_b32 v240, 7, v240\n"
"v_mfma_f32_16x16x32_bf16 v[76:79], a[76:79], a[128:131], v[76:79]\n"
"s_waitcnt lgkmcnt(6)\n"
"v_lshl_add_u32 v240, v242, 11, v240\n"
"v_mfma_f32_16x16x32_bf16 v[92:95], a[76:79], a[132:135], v[92:95]\n"
"ds_read_b64_tr_b16 a[148:149], v210 offset:41056\n"
"v_mfma_f32_16x16x32_bf16 v[88:91], a[72:75], a[132:135], v[88:91]\n"
"ds_read_b64_tr_b16 a[150:151], v210 offset:45152\n"
"s_add_u32 m0, s82, 0x15000\n"
"v_mfma_f32_16x16x32_bf16 v[84:87], a[68:71], a[132:135], v[84:87]\n"
"buffer_load_dwordx4 v217, s[56:59], s79 offen lds\n"
"v_mfma_f32_16x16x32_bf16 v[80:83], a[64:67], a[132:135], v[80:83]\n"
"s_waitcnt lgkmcnt(6)\n"
"v_mfma_f32_16x16x32_bf16 v[96:99], a[136:139], a[64:67], v[96:99]\n"
"ds_read_b64_tr_b16 a[152:153], v210 offset:41088\n"
"v_mfma_f32_16x16x32_bf16 v[100:103], a[136:139], a[68:71], v[100:103]\n"
"ds_read_b64_tr_b16 a[154:155], v210 offset:45184\n"
"v_mfma_f32_16x16x32_bf16 v[104:107], a[136:139], a[72:75], v[104:107]\n"
"v_min_u32 v241, 15, v236\n"
"v_mfma_f32_16x16x32_bf16 v[108:111], a[136:139], a[76:79], v[108:111]\n"
"s_waitcnt lgkmcnt(6)\n"
"v_lshrrev_b32 v242, 2, v241\n"
"v_mfma_f32_16x16x32_bf16 v[124:127], a[140:143], a[76:79], v[124:127]\n"
"ds_read_b64_tr_b16 a[156:157], v210 offset:41120\n"
"v_mfma_f32_16x16x32_bf16 v[120:123], a[140:143], a[72:75], v[120:123]\n"
"ds_read_b64_tr_b16 a[158:159], v210 offset:45216\n"
"s_add_u32 m0, s82, 0x15400\n"
"v_mfma_f32_16x16x32_bf16 v[116:119], a[140:143], a[68:71], v[116:119]\n"
"buffer_load_dwordx4 v218, s[56:59], s79 offen lds\n"
"v_mfma_f32_16x16x32_bf16 v[112:115], a[140:143], a[64:67], v[112:115]\n"
"s_waitcnt lgkmcnt(6)\n"
"v_mfma_f32_16x16x32_bf16 v[128:131], a[144:147], a[64:67], v[128:131]\n"
"ds_read_b64_tr_b16 a[128:129], v210 offset:41152\n"
"v_mfma_f32_16x16x32_bf16 v[132:135], a[144:147], a[68:71], v[132:135]\n"
"ds_read_b64_tr_b16 a[130:131], v210 offset:45248\n"
"v_mfma_f32_16x16x32_bf16 v[136:139], a[144:147], a[72:75], v[136:139]\n"
".Lhop1_%=:\n"
".Lpad1_%=:\n"
"s_cbranch_execz .Lpad5_%=\n"
"v_and_b32 v241, 3, v241\n"
"v_mfma_f32_16x16x32_bf16 v[140:143], a[144:147], a[76:79], v[140:143]\n"
"s_waitcnt lgkmcnt(6)\n"
"v_lshlrev_b32 v241, 8, v241\n"
"v_mfma_f32_16x16x32_bf16 v[156:159], a[148:151], a[76:79], v[156:159]\n"
"ds_read_b64_tr_b16 a[132:133], v208 offset:34816\n"
"v_mfma_f32_16x16x32_bf16 v[152:155], a[148:151], a[72:75], v[152:155]\n"
"ds_read_b64_tr_b16 a[134:135], v208 offset:37888\n"
"s_add_u32 m0, s82, 0x15800\n"
"v_mfma_f32_16x16x32_bf16 v[148:151], a[148:151], a[68:71], v[148:151]\n"
"buffer_load_dwordx4 v219, s[56:59], s79 offen lds\n"
"v_mfma_f32_16x16x32_bf16 v[144:147], a[148:151], a[64:67], v[144:147]\n"
"s_add_u32 s79, s79, 0x60000\n"
"s_waitcnt lgkmcnt(6)\n"
"v_mfma_f32_16x16x32_bf16 v[160:163], a[152:155], a[64:67], v[160:163]\n"
"ds_read_b64_tr_b16 a[136:137], v209 offset:34816\n"
"v_mfma_f32_16x16x32_bf16 v[164:167], a[152:155], a[68:71], v[164:167]\n"
"ds_read_b64_tr_b16 a[138:139], v209 offset:37888\n"
"v_mfma_f32_16x16x32_bf16 v[168:171], a[152:155], a[72:75], v[168:171]\n"
"v_lshl_add_u32 v241, v242, 11, v241\n"
"v_mfma_f32_16x16x32_bf16 v[172:175], a[152:155], a[76:79], v[172:175]\n"
"s_waitcnt lgkmcnt(6)\n"
"v_mov_b32 v243, 0x40000000\n"
"v_mfma_f32_16x16x32_bf16 v[188:191], a[156:159], a[76:79], v[188:191]\n"
"ds_read_b64_tr_b16 a[140:141], v208 offset:34880\n"
"v_mfma_f32_16x16x32_bf16 v[184:187], a[156:159], a[72:75], v[184:187]\n"
"ds_read_b64_tr_b16 a[142:143], v208 offset:37952\n"
"s_add_u32 m0, s83, 0x15000\n"
"v_mfma_f32_16x16x32_bf16 v[180:183], a[156:159], a[68:71], v[180:183]\n"
"buffer_load_dwordx4 v220, s[60:63], s80 offen lds\n"
"v_mfma_f32_16x16x32_bf16 v[176:179], a[156:159], a[64:67], v[176:179]\n"
"s_waitcnt lgkmcnt(6)\n"
"v_mfma_f32_16x16x32_bf16 v[192:195], a[128:131], a[64:67], v[192:195]\n"
"ds_read_b64_tr_b16 a[144:145], v209 offset:34880\n"
"v_mfma_f32_16x16x32_bf16 v[196:199], a[128:131], a[68:71], v[196:199]\n"
"ds_read_b64_tr_b16 a[146:147], v209 offset:37952\n"
"s_add_u32 m0, s83, 0x15400\n"
"v_mfma_f32_16x16x32_bf16 v[200:203], a[128:131], a[72:75], v[200:203]\n"
"buffer_load_dwordx4 v221, s[60:63], s80 offen lds\n"
"v_mfma_f32_16x16x32_bf16 v[204:207], a[128:131], a[76:79], v[204:207]\n"
"s_waitcnt vmcnt(9) lgkmcnt(6)\n"
"v_mfma_f32_16x16x32_bf16 v[0:3], a[132:135], a[80:83], v[0:3]\n"
"ds_read_b64_tr_b16 a[148:149], v208 offset:34944\n"
"v_mfma_f32_16x16x32_bf16 v[4:7], a[132:135], a[84:87], v[4:7]\n"
"ds_read_b64_tr_b16 a[150:151], v208 offset:38016\n"
"v_mfma_f32_16x16x32_bf16 v[8:11], a[132:135], a[88:91], v[8:11]\n"
"s_lshl_b32 s90, s99, 6\n"
"v_mfma_f32_16x16x32_bf16 v[12:15], a[132:135], a[92:95], v[12:15]\n"
"s_add_u32 s92, s90, 0x2002000\n"
"s_waitcnt lgkmcnt(6)\n"
"v_mfma_f32_16x16x32_bf16 v[28:31], a[136:139], a[92:95], v[28:31]\n"
"ds_read_b64_tr_b16 a[152:153], v209 offset:34944\n"
"v_mfma_f32_16x16x32_bf16 v[24:27], a[136:139], a[88:91], v[24:27]\n"
"ds_read_b64_tr_b16 a[154:155], v209 offset:38016\n"
"s_add_u32 m0, s83, 0x15800\n"
"v_mfma_f32_16x16x32_bf16 v[20:23], a[136:139], a[84:87], v[20:23]\n"
"buffer_load_dwordx4 v222, s[60:63], s80 offen lds\n"
"v_mfma_f32_16x16x32_bf16 v[16:19], a[136:139], a[80:83], v[16:19]\n"
"s_waitcnt lgkmcnt(6)\n"
"v_mfma_f32_16x16x32_bf16 v[32:35], a[140:143], a[80:83], v[32:35]\n"
"ds_read_b64_tr_b16 a[156:157], v210 offset:49152\n"
"v_mfma_f32_16x16x32_bf16 v[36:39], a[140:143], a[84:87], v[36:39]\n"
"ds_read_b64_tr_b16 a[158:159], v210 offset:53248\n"
"v_mfma_f32_16x16x32_bf16 v[40:43], a[140:143], a[88:91], v[40:43]\n"
"v_writelane_b32 v243, s92, 0\n"
"v_mfma_f32_16x16x32_bf16 v[44:47], a[140:143], a[92:95], v[44:47]\n"
"s_lshl_b32 s90, s86, 7\n"
"s_waitcnt lgkmcnt(6)\n"
"v_mfma_f32_16x16x32_bf16 v[60:63], a[144:147], a[92:95], v[60:63]\n"
"ds_read_b64_tr_b16 a[128:129], v210 offset:49184\n"
"v_mfma_f32_16x16x32_bf16 v[56:59], a[144:147], a[88:91], v[56:59]\n"
"ds_read_b64_tr_b16 a[130:131], v210 offset:53280\n"
"s_add_u32 m0, s83, 0x15c00\n"
"v_mfma_f32_16x16x32_bf16 v[52:55], a[144:147], a[84:87], v[52:55]\n"
"buffer_load_dwordx4 v223, s[60:63], s80 offen lds\n"
"v_mfma_f32_16x16x32_bf16 v[48:51], a[144:147], a[80:83], v[48:51]\n"
"s_add_u32 s80, s80, 0x70000\n"
"s_waitcnt lgkmcnt(6)\n"
"v_mfma_f32_16x16x32_bf16 v[64:67], a[80:83], a[148:151], v[64:67]\n"
"ds_read_b64_tr_b16 a[132:133], v210 offset:49216\n"
"v_mfma_f32_16x16x32_bf16 v[68:71], a[84:87], a[148:151], v[68:71]\n"
"ds_read_b64_tr_b16 a[134:135], v210 offset:53312\n"
"v_mfma_f32_16x16x32_bf16 v[72:75], a[88:91], a[148:151], v[72:75]\n"
"buffer_load_dwordx4 a[112:115], v224, s[64:67], s81 offen sc1\n"
"v_mfma_f32_16x16x32_bf16 v[76:79], a[92:95], a[148:151], v[76:79]\n"
"s_waitcnt lgkmcnt(6)\n"
"v_mfma_f32_16x16x32_bf16 v[92:95], a[92:95], a[152:155], v[92:95]\n"
"ds_read_b64_tr_b16 a[136:137], v210 offset:49248\n"
"v_mfma_f32_16x16x32_bf16 v[88:91], a[88:91], a[152:155], v[88:91]\n"
"ds_read_b64_tr_b16 a[138:139], v210 offset:53344\n"
"v_mfma_f32_16x16x32_bf16 v[84:87], a[84:87], a[152:155], v[84:87]\n"
"buffer_load_dwordx4 a[116:119], v224, s[64:67], s81 offen offset:1024 sc1\n"
"v_mfma_f32_16x16x32_bf16 v[80:83], a[80:83], a[152:155], v[80:83]\n"
"s_waitcnt lgkmcnt(6)\n"
"v_mfma_f32_16x16x32_bf16 v[96:99], a[156:159], a[80:83], v[96:99]\n"
"ds_read_b64_tr_b16 a[140:141], v210 offset:49280\n"
"v_mfma_f32_16x16x32_bf16 v[100:103], a[156:159], a[84:87], v[100:103]\n"
"ds_read_b64_tr_b16 a[142:143], v210 offset:53376\n"
"v_mfma_f32_16x16x32_bf16 v[104:107], a[156:159], a[88:91], v[104:107]\n"
"buffer_load_dwordx4 a[120:123], v224, s[64:67], s81 offen offset:2048 sc1\n"
"v_mfma_f32_16x16x32_bf16 v[108:111], a[156:159], a[92:95], v[108:111]\n"
"s_waitcnt lgkmcnt(6)\n"
"v_mfma_f32_16x16x32_bf16 v[124:127], a[128:131], a[92:95], v[124:127]\n"
"ds_read_b64_tr_b16 a[144:145], v210 offset:49312\n"
"v_mfma_f32_16x16x32_bf16 v[120:123], a[128:131], a[88:91], v[120:123]\n"
"ds_read_b64_tr_b16 a[146:147], v210 offset:53408\n"
"v_mfma_f32_16x16x32_bf16 v[116:119], a[128:131], a[84:87], v[116:119]\n"
"buffer_load_dwordx4 a[124:127], v224, s[64:67], s81 offen offset:3072 sc1\n"
"v_mfma_f32_16x16x32_bf16 v[112:115], a[128:131], a[80:83], v[112:115]\n"
"s_add_u32 s81, s81, 0x1000\n"
"s_waitcnt lgkmcnt(6)\n"
"v_mfma_f32_16x16x32_bf16 v[128:131], a[132:135], a[80:83], v[128:131]\n"
"ds_read_b64_tr_b16 a[148:149], v210 offset:49344\n"
"v_mfma_f32_16x16x32_bf16 v[132:135], a[132:135], a[84:87], v[132:135]\n"
"ds_read_b64_tr_b16 a[150:151], v210 offset:53440\n"
"v_mfma_f32_16x16x32_bf16 v[136:139], a[132:135], a[88:91], v[136:139]\n"
"buffer_load_dwordx4 a[0:3], v224, s[64:67], s81 offen sc1\n"
"v_mfma_f32_16x16x32_bf16 v[140:143], a[132:135], a[92:95], v[140:143]\n"
"s_barrier\n"
"s_waitcnt lgkmcnt(6)\n"
"v_mfma_f32_16x16x32_bf16 v[156:159], a[136:139], a[92:95], v[156:159]\n"
"ds_read_b64_tr_b16 a[152:153], v211\n"
"v_mfma_f32_16x16x32_bf16 v[152:155], a[136:139], a[88:91], v[152:155]\n"
"ds_read_b64_tr_b16 a[154:155], v211 offset:3072\n"
"v_mfma_f32_16x16x32_bf16 v[148:151], a[136:139], a[84:87], v[148:151]\n"
"buffer_load_dwordx4 a[4:7], v224, s[64:67], s81 offen offset:1024 sc1\n"
"v_mfma_f32_16x16x32_bf16 v[144:147], a[136:139], a[80:83], v[144:147]\n"
"s_waitcnt lgkmcnt(6)\n"
"v_mfma_f32_16x16x32_bf16 v[160:163], a[140:143], a[80:83], v[160:163]\n"
"ds_read_b64_tr_b16 a[156:157], v212\n"
"v_mfma_f32_16x16x32_bf16 v[164:167], a[140:143], a[84:87], v[164:167]\n"
"ds_read_b64_tr_b16 a[158:159], v212 offset:3072\n"
"v_mfma_f32_16x16x32_bf16 v[168:171], a[140:143], a[88:91], v[168:171]\n"
"buffer_load_dwordx4 a[8:11], v224, s[64:67], s81 offen offset:2048 sc1\n"
"v_mfma_f32_16x16x32_bf16 v[172:175], a[140:143], a[92:95], v[172:175]\n"
"s_waitcnt lgkmcnt(6)\n"
"v_mfma_f32_16x16x32_bf16 v[188:191], a[144:147], a[92:95], v[188:191]\n"
"ds_read_b64_tr_b16 a[128:129], v211 offset:64\n"
"v_mfma_f32_16x16x32_bf16 v[184:187], a[144:147], a[88:91], v[184:187]\n"
"ds_read_b64_tr_b16 a[130:131], v211 offset:3136\n"
"v_mfma_f32_16x16x32_bf16 v[180:183], a[144:147], a[84:87], v[180:183]\n"
"s_add_u32 s92, s90, 0x2100000\n"
"v_mfma_f32_16x16x32_bf16 v[176:179], a[144:147], a[80:83], v[176:179]\n"
"s_waitcnt lgkmcnt(6)\n"
"v_writelane_b32 v243, s92, 1\n"
"v_mfma_f32_16x16x32_bf16 v[192:195], a[148:151], a[80:83], v[192:195]\n"
"ds_read_b64_tr_b16 a[132:133], v212 offset:64\n"
"v_mfma_f32_16x16x32_bf16 v[196:199], a[148:151], a[84:87], v[196:199]\n"
"ds_read_b64_tr_b16 a[134:135], v212 offset:3136\n"
"v_mfma_f32_16x16x32_bf16 v[200:203], a[148:151], a[88:91], v[200:203]\n"
"buffer_load_dwordx4 a[12:15], v224, s[64:67], s81 offen offset:3072 sc1\n"
"v_mfma_f32_16x16x32_bf16 v[204:207], a[148:151], a[92:95], v[204:207]\n"
"s_add_u32 s81, s81, 0x1000\n"
"s_waitcnt vmcnt(15) lgkmcnt(6)\n"
"v_mfma_f32_16x16x32_bf16 v[0:3], a[152:155], a[96:99], v[0:3]\n"
"ds_read_b64_tr_b16 a[136:137], v211 offset:128\n"
"v_mfma_f32_16x16x32_bf16 v[4:7], a[152:155], a[100:103], v[4:7]\n"
"ds_read_b64_tr_b16 a[138:139], v211 offset:3200\n"
"v_mfma_f32_16x16x32_bf16 v[8:11], a[152:155], a[104:107], v[8:11]\n"
"s_lshl_b32 s90, s76, 2\n"
"v_mfma_f32_16x16x32_bf16 v[12:15], a[152:155], a[108:111], v[12:15]\n"
"s_add_u32 s90, s90, s78\n"
"s_waitcnt lgkmcnt(6)\n"
"v_mfma_f32_16x16x32_bf16 v[28:31], a[156:159], a[108:111], v[28:31]\n"
"ds_read_b64_tr_b16 a[140:141], v212 offset:128\n"
"v_mfma_f32_16x16x32_bf16 v[24:27], a[156:159], a[104:107], v[24:27]\n"
"ds_read_b64_tr_b16 a[142:143], v212 offset:3200\n"
"s_add_u32 m0, s82, 0x1c000\n"
"v_mfma_f32_16x16x32_bf16 v[20:23], a[156:159], a[100:103], v[20:23]\n"
"buffer_load_dwordx4 v217, s[56:59], s79 offen lds\n"
"v_mfma_f32_16x16x32_bf16 v[16:19], a[156:159], a[96:99], v[16:19]\n"
"s_waitcnt lgkmcnt(6)\n"
"v_mfma_f32_16x16x32_bf16 v[32:35], a[128:131], a[96:99], v[32:35]\n"
"ds_read_b64_tr_b16 a[144:145], v213 offset:12288\n"
"v_mfma_f32_16x16x32_bf16 v[36:39], a[128:131], a[100:103], v[36:39]\n"
"ds_read_b64_tr_b16 a[146:147], v213 offset:16384\n"
"v_mfma_f32_16x16x32_bf16 v[40:43], a[128:131], a[104:107], v[40:43]\n"
"s_lshl_b32 s90, s90, 5\n"
"v_mfma_f32_16x16x32_bf16 v[44:47], a[128:131], a[108:111], v[44:47]\n"
"s_add_u32 s90, s90, s77\n"
"s_waitcnt lgkmcnt(6)\n"
"v_mfma_f32_16x16x32_bf16 v[60:63], a[132:135], a[108:111], v[60:63]\n"
"ds_read_b64_tr_b16 a[148:149], v213 offset:12320\n"
"v_mfma_f32_16x16x32_bf16 v[56:59], a[132:135], a[104:107], v[56:59]\n"
"ds_read_b64_tr_b16 a[150:151], v213 offset:16416\n"
"s_add_u32 m0, s82, 0x1c400\n"
"v_mfma_f32_16x16x32_bf16 v[52:55], a[132:135], a[100:103], v[52:55]\n"
"buffer_load_dwordx4 v218, s[56:59], s79 offen lds\n"
"v_mfma_f32_16x16x32_bf16 v[48:51], a[132:135], a[96:99], v[48:51]\n"
"s_waitcnt lgkmcnt(6)\n"
"v_mfma_f32_16x16x32_bf16 v[64:67], a[96:99], a[136:139], v[64:67]\n"
"ds_read_b64_tr_b16 a[152:153], v213 offset:12352\n"
"v_mfma_f32_16x16x32_bf16 v[68:71], a[100:103], a[136:139], v[68:71]\n"
"ds_read_b64_tr_b16 a[154:155], v213 offset:16448\n"
"v_mfma_f32_16x16x32_bf16 v[72:75], a[104:107], a[136:139], v[72:75]\n"
"s_lshl_b32 s90, s90, 7\n"
"v_mfma_f32_16x16x32_bf16 v[76:79], a[108:111], a[136:139], v[76:79]\n"
"s_add_u32 s92, s90, 0x20a0000\n"
"s_waitcnt lgkmcnt(6)\n"
"v_mfma_f32_16x16x32_bf16 v[92:95], a[108:111], a[140:143], v[92:95]\n"
"ds_read_b64_tr_b16 a[156:157], v213 offset:12384\n"
"v_mfma_f32_16x16x32_bf16 v[88:91], a[104:107], a[140:143], v[88:91]\n"
"ds_read_b64_tr_b16 a[158:159], v213 offset:16480\n"
"s_add_u32 m0, s82, 0x1c800\n"
"v_mfma_f32_16x16x32_bf16 v[84:87], a[100:103], a[140:143], v[84:87]\n"
"buffer_load_dwordx4 v219, s[56:59], s79 offen lds\n"
"v_mfma_f32_16x16x32_bf16 v[80:83], a[96:99], a[140:143], v[80:83]\n"
"s_add_u32 s79, s79, 0x60000\n"
"s_waitcnt lgkmcnt(6)\n"
"v_mfma_f32_16x16x32_bf16 v[96:99], a[144:147], a[96:99], v[96:99]\n"
"ds_read_b64_tr_b16 a[128:129], v213 offset:12416\n"
"v_mfma_f32_16x16x32_bf16 v[100:103], a[144:147], a[100:103], v[100:103]\n"
"ds_read_b64_tr_b16 a[130:131], v213 offset:16512\n"
"v_mfma_f32_16x16x32_bf16 v[104:107], a[144:147], a[104:107], v[104:107]\n"
"v_writelane_b32 v243, s92, 2\n"
"v_mfma_f32_16x16x32_bf16 v[108:111], a[144:147], a[108:111], v[108:111]\n"
"s_add_u32 s92, s90, 0x20c0000\n"
"s_waitcnt lgkmcnt(6)\n"
"v_mfma_f32_16x16x32_bf16 v[124:127], a[148:151], a[108:111], v[124:127]\n"
"ds_read_b64_tr_b16 a[132:133], v213 offset:12448\n"
"v_mfma_f32_16x16x32_bf16 v[120:123], a[148:151], a[104:107], v[120:123]\n"
"ds_read_b64_tr_b16 a[134:135], v213 offset:16544\n"
"v_mfma_f32_16x16x32_bf16 v[116:119], a[148:151], a[100:103], v[116:119]\n"
"buffer_load_dword a164, v249, s[56:59], s29 offen\n"
"v_mfma_f32_16x16x32_bf16 v[112:115], a[148:151], a[96:99], v[112:115]\n"
"s_add_u32 s29, s29, 0x180000\n"
"s_waitcnt lgkmcnt(6)\n"
"v_mfma_f32_16x16x32_bf16 v[128:131], a[152:155], a[96:99], v[128:131]\n"
"ds_read_b64_tr_b16 a[136:137], v213 offset:12480\n"
"v_mfma_f32_16x16x32_bf16 v[132:135], a[152:155], a[100:103], v[132:135]\n"
"ds_read_b64_tr_b16 a[138:139], v213 offset:16576\n"
"v_mfma_f32_16x16x32_bf16 v[136:139], a[152:155], a[104:107], v[136:139]\n"
"buffer_load_dword a165, v253, s[60:63], s30 offen\n"
"v_mfma_f32_16x16x32_bf16 v[140:143], a[152:155], a[108:111], v[140:143]\n"
"s_add_u32 s30, s30, 0x1c0000\n"
"s_waitcnt lgkmcnt(6)\n"
"v_mfma_f32_16x16x32_bf16 v[156:159], a[156:159], a[108:111], v[156:159]\n"
"ds_read_b64_tr_b16 a[140:141], v211 offset:6144\n"
"v_mfma_f32_16x16x32_bf16 v[152:155], a[156:159], a[104:107], v[152:155]\n"
"ds_read_b64_tr_b16 a[142:143], v211 offset:9216\n"
"v_mfma_f32_16x16x32_bf16 v[148:151], a[156:159], a[100:103], v[148:151]\n"
"v_writelane_b32 v243, s92, 3\n"
"v_mfma_f32_16x16x32_bf16 v[144:147], a[156:159], a[96:99], v[144:147]\n"
"s_waitcnt lgkmcnt(6)\n"
"v_mfma_f32_16x16x32_bf16 v[160:163], a[128:131], a[96:99], v[160:163]\n"
"ds_read_b64_tr_b16 a[144:145], v212 offset:6144\n"
"v_mfma_f32_16x16x32_bf16 v[164:167], a[128:131], a[100:103], v[164:167]\n"
"ds_read_b64_tr_b16 a[146:147], v212 offset:9216\n"
"v_mfma_f32_16x16x32_bf16 v[168:171], a[128:131], a[104:107], v[168:171]\n"
"v_mfma_f32_16x16x32_bf16 v[172:175], a[128:131], a[108:111], v[172:175]\n"
"s_waitcnt lgkmcnt(6)\n"
"v_mfma_f32_16x16x32_bf16 v[188:191], a[132:135], a[108:111], v[188:191]\n"
"ds_read_b64_tr_b16 a[148:149], v211 offset:6208\n"
"v_mfma_f32_16x16x32_bf16 v[184:187], a[132:135], a[104:107], v[184:187]\n"
"ds_read_b64_tr_b16 a[150:151], v211 offset:9280\n"
"v_mfma_f32_16x16x32_bf16 v[180:183], a[132:135], a[100:103], v[180:183]\n"
"v_mfma_f32_16x16x32_bf16 v[176:179], a[132:135], a[96:99], v[176:179]\n"
"s_waitcnt lgkmcnt(6)\n"
"v_mfma_f32_16x16x32_bf16 v[192:195], a[136:139], a[96:99], v[192:195]\n"
"ds_read_b64_tr_b16 a[152:153], v212 offset:6208\n"
"v_mfma_f32_16x16x32_bf16 v[196:199], a[136:139], a[100:103], v[196:199]\n"
"ds_read_b64_tr_b16 a[154:155], v212 offset:9280\n"
"v_mfma_f32_16x16x32_bf16 v[200:203], a[136:139], a[104:107], v[200:203]\n"
"v_mfma_f32_16x16x32_bf16 v[204:207], a[136:139], a[108:111], v[204:207]\n"
"s_waitcnt vmcnt(9) lgkmcnt(6)\n"
"v_mfma_f32_16x16x32_bf16 v[0:3], a[140:143], a[112:115], v[0:3]\n"
"ds_read_b64_tr_b16 a[156:157], v211 offset:6272\n"
"v_mfma_f32_16x16x32_bf16 v[4:7], a[140:143], a[116:119], v[4:7]\n"
"ds_read_b64_tr_b16 a[158:159], v211 offset:9344\n"
"v_mfma_f32_16x16x32_bf16 v[8:11], a[140:143], a[120:123], v[8:11]\n"
"v_mfma_f32_16x16x32_bf16 v[12:15], a[140:143], a[124:127], v[12:15]\n"
"s_waitcnt lgkmcnt(6)\n"
"v_mfma_f32_16x16x32_bf16 v[28:31], a[144:147], a[124:127], v[28:31]\n"
"ds_read_b64_tr_b16 a[128:129], v212 offset:6272\n"
"v_mfma_f32_16x16x32_bf16 v[24:27], a[144:147], a[120:123], v[24:27]\n"
"ds_read_b64_tr_b16 a[130:131], v212 offset:9344\n"
"v_mfma_f32_16x16x32_bf16 v[20:23], a[144:147], a[116:119], v[20:23]\n"
"v_mfma_f32_16x16x32_bf16 v[16:19], a[144:147], a[112:115], v[16:19]\n"
"s_waitcnt lgkmcnt(6)\n"
"v_mfma_f32_16x16x32_bf16 v[32:35], a[148:151], a[112:115], v[32:35]\n"
"ds_read_b64_tr_b16 a[132:133], v213 offset:20480\n"
"v_mfma_f32_16x16x32_bf16 v[36:39], a[148:151], a[116:119], v[36:39]\n"
"ds_read_b64_tr_b16 a[134:135], v213 offset:24576\n"
"s_add_u32 m0, s83, 0x1c000\n"
"v_mfma_f32_16x16x32_bf16 v[40:43], a[148:151], a[120:123], v[40:43]\n"
"buffer_load_dwordx4 v220, s[60:63], s80 offen lds\n"
"v_mfma_f32_16x16x32_bf16 v[44:47], a[148:151], a[124:127], v[44:47]\n"
"s_waitcnt lgkmcnt(6)\n"
"v_mfma_f32_16x16x32_bf16 v[60:63], a[152:155], a[124:127], v[60:63]\n"
"ds_read_b64_tr_b16 a[136:137], v213 offset:20512\n"
"v_mfma_f32_16x16x32_bf16 v[56:59], a[152:155], a[120:123], v[56:59]\n"
"ds_read_b64_tr_b16 a[138:139], v213 offset:24608\n"
"s_add_u32 m0, s83, 0x1c400\n"
"v_mfma_f32_16x16x32_bf16 v[52:55], a[152:155], a[116:119], v[52:55]\n"
"buffer_load_dwordx4 v221, s[60:63], s80 offen lds\n"
"v_mfma_f32_16x16x32_bf16 v[48:51], a[152:155], a[112:115], v[48:51]\n"
"s_waitcnt lgkmcnt(6)\n"
"v_mfma_f32_16x16x32_bf16 v[64:67], a[112:115], a[156:159], v[64:67]\n"
"ds_read_b64_tr_b16 a[140:141], v213 offset:20544\n"
"v_mfma_f32_16x16x32_bf16 v[68:71], a[116:119], a[156:159], v[68:71]\n"
"ds_read_b64_tr_b16 a[142:143], v213 offset:24640\n"
"s_add_u32 m0, s83, 0x1c800\n"
"v_mfma_f32_16x16x32_bf16 v[72:75], a[120:123], a[156:159], v[72:75]\n"
"buffer_load_dwordx4 v222, s[60:63], s80 offen lds\n"
"v_mfma_f32_16x16x32_bf16 v[76:79], a[124:127], a[156:159], v[76:79]\n"
"s_waitcnt lgkmcnt(6)\n"
"v_mfma_f32_16x16x32_bf16 v[92:95], a[124:127], a[128:131], v[92:95]\n"
"ds_read_b64_tr_b16 a[144:145], v213 offset:20576\n"
"v_mfma_f32_16x16x32_bf16 v[88:91], a[120:123], a[128:131], v[88:91]\n"
"ds_read_b64_tr_b16 a[146:147], v213 offset:24672\n"
"s_add_u32 m0, s83, 0x1cc00\n"
"v_mfma_f32_16x16x32_bf16 v[84:87], a[116:119], a[128:131], v[84:87]\n"
"buffer_load_dwordx4 v223, s[60:63], s80 offen lds\n"
"v_mfma_f32_16x16x32_bf16 v[80:83], a[112:115], a[128:131], v[80:83]\n"
"s_add_u32 s80, s80, 0x70000\n"
"s_waitcnt lgkmcnt(6)\n"
"v_mfma_f32_16x16x32_bf16 v[96:99], a[132:135], a[112:115], v[96:99]\n"
"ds_read_b64_tr_b16 a[148:149], v213 offset:20608\n"
"v_mfma_f32_16x16x32_bf16 v[100:103], a[132:135], a[116:119], v[100:103]\n"
"ds_read_b64_tr_b16 a[150:151], v213 offset:24704\n"
"v_mfma_f32_16x16x32_bf16 v[104:107], a[132:135], a[120:123], v[104:107]\n"
"buffer_load_dwordx4 a[16:19], v224, s[64:67], s81 offen sc1\n"
"v_mfma_f32_16x16x32_bf16 v[108:111], a[132:135], a[124:127], v[108:111]\n"
"s_waitcnt lgkmcnt(6)\n"
"v_mfma_f32_16x16x32_bf16 v[124:127], a[136:139], a[124:127], v[124:127]\n"
"ds_read_b64_tr_b16 a[152:153], v213 offset:20640\n"
"v_mfma_f32_16x16x32_bf16 v[120:123], a[136:139], a[120:123], v[120:123]\n"
"ds_read_b64_tr_b16 a[154:155], v213 offset:24736\n"
"v_mfma_f32_16x16x32_bf16 v[116:119], a[136:139], a[116:119], v[116:119]\n"
"v_mfma_f32_16x16x32_bf16 v[112:115], a[136:139], a[112:115], v[112:115]\n"
"s_waitcnt lgkmcnt(6)\n"
"v_mfma_f32_16x16x32_bf16 v[128:131], a[140:143], a[112:115], v[128:131]\n"
"ds_read_b64_tr_b16 a[156:157], v213 offset:20672\n"
"v_mfma_f32_16x16x32_bf16 v[132:135], a[140:143], a[116:119], v[132:135]\n"
"ds_read_b64_tr_b16 a[158:159], v213 offset:24768\n"
"v_mfma_f32_16x16x32_bf16 v[136:139], a[140:143], a[120:123], v[136:139]\n"
"buffer_load_dwordx4 a[20:23], v224, s[64:67], s81 offen offset:1024 sc1\n"
"v_mfma_f32_16x16x32_bf16 v[140:143], a[140:143], a[124:127], v[140:143]\n"
"s_barrier\n"
"s_waitcnt lgkmcnt(6)\n"
"v_mfma_f32_16x16x32_bf16 v[156:159], a[144:147], a[124:127], v[156:159]\n"
"ds_read_b64_tr_b16 a[128:129], v211 offset:28672\n"
"v_mfma_f32_16x16x32_bf16 v[152:155], a[144:147], a[120:123], v[152:155]\n"
"ds_read_b64_tr_b16 a[130:131], v211 offset:31744\n"
"v_mfma_f32_16x16x32_bf16 v[148:151], a[144:147], a[116:119], v[148:151]\n"
"buffer_load_dwordx4 a[24:27], v224, s[64:67], s81 offen offset:2048 sc1\n"
"v_mfma_f32_16x16x32_bf16 v[144:147], a[144:147], a[112:115], v[144:147]\n"
"s_waitcnt lgkmcnt(6)\n"
"v_mfma_f32_16x16x32_bf16 v[160:163], a[148:151], a[112:115], v[160:163]\n"
"ds_read_b64_tr_b16 a[132:133], v212 offset:28672\n"
"v_mfma_f32_16x16x32_bf16 v[164:167], a[148:151], a[116:119], v[164:167]\n"
"ds_read_b64_tr_b16 a[134:135], v212 offset:31744\n"
"v_mfma_f32_16x16x32_bf16 v[168:171], a[148:151], a[120:123], v[168:171]\n"
"buffer_load_dwordx4 a[28:31], v224, s[64:67], s81 offen offset:3072 sc1\n"
"v_mfma_f32_16x16x32_bf16 v[172:175], a[148:151], a[124:127], v[172:175]\n"
"s_add_u32 s81, s81, 0x1000\n"
"s_waitcnt lgkmcnt(6)\n"
"v_mfma_f32_16x16x32_bf16 v[188:191], a[152:155], a[124:127], v[188:191]\n"
"ds_read_b64_tr_b16 a[136:137], v211 offset:28736\n"
"v_mfma_f32_16x16x32_bf16 v[184:187], a[152:155], a[120:123], v[184:187]\n"
"ds_read_b64_tr_b16 a[138:139], v211 offset:31808\n"
"v_mfma_f32_16x16x32_bf16 v[180:183], a[152:155], a[116:119], v[180:183]\n"
"buffer_load_dwordx4 a[32:35], v224, s[64:67], s81 offen sc1\n"
"v_mfma_f32_16x16x32_bf16 v[176:179], a[152:155], a[112:115], v[176:179]\n"
"s_waitcnt lgkmcnt(6)\n"
"v_mfma_f32_16x16x32_bf16 v[192:195], a[156:159], a[112:115], v[192:195]\n"
"ds_read_b64_tr_b16 a[140:141], v212 offset:28736\n"
"v_mfma_f32_16x16x32_bf16 v[196:199], a[156:159], a[116:119], v[196:199]\n"
"ds_read_b64_tr_b16 a[142:143], v212 offset:31808\n"
"v_mfma_f32_16x16x32_bf16 v[200:203], a[156:159], a[120:123], v[200:203]\n"
"buffer_load_dwordx4 a[36:39], v224, s[64:67], s81 offen offset:1024 sc1\n"
"v_mfma_f32_16x16x32_bf16 v[204:207], a[156:159], a[124:127], v[204:207]\n"
"s_waitcnt vmcnt(15) lgkmcnt(6)\n"
"v_mfma_f32_16x16x32_bf16 v[0:3], a[128:131], a[0:3], v[0:3]\n"
"ds_read_b64_tr_b16 a[144:145], v211 offset:28800\n"
"v_mfma_f32_16x16x32_bf16 v[4:7], a[128:131], a[4:7], v[4:7]\n"
"ds_read_b64_tr_b16 a[146:147], v211 offset:31872\n"
"v_mfma_f32_16x16x32_bf16 v[8:11], a[128:131], a[8:11], v[8:11]\n"
"buffer_load_dwordx4 a[40:43], v224, s[64:67], s81 offen offset:2048 sc1\n"
"v_mfma_f32_16x16x32_bf16 v[12:15], a[128:131], a[12:15], v[12:15]\n"
"s_waitcnt lgkmcnt(6)\n"
"v_mfma_f32_16x16x32_bf16 v[28:31], a[132:135], a[12:15], v[28:31]\n"
"ds_read_b64_tr_b16 a[148:149], v212 offset:28800\n"
"v_mfma_f32_16x16x32_bf16 v[24:27], a[132:135], a[8:11], v[24:27]\n"
"ds_read_b64_tr_b16 a[150:151], v212 offset:31872\n"
"v_mfma_f32_16x16x32_bf16 v[20:23], a[132:135], a[4:7], v[20:23]\n"
"buffer_load_dwordx4 a[44:47], v224, s[64:67], s81 offen offset:3072 sc1\n"
"v_mfma_f32_16x16x32_bf16 v[16:19], a[132:135], a[0:3], v[16:19]\n"
"s_add_u32 s81, s81, 0x1000\n"
"s_waitcnt lgkmcnt(6)\n"
"v_mfma_f32_16x16x32_bf16 v[32:35], a[136:139], a[0:3], v[32:35]\n"
"ds_read_b64_tr_b16 a[152:153], v213 offset:40960\n"
"v_mfma_f32_16x16x32_bf16 v[36:39], a[136:139], a[4:7], v[36:39]\n"
"ds_read_b64_tr_b16 a[154:155], v213 offset:45056\n"
"v_mfma_f32_16x16x32_bf16 v[40:43], a[136:139], a[8:11], v[40:43]\n"
"v_mfma_f32_16x16x32_bf16 v[44:47], a[136:139], a[12:15], v[44:47]\n"
"s_waitcnt lgkmcnt(6)\n"
"v_mfma_f32_16x16x32_bf16 v[60:63], a[140:143], a[12:15], v[60:63]\n"
"ds_read_b64_tr_b16 a[156:157], v213 offset:40992\n"
"v_mfma_f32_16x16x32_bf16 v[56:59], a[140:143], a[8:11], v[56:59]\n"
"ds_read_b64_tr_b16 a[158:159], v213 offset:45088\n"
"v_mfma_f32_16x16x32_bf16 v[52:55], a[140:143], a[4:7], v[52:55]\n"
"v_mfma_f32_16x16x32_bf16 v[48:51], a[140:143], a[0:3], v[48:51]\n"
"s_waitcnt lgkmcnt(6)\n"
"v_mfma_f32_16x16x32_bf16 v[64:67], a[0:3], a[144:147], v[64:67]\n"
"ds_read_b64_tr_b16 a[128:129], v213 offset:41024\n"
"v_mfma_f32_16x16x32_bf16 v[68:71], a[4:7], a[144:147], v[68:71]\n"
"ds_read_b64_tr_b16 a[130:131], v213 offset:45120\n"
"s_add_u32 m0, s82, 0\n"
"v_mfma_f32_16x16x32_bf16 v[72:75], a[8:11], a[144:147], v[72:75]\n"
"buffer_load_dwordx4 v217, s[56:59], s79 offen lds\n"
"v_mfma_f32_16x16x32_bf16 v[76:79], a[12:15], a[144:147], v[76:79]\n"
"s_waitcnt lgkmcnt(6)\n"
"v_mfma_f32_16x16x32_bf16 v[92:95], a[12:15], a[148:151], v[92:95]\n"
"ds_read_b64_tr_b16 a[132:133], v213 offset:41056\n"
"v_mfma_f32_16x16x32_bf16 v[88:91], a[8:11], a[148:151], v[88:91]\n"
"ds_read_b64_tr_b16 a[134:135], v213 offset:45152\n"
"v_mfma_f32_16x16x32_bf16 v[84:87], a[4:7], a[148:151], v[84:87]\n"
"v_mfma_f32_16x16x32_bf16 v[80:83], a[0:3], a[148:151], v[80:83]\n"
"s_waitcnt lgkmcnt(6)\n"
"v_mfma_f32_16x16x32_bf16 v[96:99], a[152:155], a[0:3], v[96:99]\n"
"ds_read_b64_tr_b16 a[136:137], v213 offset:41088\n"
"v_mfma_f32_16x16x32_bf16 v[100:103], a[152:155], a[4:7], v[100:103]\n"
"ds_read_b64_tr_b16 a[138:139], v213 offset:45184\n"
"s_add_u32 m0, s82, 0x400\n"
"v_mfma_f32_16x16x32_bf16 v[104:107], a[152:155], a[8:11], v[104:107]\n"
"buffer_load_dwordx4 v218, s[56:59], s79 offen lds\n"
"v_mfma_f32_16x16x32_bf16 v[108:111], a[152:155], a[12:15], v[108:111]\n"
"s_waitcnt lgkmcnt(6)\n"
"v_mfma_f32_16x16x32_bf16 v[124:127], a[156:159], a[12:15], v[124:127]\n"
"ds_read_b64_tr_b16 a[140:141], v213 offset:41120\n"
"v_mfma_f32_16x16x32_bf16 v[120:123], a[156:159], a[8:11], v[120:123]\n"
"ds_read_b64_tr_b16 a[142:143], v213 offset:45216\n"
"v_mfma_f32_16x16x32_bf16 v[116:119], a[156:159], a[4:7], v[116:119]\n"
"v_mfma_f32_16x16x32_bf16 v[112:115], a[156:159], a[0:3], v[112:115]\n"
"s_waitcnt lgkmcnt(6)\n"
"v_mfma_f32_16x16x32_bf16 v[128:131], a[128:131], a[0:3], v[128:131]\n"
"ds_read_b64_tr_b16 a[144:145], v213 offset:41152\n"
"v_mfma_f32_16x16x32_bf16 v[132:135], a[128:131], a[4:7], v[132:135]\n"
"ds_read_b64_tr_b16 a[146:147], v213 offset:45248\n"
"s_add_u32 m0, s82, 0x800\n"
"v_mfma_f32_16x16x32_bf16 v[136:139], a[128:131], a[8:11], v[136:139]\n"
"buffer_load_dwordx4 v219, s[56:59], s79 offen lds\n"
"v_mfma_f32_16x16x32_bf16 v[140:143], a[128:131], a[12:15], v[140:143]\n"
"s_add_u32 s79, s79, 0x60000\n"
"s_waitcnt lgkmcnt(6)\n"
"v_mfma_f32_16x16x32_bf16 v[156:159], a[132:135], a[12:15], v[156:159]\n"
"ds_read_b64_tr_b16 a[148:149], v211 offset:34816\n"
"v_mfma_f32_16x16x32_bf16 v[152:155], a[132:135], a[8:11], v[152:155]\n"
"ds_read_b64_tr_b16 a[150:151], v211 offset:37888\n"
"v_mfma_f32_16x16x32_bf16 v[148:151], a[132:135], a[4:7], v[148:151]\n"
"v_mfma_f32_16x16x32_bf16 v[144:147], a[132:135], a[0:3], v[144:147]\n"
"s_waitcnt lgkmcnt(6)\n"
"v_mfma_f32_16x16x32_bf16 v[160:163], a[136:139], a[0:3], v[160:163]\n"
"ds_read_b64_tr_b16 a[152:153], v212 offset:34816\n"
"v_mfma_f32_16x16x32_bf16 v[164:167], a[136:139], a[4:7], v[164:167]\n"
"ds_read_b64_tr_b16 a[154:155], v212 offset:37888\n"
"s_add_u32 m0, s83, 0\n"
"v_mfma_f32_16x16x32_bf16 v[168:171], a[136:139], a[8:11], v[168:171]\n"
"buffer_load_dwordx4 v220, s[60:63], s80 offen lds\n"
"v_mfma_f32_16x16x32_bf16 v[172:175], a[136:139], a[12:15], v[172:175]\n"
"s_waitcnt lgkmcnt(6)\n"
"v_mfma_f32_16x16x32_bf16 v[188:191], a[140:143], a[12:15], v[188:191]\n"
"ds_read_b64_tr_b16 a[156:157], v211 offset:34880\n"
"v_mfma_f32_16x16x32_bf16 v[184:187], a[140:143], a[8:11], v[184:187]\n"
"ds_read_b64_tr_b16 a[158:159], v211 offset:37952\n"
"s_add_u32 m0, s83, 0x400\n"
"v_mfma_f32_16x16x32_bf16 v[180:183], a[140:143], a[4:7], v[180:183]\n"
"buffer_load_dwordx4 v221, s[60:63], s80 offen lds\n"
"v_mfma_f32_16x16x32_bf16 v[176:179], a[140:143], a[0:3], v[176:179]\n"
"s_waitcnt lgkmcnt(6)\n"
"v_mfma_f32_16x16x32_bf16 v[192:195], a[144:147], a[0:3], v[192:195]\n"
"ds_read_b64_tr_b16 a[128:129], v212 offset:34880\n"
"v_mfma_f32_16x16x32_bf16 v[196:199], a[144:147], a[4:7], v[196:199]\n"
"ds_read_b64_tr_b16 a[130:131], v212 offset:37952\n"
"v_mfma_f32_16x16x32_bf16 v[200:203], a[144:147], a[8:11], v[200:203]\n"
"v_mfma_f32_16x16x32_bf16 v[204:207], a[144:147], a[12:15], v[204:207]\n"
"s_waitcnt vmcnt(9) lgkmcnt(6)\n"
"v_mfma_f32_16x16x32_bf16 v[0:3], a[148:151], a[16:19], v[0:3]\n"
"ds_read_b64_tr_b16 a[132:133], v211 offset:34944\n"
"v_mfma_f32_16x16x32_bf16 v[4:7], a[148:151], a[20:23], v[4:7]\n"
"ds_read_b64_tr_b16 a[134:135], v211 offset:38016\n"
"s_add_u32 m0, s83, 0x800\n"
"v_mfma_f32_16x16x32_bf16 v[8:11], a[148:151], a[24:27], v[8:11]\n"
"buffer_load_dwordx4 v222, s[60:63], s80 offen lds\n"
"v_mfma_f32_16x16x32_bf16 v[12:15], a[148:151], a[28:31], v[12:15]\n"
"s_waitcnt lgkmcnt(6)\n"
"v_mfma_f32_16x16x32_bf16 v[28:31], a[152:155], a[28:31], v[28:31]\n"
"ds_read_b64_tr_b16 a[136:137], v212 offset:34944\n"
"v_mfma_f32_16x16x32_bf16 v[24:27], a[152:155], a[24:27], v[24:27]\n"
"ds_read_b64_tr_b16 a[138:139], v212 offset:38016\n"
"s_add_u32 m0, s83, 0xc00\n"
"v_mfma_f32_16x16x32_bf16 v[20:23], a[152:155], a[20:23], v[20:23]\n"
"buffer_load_dwordx4 v223, s[60:63], s80 offen lds\n"
"v_mfma_f32_16x16x32_bf16 v[16:19], a[152:155], a[16:19], v[16:19]\n"
"s_add_u32 s80, s80, 0x70000\n"
"s_waitcnt lgkmcnt(6)\n"
"v_mfma_f32_16x16x32_bf16 v[32:35], a[156:159], a[16:19], v[32:35]\n"
"ds_read_b64_tr_b16 a[140:141], v213 offset:49152\n"
"v_mfma_f32_16x16x32_bf16 v[36:39], a[156:159], a[20:23], v[36:39]\n"
"ds_read_b64_tr_b16 a[142:143], v213 offset:53248\n"
"v_mfma_f32_16x16x32_bf16 v[40:43], a[156:159], a[24:27], v[40:43]\n"
"buffer_load_dwordx4 a[48:51], v224, s[64:67], s81 offen sc1\n"
"v_mfma_f32_16x16x32_bf16 v[44:47], a[156:159], a[28:31], v[44:47]\n"
"s_waitcnt lgkmcnt(6)\n"
"v_mfma_f32_16x16x32_bf16 v[60:63], a[128:131], a[28:31], v[60:63]\n"
"ds_read_b64_tr_b16 a[144:145], v213 offset:49184\n"
"v_mfma_f32_16x16x32_bf16 v[56:59], a[128:131], a[24:27], v[56:59]\n"
"ds_read_b64_tr_b16 a[146:147], v213 offset:53280\n"
"v_mfma_f32_16x16x32_bf16 v[52:55], a[128:131], a[20:23], v[52:55]\n"
"s_lshr_b32 s44, s54, 3\n"
"s_lshl_b32 s44, s44, 2\n"
"v_mfma_f32_16x16x32_bf16 v[48:51], a[128:131], a[16:19], v[48:51]\n"
"s_add_u32 s44, s44, s78\n"
"s_mul_i32 s44, s44, 128\n"
"s_waitcnt lgkmcnt(6)\n"
"v_mfma_f32_16x16x32_bf16 v[64:67], a[16:19], a[132:135], v[64:67]\n"
"ds_read_b64_tr_b16 a[148:149], v213 offset:49216\n"
"v_mfma_f32_16x16x32_bf16 v[68:71], a[20:23], a[132:135], v[68:71]\n"
"ds_read_b64_tr_b16 a[150:151], v213 offset:53312\n"
"v_mfma_f32_16x16x32_bf16 v[72:75], a[24:27], a[132:135], v[72:75]\n"
"buffer_load_dwordx4 a[52:55], v224, s[64:67], s81 offen offset:1024 sc1\n"
"v_mfma_f32_16x16x32_bf16 v[76:79], a[28:31], a[132:135], v[76:79]\n"
"s_waitcnt lgkmcnt(6)\n"
"v_mfma_f32_16x16x32_bf16 v[92:95], a[28:31], a[136:139], v[92:95]\n"
"ds_read_b64_tr_b16 a[152:153], v213 offset:49248\n"
"v_mfma_f32_16x16x32_bf16 v[88:91], a[24:27], a[136:139], v[88:91]\n"
"ds_read_b64_tr_b16 a[154:155], v213 offset:53344\n"
"v_mfma_f32_16x16x32_bf16 v[84:87], a[20:23], a[136:139], v[84:87]\n"
"buffer_load_dwordx4 a[56:59], v224, s[64:67], s81 offen offset:2048 sc1\n"
"v_mfma_f32_16x16x32_bf16 v[80:83], a[16:19], a[136:139], v[80:83]\n"
"s_waitcnt lgkmcnt(6)\n"
"v_mfma_f32_16x16x32_bf16 v[96:99], a[140:143], a[16:19], v[96:99]\n"
"ds_read_b64_tr_b16 a[156:157], v213 offset:49280\n"
"v_mfma_f32_16x16x32_bf16 v[100:103], a[140:143], a[20:23], v[100:103]\n"
"ds_read_b64_tr_b16 a[158:159], v213 offset:53376\n"
"v_mfma_f32_16x16x32_bf16 v[104:107], a[140:143], a[24:27], v[104:107]\n"
"buffer_load_dwordx4 a[60:63], v224, s[64:67], s81 offen offset:3072 sc1\n"
"v_mfma_f32_16x16x32_bf16 v[108:111], a[140:143], a[28:31], v[108:111]\n"
"s_add_u32 s81, s81, 0x1000\n"
"s_waitcnt lgkmcnt(6)\n"
"v_mfma_f32_16x16x32_bf16 v[124:127], a[144:147], a[28:31], v[124:127]\n"
"ds_read_b64_tr_b16 a[128:129], v213 offset:49312\n"
"v_mfma_f32_16x16x32_bf16 v[120:123], a[144:147], a[24:27], v[120:123]\n"
"ds_read_b64_tr_b16 a[130:131], v213 offset:53408\n"
"v_mfma_f32_16x16x32_bf16 v[116:119], a[144:147], a[20:23], v[116:119]\n"
"buffer_load_dwordx4 a[64:67], v224, s[64:67], s81 offen sc1\n"
"v_mfma_f32_16x16x32_bf16 v[112:115], a[144:147], a[16:19], v[112:115]\n"
"s_waitcnt lgkmcnt(6)\n"
"v_mfma_f32_16x16x32_bf16 v[128:131], a[148:151], a[16:19], v[128:131]\n"
"ds_read_b64_tr_b16 a[132:133], v213 offset:49344\n"
"v_mfma_f32_16x16x32_bf16 v[132:135], a[148:151], a[20:23], v[132:135]\n"
".Lhop2_%=:\n"
".Lpad2_%=:\n"
"s_cbranch_execz .Lpad6_%=\n"
"ds_read_b64_tr_b16 a[134:135], v213 offset:53440\n"
"v_mfma_f32_16x16x32_bf16 v[136:139], a[148:151], a[24:27], v[136:139]\n"
"buffer_load_dwordx4 a[68:71], v224, s[64:67], s81 offen offset:1024 sc1\n"
"v_mfma_f32_16x16x32_bf16 v[140:143], a[148:151], a[28:31], v[140:143]\n"
"s_barrier\n"
"s_waitcnt lgkmcnt(6)\n"
"v_mfma_f32_16x16x32_bf16 v[156:159], a[152:155], a[28:31], v[156:159]\n"
"ds_read_b64_tr_b16 a[136:137], v214\n"
"v_mfma_f32_16x16x32_bf16 v[152:155], a[152:155], a[24:27], v[152:155]\n"
"ds_read_b64_tr_b16 a[138:139], v214 offset:3072\n"
"v_mfma_f32_16x16x32_bf16 v[148:151], a[152:155], a[20:23], v[148:151]\n"
"buffer_load_dwordx4 a[72:75], v224, s[64:67], s81 offen offset:2048 sc1\n"
"v_mfma_f32_16x16x32_bf16 v[144:147], a[152:155], a[16:19], v[144:147]\n"
"s_waitcnt lgkmcnt(6)\n"
"v_mfma_f32_16x16x32_bf16 v[160:163], a[156:159], a[16:19], v[160:163]\n"
"ds_read_b64_tr_b16 a[140:141], v215\n"
"v_mfma_f32_16x16x32_bf16 v[164:167], a[156:159], a[20:23], v[164:167]\n"
"ds_read_b64_tr_b16 a[142:143], v215 offset:3072\n"
"v_mfma_f32_16x16x32_bf16 v[168:171], a[156:159], a[24:27], v[168:171]\n"
"buffer_load_dwordx4 a[76:79], v224, s[64:67], s81 offen offset:3072 sc1\n"
"v_mfma_f32_16x16x32_bf16 v[172:175], a[156:159], a[28:31], v[172:175]\n"
"s_add_u32 s81, s81, 0x1000\n"
"s_waitcnt lgkmcnt(6)\n"
"v_mfma_f32_16x16x32_bf16 v[188:191], a[128:131], a[28:31], v[188:191]\n"
"ds_read_b64_tr_b16 a[144:145], v214 offset:64\n"
"v_mfma_f32_16x16x32_bf16 v[184:187], a[128:131], a[24:27], v[184:187]\n"
"ds_read_b64_tr_b16 a[146:147], v214 offset:3136\n"
"v_mfma_f32_16x16x32_bf16 v[180:183], a[128:131], a[20:23], v[180:183]\n"
"s_add_u32 s87, s44, 4096\n"
"s_getpc_b64 s[44:45]\n"
"v_mfma_f32_16x16x32_bf16 v[176:179], a[128:131], a[16:19], v[176:179]\n"
"s_add_u32 s44, s44, s87\n"
"s_addc_u32 s45, s45, 0\n"
"s_load_dword s33, s[44:45], 0x0\n"
"s_waitcnt lgkmcnt(6)\n"
"v_mfma_f32_16x16x32_bf16 v[192:195], a[132:135], a[16:19], v[192:195]\n"
"ds_read_b64_tr_b16 a[148:149], v215 offset:64\n"
"v_mfma_f32_16x16x32_bf16 v[196:199], a[132:135], a[20:23], v[196:199]\n"
"ds_read_b64_tr_b16 a[150:151], v215 offset:3136\n"
"v_mfma_f32_16x16x32_bf16 v[200:203], a[132:135], a[24:27], v[200:203]\n"
"s_load_dword s34, s[44:45], 0x40\n"
"v_mfma_f32_16x16x32_bf16 v[204:207], a[132:135], a[28:31], v[204:207]\n"
"s_waitcnt vmcnt(15) lgkmcnt(6)\n"
"v_mfma_f32_16x16x32_bf16 v[0:3], a[136:139], a[32:35], v[0:3]\n"
"ds_read_b64_tr_b16 a[152:153], v214 offset:128\n"
"v_mfma_f32_16x16x32_bf16 v[4:7], a[136:139], a[36:39], v[4:7]\n"
"ds_read_b64_tr_b16 a[154:155], v214 offset:3200\n"
"s_add_u32 m0, s82, 0x7000\n"
"v_mfma_f32_16x16x32_bf16 v[8:11], a[136:139], a[40:43], v[8:11]\n"
"buffer_load_dwordx4 v217, s[56:59], s79 offen lds\n"
"v_mfma_f32_16x16x32_bf16 v[12:15], a[136:139], a[44:47], v[12:15]\n"
"s_waitcnt lgkmcnt(6)\n"
"v_mfma_f32_16x16x32_bf16 v[28:31], a[140:143], a[44:47], v[28:31]\n"
"ds_read_b64_tr_b16 a[156:157], v215 offset:128\n"
"v_mfma_f32_16x16x32_bf16 v[24:27], a[140:143], a[40:43], v[24:27]\n"
"ds_read_b64_tr_b16 a[158:159], v215 offset:3200\n"
"v_mfma_f32_16x16x32_bf16 v[20:23], a[140:143], a[36:39], v[20:23]\n"
"v_mfma_f32_16x16x32_bf16 v[16:19], a[140:143], a[32:35], v[16:19]\n"
"s_waitcnt lgkmcnt(6)\n"
"v_mfma_f32_16x16x32_bf16 v[32:35], a[144:147], a[32:35], v[32:35]\n"
"ds_read_b64_tr_b16 a[128:129], v216 offset:12288\n"
"v_mfma_f32_16x16x32_bf16 v[36:39], a[144:147], a[36:39], v[36:39]\n"
"ds_read_b64_tr_b16 a[130:131], v216 offset:16384\n"
"s_add_u32 m0, s82, 0x7400\n"
"v_mfma_f32_16x16x32_bf16 v[40:43], a[144:147], a[40:43], v[40:43]\n"
"buffer_load_dwordx4 v218, s[56:59], s79 offen lds\n"
"v_mfma_f32_16x16x32_bf16 v[44:47], a[144:147], a[44:47], v[44:47]\n"
"s_waitcnt lgkmcnt(6)\n"
"v_mfma_f32_16x16x32_bf16 v[60:63], a[148:151], a[44:47], v[60:63]\n"
"ds_read_b64_tr_b16 a[132:133], v216 offset:12320\n"
"v_mfma_f32_16x16x32_bf16 v[56:59], a[148:151], a[40:43], v[56:59]\n"
"ds_read_b64_tr_b16 a[134:135], v216 offset:16416\n"
"v_mfma_f32_16x16x32_bf16 v[52:55], a[148:151], a[36:39], v[52:55]\n"
"v_mfma_f32_16x16x32_bf16 v[48:51], a[148:151], a[32:35], v[48:51]\n"
"s_waitcnt lgkmcnt(6)\n"
"v_mfma_f32_16x16x32_bf16 v[64:67], a[32:35], a[152:155], v[64:67]\n"
"ds_read_b64_tr_b16 a[136:137], v216 offset:12352\n"
"v_mfma_f32_16x16x32_bf16 v[68:71], a[36:39], a[152:155], v[68:71]\n"
"ds_read_b64_tr_b16 a[138:139], v216 offset:16448\n"
"s_add_u32 m0, s82, 0x7800\n"
"v_mfma_f32_16x16x32_bf16 v[72:75], a[40:43], a[152:155], v[72:75]\n"
"buffer_load_dwordx4 v219, s[56:59], s79 offen lds\n"
"v_mfma_f32_16x16x32_bf16 v[76:79], a[44:47], a[152:155], v[76:79]\n"
"s_add_u32 s79, s79, 0x60000\n"
"s_waitcnt lgkmcnt(6)\n"
"v_mfma_f32_16x16x32_bf16 v[92:95], a[44:47], a[156:159], v[92:95]\n"
"ds_read_b64_tr_b16 a[140:141], v216 offset:12384\n"
"v_mfma_f32_16x16x32_bf16 v[88:91], a[40:43], a[156:159], v[88:91]\n"
"ds_read_b64_tr_b16 a[142:143], v216 offset:16480\n"
"v_mfma_f32_16x16x32_bf16 v[84:87], a[36:39], a[156:159], v[84:87]\n"
"v_mfma_f32_16x16x32_bf16 v[80:83], a[32:35], a[156:159], v[80:83]\n"
"s_waitcnt lgkmcnt(6)\n"
"v_mfma_f32_16x16x32_bf16 v[96:99], a[128:131], a[32:35], v[96:99]\n"
"ds_read_b64_tr_b16 a[144:145], v216 offset:12416\n"
"v_mfma_f32_16x16x32_bf16 v[100:103], a[128:131], a[36:39], v[100:103]\n"
"ds_read_b64_tr_b16 a[146:147], v216 offset:16512\n"
"s_add_u32 m0, s83, 0x7000\n"
"v_mfma_f32_16x16x32_bf16 v[104:107], a[128:131], a[40:43], v[104:107]\n"
"buffer_load_dwordx4 v220, s[60:63], s80 offen lds\n"
"v_mfma_f32_16x16x32_bf16 v[108:111], a[128:131], a[44:47], v[108:111]\n"
"s_waitcnt lgkmcnt(6)\n"
"v_mfma_f32_16x16x32_bf16 v[124:127], a[132:135], a[44:47], v[124:127]\n"
"ds_read_b64_tr_b16 a[148:149], v216 offset:12448\n"
"v_mfma_f32_16x16x32_bf16 v[120:123], a[132:135], a[40:43], v[120:123]\n"
"ds_read_b64_tr_b16 a[150:151], v216 offset:16544\n"
"s_add_u32 m0, s83, 0x7400\n"
"v_mfma_f32_16x16x32_bf16 v[116:119], a[132:135], a[36:39], v[116:119]\n"
"buffer_load_dwordx4 v221, s[60:63], s80 offen lds\n"
"v_mfma_f32_16x16x32_bf16 v[112:115], a[132:135], a[32:35], v[112:115]\n"
"s_waitcnt lgkmcnt(6)\n"
"v_mfma_f32_16x16x32_bf16 v[128:131], a[136:139], a[32:35], v[128:131]\n"
"ds_read_b64_tr_b16 a[152:153], v216 offset:12480\n"
"v_mfma_f32_16x16x32_bf16 v[132:135], a[136:139], a[36:39], v[132:135]\n"
"ds_read_b64_tr_b16 a[154:155], v216 offset:16576\n"
"v_mfma_f32_16x16x32_bf16 v[136:139], a[136:139], a[40:43], v[136:139]\n"
"v_mfma_f32_16x16x32_bf16 v[140:143], a[136:139], a[44:47], v[140:143]\n"
"s_waitcnt lgkmcnt(6)\n"
"v_mfma_f32_16x16x32_bf16 v[156:159], a[140:143], a[44:47], v[156:159]\n"
"ds_read_b64_tr_b16 a[156:157], v214 offset:6144\n"
"v_mfma_f32_16x16x32_bf16 v[152:155], a[140:143], a[40:43], v[152:155]\n"
"ds_read_b64_tr_b16 a[158:159], v214 offset:9216\n"
"s_add_u32 m0, s83, 0x7800\n"
"v_mfma_f32_16x16x32_bf16 v[148:151], a[140:143], a[36:39], v[148:151]\n"
"buffer_load_dwordx4 v222, s[60:63], s80 offen lds\n"
"v_mfma_f32_16x16x32_bf16 v[144:147], a[140:143], a[32:35], v[144:147]\n"
"s_waitcnt lgkmcnt(6)\n"
"v_mfma_f32_16x16x32_bf16 v[160:163], a[144:147], a[32:35], v[160:163]\n"
"ds_read_b64_tr_b16 a[128:129], v215 offset:6144\n"
"v_mfma_f32_16x16x32_bf16 v[164:167], a[144:147], a[36:39], v[164:167]\n"
"ds_read_b64_tr_b16 a[130:131], v215 offset:9216\n"
"v_mfma_f32_16x16x32_bf16 v[168:171], a[144:147], a[40:43], v[168:171]\n"
"buffer_load_dword a164, v249, s[56:59], s29 offen\n"
"v_mfma_f32_16x16x32_bf16 v[172:175], a[144:147], a[44:47], v[172:175]\n"
"s_add_u32 s29, s29, 0x180000\n"
"s_waitcnt lgkmcnt(6)\n"
"v_mfma_f32_16x16x32_bf16 v[188:191], a[148:151], a[44:47], v[188:191]\n"
"ds_read_b64_tr_b16 a[132:133], v214 offset:6208\n"
"v_mfma_f32_16x16x32_bf16 v[184:187], a[148:151], a[40:43], v[184:187]\n"
"ds_read_b64_tr_b16 a[134:135], v214 offset:9280\n"
"v_mfma_f32_16x16x32_bf16 v[180:183], a[148:151], a[36:39], v[180:183]\n"
"buffer_load_dword a165, v253, s[60:63], s30 offen\n"
"v_mfma_f32_16x16x32_bf16 v[176:179], a[148:151], a[32:35], v[176:179]\n"
"s_add_u32 s30, s30, 0x1c0000\n"
"s_waitcnt lgkmcnt(6)\n"
"v_mfma_f32_16x16x32_bf16 v[192:195], a[152:155], a[32:35], v[192:195]\n"
"ds_read_b64_tr_b16 a[136:137], v215 offset:6208\n"
"v_mfma_f32_16x16x32_bf16 v[196:199], a[152:155], a[36:39], v[196:199]\n"
"ds_read_b64_tr_b16 a[138:139], v215 offset:9280\n"
"v_mfma_f32_16x16x32_bf16 v[200:203], a[152:155], a[40:43], v[200:203]\n"
"v_mfma_f32_16x16x32_bf16 v[204:207], a[152:155], a[44:47], v[204:207]\n"
"s_waitcnt vmcnt(12) lgkmcnt(6)\n"
"v_mfma_f32_16x16x32_bf16 v[0:3], a[156:159], a[48:51], v[0:3]\n"
"ds_read_b64_tr_b16 a[140:141], v214 offset:6272\n"
"v_mfma_f32_16x16x32_bf16 v[4:7], a[156:159], a[52:55], v[4:7]\n"
"ds_read_b64_tr_b16 a[142:143], v214 offset:9344\n"
"v_mfma_f32_16x16x32_bf16 v[8:11], a[156:159], a[56:59], v[8:11]\n"
"v_mfma_f32_16x16x32_bf16 v[12:15], a[156:159], a[60:63], v[12:15]\n"
"s_waitcnt lgkmcnt(6)\n"
"v_mfma_f32_16x16x32_bf16 v[28:31], a[128:131], a[60:63], v[28:31]\n"
"ds_read_b64_tr_b16 a[144:145], v215 offset:6272\n"
"v_mfma_f32_16x16x32_bf16 v[24:27], a[128:131], a[56:59], v[24:27]\n"
"ds_read_b64_tr_b16 a[146:147], v215 offset:9344\n"
"v_mfma_f32_16x16x32_bf16 v[20:23], a[128:131], a[52:55], v[20:23]\n"
"v_mfma_f32_16x16x32_bf16 v[16:19], a[128:131], a[48:51], v[16:19]\n"
"s_waitcnt lgkmcnt(6)\n"
"v_mfma_f32_16x16x32_bf16 v[32:35], a[132:135], a[48:51], v[32:35]\n"
"ds_read_b64_tr_b16 a[148:149], v216 offset:20480\n"
"v_mfma_f32_16x16x32_bf16 v[36:39], a[132:135], a[52:55], v[36:39]\n"
"ds_read_b64_tr_b16 a[150:151], v216 offset:24576\n"
"v_mfma_f32_16x16x32_bf16 v[40:43], a[132:135], a[56:59], v[40:43]\n"
"s_lshl_b32 s90, s77, 12\n"
"s_add_u32 s38, s95, s90\n"
"v_mfma_f32_16x16x32_bf16 v[44:47], a[132:135], a[60:63], v[44:47]\n"
"s_lshl_b32 s90, s76, 2\n"
"s_add_u32 s90, s90, s78\n"
"s_waitcnt lgkmcnt(6)\n"
"v_mfma_f32_16x16x32_bf16 v[60:63], a[136:139], a[60:63], v[60:63]\n"
"ds_read_b64_tr_b16 a[152:153], v216 offset:20512\n"
"v_mfma_f32_16x16x32_bf16 v[56:59], a[136:139], a[56:59], v[56:59]\n"
"ds_read_b64_tr_b16 a[154:155], v216 offset:24608\n"
"v_mfma_f32_16x16x32_bf16 v[52:55], a[136:139], a[52:55], v[52:55]\n"
"s_lshl_b32 s90, s90, 5\n"
"s_add_u32 s90, s90, s77\n"
"v_mfma_f32_16x16x32_bf16 v[48:51], a[136:139], a[48:51], v[48:51]\n"
"s_lshl_b32 s90, s90, 7\n"
"s_add_u32 s42, s90, 0x20a0000\n"
"s_waitcnt lgkmcnt(6)\n"
"v_mfma_f32_16x16x32_bf16 v[64:67], a[48:51], a[140:143], v[64:67]\n"
"ds_read_b64_tr_b16 a[156:157], v216 offset:20544\n"
"v_mfma_f32_16x16x32_bf16 v[68:71], a[52:55], a[140:143], v[68:71]\n"
"ds_read_b64_tr_b16 a[158:159], v216 offset:24640\n"
"s_add_u32 m0, s83, 0x7c00\n"
"v_mfma_f32_16x16x32_bf16 v[72:75], a[56:59], a[140:143], v[72:75]\n"
"buffer_load_dwordx4 v223, s[60:63], s80 offen lds\n"
"v_mfma_f32_16x16x32_bf16 v[76:79], a[60:63], a[140:143], v[76:79]\n"
"s_add_u32 s80, s80, 0x70000\n"
"s_waitcnt lgkmcnt(6)\n"
"v_mfma_f32_16x16x32_bf16 v[92:95], a[60:63], a[144:147], v[92:95]\n"
"ds_read_b64_tr_b16 a[128:129], v216 offset:20576\n"
"v_mfma_f32_16x16x32_bf16 v[88:91], a[56:59], a[144:147], v[88:91]\n"
"ds_read_b64_tr_b16 a[130:131], v216 offset:24672\n"
"v_mfma_f32_16x16x32_bf16 v[84:87], a[52:55], a[144:147], v[84:87]\n"
"buffer_load_dwordx4 a[80:83], v224, s[64:67], s81 offen sc1\n"
"v_mfma_f32_16x16x32_bf16 v[80:83], a[48:51], a[144:147], v[80:83]\n"
"s_and_b32 s90, s54, 1\n"
"s_lshl_b32 s90, s90, 2\n"
"s_waitcnt lgkmcnt(6)\n"
"v_mfma_f32_16x16x32_bf16 v[96:99], a[148:151], a[48:51], v[96:99]\n"
"ds_read_b64_tr_b16 a[132:133], v216 offset:20608\n"
"v_mfma_f32_16x16x32_bf16 v[100:103], a[148:151], a[52:55], v[100:103]\n"
"ds_read_b64_tr_b16 a[134:135], v216 offset:24704\n"
"v_mfma_f32_16x16x32_bf16 v[104:107], a[148:151], a[56:59], v[104:107]\n"
"buffer_load_dwordx4 a[84:87], v224, s[64:67], s81 offen offset:1024 sc1\n"
"v_mfma_f32_16x16x32_bf16 v[108:111], a[148:151], a[60:63], v[108:111]\n"
"s_lshr_b32 s92, s54, 6\n"
"s_add_u32 s90, s90, s92\n"
"s_waitcnt lgkmcnt(6)\n"
"v_mfma_f32_16x16x32_bf16 v[124:127], a[152:155], a[60:63], v[124:127]\n"
"ds_read_b64_tr_b16 a[136:137], v216 offset:20640\n"
"v_mfma_f32_16x16x32_bf16 v[120:123], a[152:155], a[56:59], v[120:123]\n"
"ds_read_b64_tr_b16 a[138:139], v216 offset:24736\n"
"v_mfma_f32_16x16x32_bf16 v[116:119], a[152:155], a[52:55], v[116:119]\n"
"buffer_load_dwordx4 a[88:91], v224, s[64:67], s81 offen offset:2048 sc1\n"
"v_mfma_f32_16x16x32_bf16 v[112:115], a[152:155], a[48:51], v[112:115]\n"
"s_lshl_b32 s90, s90, 2\n"
"s_add_u32 s35, s90, s78\n"
"s_waitcnt lgkmcnt(6)\n"
"v_mfma_f32_16x16x32_bf16 v[128:131], a[156:159], a[48:51], v[128:131]\n"
"ds_read_b64_tr_b16 a[140:141], v216 offset:20672\n"
"v_mfma_f32_16x16x32_bf16 v[132:135], a[156:159], a[52:55], v[132:135]\n"
"ds_read_b64_tr_b16 a[142:143], v216 offset:24768\n"
"v_mfma_f32_16x16x32_bf16 v[136:139], a[156:159], a[56:59], v[136:139]\n"
"buffer_load_dwordx4 a[92:95], v224, s[64:67], s81 offen offset:3072 sc1\n"
"v_mfma_f32_16x16x32_bf16 v[140:143], a[156:159], a[60:63], v[140:143]\n"
"s_add_u32 s81, s81, 0x1000\n"
"s_barrier\n"
"s_waitcnt lgkmcnt(6)\n"
"v_mfma_f32_16x16x32_bf16 v[156:159], a[128:131], a[60:63], v[156:159]\n"
"ds_read_b64_tr_b16 a[144:145], v208\n"
"v_mfma_f32_16x16x32_bf16 v[152:155], a[128:131], a[56:59], v[152:155]\n"
"ds_read_b64_tr_b16 a[146:147], v208 offset:3072\n"
"v_mfma_f32_16x16x32_bf16 v[148:151], a[128:131], a[52:55], v[148:151]\n"
"buffer_load_dwordx4 a[96:99], v224, s[64:67], s81 offen sc1\n"
"v_mfma_f32_16x16x32_bf16 v[144:147], a[128:131], a[48:51], v[144:147]\n"
"s_bfe_u32 s30, s54, 0x20001\n"
"s_mul_i32 s95, s35, 0x90000\n"
"s_waitcnt lgkmcnt(6)\n"
"v_mfma_f32_16x16x32_bf16 v[160:163], a[132:135], a[48:51], v[160:163]\n"
"ds_read_b64_tr_b16 a[148:149], v209\n"
"v_mfma_f32_16x16x32_bf16 v[164:167], a[132:135], a[52:55], v[164:167]\n"
"ds_read_b64_tr_b16 a[150:151], v209 offset:3072\n"
"v_mfma_f32_16x16x32_bf16 v[168:171], a[132:135], a[56:59], v[168:171]\n"
"buffer_load_dwordx4 a[100:103], v224, s[64:67], s81 offen offset:1024 sc1\n"
"v_mfma_f32_16x16x32_bf16 v[172:175], a[132:135], a[60:63], v[172:175]\n"
"s_add_u32 s95, s95, 0x400000\n"
"s_bfe_u32 s90, s54, 0x30003\n"
"s_waitcnt lgkmcnt(6)\n"
"v_mfma_f32_16x16x32_bf16 v[188:191], a[136:139], a[60:63], v[188:191]\n"
"ds_read_b64_tr_b16 a[152:153], v208 offset:64\n"
"v_mfma_f32_16x16x32_bf16 v[184:187], a[136:139], a[56:59], v[184:187]\n"
"ds_read_b64_tr_b16 a[154:155], v208 offset:3136\n"
"v_mfma_f32_16x16x32_bf16 v[180:183], a[136:139], a[52:55], v[180:183]\n"
"buffer_load_dwordx4 a[104:107], v224, s[64:67], s81 offen offset:2048 sc1\n"
"v_mfma_f32_16x16x32_bf16 v[176:179], a[136:139], a[48:51], v[176:179]\n"
"s_xor_b32 s90, s90, 1\n"
"s_lshl_b32 s90, s90, 8\n"
"s_waitcnt lgkmcnt(6)\n"
"v_mfma_f32_16x16x32_bf16 v[192:195], a[140:143], a[48:51], v[192:195]\n"
"ds_read_b64_tr_b16 a[156:157], v209 offset:64\n"
"v_mfma_f32_16x16x32_bf16 v[196:199], a[140:143], a[52:55], v[196:199]\n"
"ds_read_b64_tr_b16 a[158:159], v209 offset:3136\n"
"v_mfma_f32_16x16x32_bf16 v[200:203], a[140:143], a[56:59], v[200:203]\n"
"buffer_load_dwordx4 a[108:111], v224, s[64:67], s81 offen offset:3072 sc1\n"
"v_mfma_f32_16x16x32_bf16 v[204:207], a[140:143], a[60:63], v[204:207]\n"
"s_add_u32 s81, s81, 0x1000\n"
"s_waitcnt vmcnt(17) lgkmcnt(6)\n"
"v_mfma_f32_16x16x32_bf16 v[0:3], a[144:147], a[64:67], v[0:3]\n"
"ds_read_b64_tr_b16 a[128:129], v208 offset:128\n"
"v_mfma_f32_16x16x32_bf16 v[4:7], a[144:147], a[68:71], v[4:7]\n"
"ds_read_b64_tr_b16 a[130:131], v208 offset:3200\n"
"v_mfma_f32_16x16x32_bf16 v[8:11], a[144:147], a[72:75], v[8:11]\n"
"s_lshr_b32 s92, s30, 1\n"
"s_lshl_b32 s92, s92, 7\n"
"v_mfma_f32_16x16x32_bf16 v[12:15], a[144:147], a[76:79], v[12:15]\n"
"s_add_u32 s39, s90, s92\n"
"s_mul_i32 s36, s30, 28\n"
"s_waitcnt lgkmcnt(6)\n"
"v_mfma_f32_16x16x32_bf16 v[28:31], a[148:151], a[76:79], v[28:31]\n"
"ds_read_b64_tr_b16 a[132:133], v209 offset:128\n"
"v_mfma_f32_16x16x32_bf16 v[24:27], a[148:151], a[72:75], v[24:27]\n"
"ds_read_b64_tr_b16 a[134:135], v209 offset:3200\n"
"s_add_u32 m0, s82, 0xe000\n"
"v_mfma_f32_16x16x32_bf16 v[20:23], a[148:151], a[68:71], v[20:23]\n"
"buffer_load_dwordx4 v217, s[56:59], s79 offen lds\n"
"v_mfma_f32_16x16x32_bf16 v[16:19], a[148:151], a[64:67], v[16:19]\n"
"s_add_u32 s36, s36, s78\n"
"s_lshl_b32 s36, s36, 16\n"
"s_waitcnt lgkmcnt(6)\n"
"v_mfma_f32_16x16x32_bf16 v[32:35], a[152:155], a[64:67], v[32:35]\n"
"ds_read_b64_tr_b16 a[136:137], v210 offset:12288\n"
"v_mfma_f32_16x16x32_bf16 v[36:39], a[152:155], a[68:71], v[36:39]\n"
"ds_read_b64_tr_b16 a[138:139], v210 offset:16384\n"
"v_mfma_f32_16x16x32_bf16 v[40:43], a[152:155], a[72:75], v[40:43]\n"
"s_add_u32 s36, s36, s39\n"
"s_xor_b32 s36, s36, 0x80\n"
"v_mfma_f32_16x16x32_bf16 v[44:47], a[152:155], a[76:79], v[44:47]\n"
"s_mul_i32 s92, s30, 4\n"
"s_add_u32 s31, s92, s78\n"
"s_waitcnt lgkmcnt(6)\n"
"v_mfma_f32_16x16x32_bf16 v[60:63], a[156:159], a[76:79], v[60:63]\n"
"ds_read_b64_tr_b16 a[140:141], v210 offset:12320\n"
"v_mfma_f32_16x16x32_bf16 v[56:59], a[156:159], a[72:75], v[56:59]\n"
"ds_read_b64_tr_b16 a[142:143], v210 offset:16416\n"
"s_add_u32 m0, s82, 0xe400\n"
"v_mfma_f32_16x16x32_bf16 v[52:55], a[156:159], a[68:71], v[52:55]\n"
"buffer_load_dwordx4 v218, s[56:59], s79 offen lds\n"
"v_mfma_f32_16x16x32_bf16 v[48:51], a[156:159], a[64:67], v[48:51]\n"
"s_lshl_b32 s31, s31, 16\n"
"s_add_u32 s31, s31, s39\n"
"s_waitcnt lgkmcnt(6)\n"
"v_mfma_f32_16x16x32_bf16 v[64:67], a[64:67], a[128:131], v[64:67]\n"
"ds_read_b64_tr_b16 a[144:145], v210 offset:12352\n"
"v_mfma_f32_16x16x32_bf16 v[68:71], a[68:71], a[128:131], v[68:71]\n"
"ds_read_b64_tr_b16 a[146:147], v210 offset:16448\n"
"v_mfma_f32_16x16x32_bf16 v[72:75], a[72:75], a[128:131], v[72:75]\n"
"s_xor_b32 s37, s31, 0x80\n"
"s_lshl_b32 s92, s92, 12\n"
"v_mfma_f32_16x16x32_bf16 v[76:79], a[76:79], a[128:131], v[76:79]\n"
"s_add_u32 s29, s95, s92\n"
"s_add_u32 s95, s95, 0x10000\n"
"s_waitcnt lgkmcnt(6)\n"
"v_mfma_f32_16x16x32_bf16 v[92:95], a[76:79], a[132:135], v[92:95]\n"
"ds_read_b64_tr_b16 a[148:149], v210 offset:12384\n"
"v_mfma_f32_16x16x32_bf16 v[88:91], a[72:75], a[132:135], v[88:91]\n"
"ds_read_b64_tr_b16 a[150:151], v210 offset:16480\n"
"s_add_u32 m0, s82, 0xe800\n"
"v_mfma_f32_16x16x32_bf16 v[84:87], a[68:71], a[132:135], v[84:87]\n"
"buffer_load_dwordx4 v219, s[56:59], s79 offen lds\n"
"v_mfma_f32_16x16x32_bf16 v[80:83], a[64:67], a[132:135], v[80:83]\n"
"s_add_u32 s79, s79, 0x60000\n"
"s_waitcnt lgkmcnt(6)\n"
"v_mfma_f32_16x16x32_bf16 v[96:99], a[136:139], a[64:67], v[96:99]\n"
"ds_read_b64_tr_b16 a[152:153], v210 offset:12416\n"
"v_mfma_f32_16x16x32_bf16 v[100:103], a[136:139], a[68:71], v[100:103]\n"
"ds_read_b64_tr_b16 a[154:155], v210 offset:16512\n"
"v_mfma_f32_16x16x32_bf16 v[104:107], a[136:139], a[72:75], v[104:107]\n"
"s_lshl_b32 s43, s35, 12\n"
"s_lshl_b32 s90, s30, 10\n"
"v_mfma_f32_16x16x32_bf16 v[108:111], a[136:139], a[76:79], v[108:111]\n"
"s_add_u32 s43, s43, s90\n"
"s_add_u32 s43, s43, 0x20a0000\n"
"s_waitcnt lgkmcnt(6)\n"
"v_mfma_f32_16x16x32_bf16 v[124:127], a[140:143], a[76:79], v[124:127]\n"
"ds_read_b64_tr_b16 a[156:157], v210 offset:12448\n"
"v_mfma_f32_16x16x32_bf16 v[120:123], a[140:143], a[72:75], v[120:123]\n"
"ds_read_b64_tr_b16 a[158:159], v210 offset:16544\n"
"s_add_u32 m0, s83, 0xe000\n"
"v_mfma_f32_16x16x32_bf16 v[116:119], a[140:143], a[68:71], v[116:119]\n"
"buffer_load_dwordx4 v220, s[60:63], s80 offen lds\n"
"v_mfma_f32_16x16x32_bf16 v[112:115], a[140:143], a[64:67], v[112:115]\n"
"s_waitcnt lgkmcnt(6)\n"
"v_and_b32 v226, 7, v236\n"
"v_lshlrev_b32 v226, 7, v226\n"
"v_mfma_f32_16x16x32_bf16 v[128:131], a[144:147], a[64:67], v[128:131]\n"
"ds_read_b64_tr_b16 a[128:129], v210 offset:12480\n"
"v_mfma_f32_16x16x32_bf16 v[132:135], a[144:147], a[68:71], v[132:135]\n"
"ds_read_b64_tr_b16 a[130:131], v210 offset:16576\n"
"v_mfma_f32_16x16x32_bf16 v[136:139], a[144:147], a[72:75], v[136:139]\n"
"v_lshrrev_b32 v250, 3, v236\n"
"v_and_b32 v251, 4, v250\n"
"v_mfma_f32_16x16x32_bf16 v[140:143], a[144:147], a[76:79], v[140:143]\n"
"s_waitcnt lgkmcnt(6)\n"
"v_add_u32 v251, v251, v250\n"
"v_lshlrev_b32 v251, 11, v251\n"
"v_mfma_f32_16x16x32_bf16 v[156:159], a[148:151], a[76:79], v[156:159]\n"
"ds_read_b64_tr_b16 a[132:133], v208 offset:6144\n"
"v_mfma_f32_16x16x32_bf16 v[152:155], a[148:151], a[72:75], v[152:155]\n"
"ds_read_b64_tr_b16 a[134:135], v208 offset:9216\n"
"s_add_u32 m0, s83, 0xe400\n"
"v_mfma_f32_16x16x32_bf16 v[148:151], a[148:151], a[68:71], v[148:151]\n"
"buffer_load_dwordx4 v221, s[60:63], s80 offen lds\n"
"v_mfma_f32_16x16x32_bf16 v[144:147], a[148:151], a[64:67], v[144:147]\n"
"s_waitcnt lgkmcnt(6)\n"
"v_and_b32 v250, 6, v250\n"
"v_and_b32 v252, 7, v236\n"
"v_mfma_f32_16x16x32_bf16 v[160:163], a[152:155], a[64:67], v[160:163]\n"
"ds_read_b64_tr_b16 a[136:137], v209 offset:6144\n"
"v_mfma_f32_16x16x32_bf16 v[164:167], a[152:155], a[68:71], v[164:167]\n"
"ds_read_b64_tr_b16 a[138:139], v209 offset:9216\n"
"s_add_u32 m0, s83, 0xe800\n"
"v_mfma_f32_16x16x32_bf16 v[168:171], a[152:155], a[72:75], v[168:171]\n"
"buffer_load_dwordx4 v222, s[60:63], s80 offen lds\n"
"v_mfma_f32_16x16x32_bf16 v[172:175], a[152:155], a[76:79], v[172:175]\n"
"s_waitcnt lgkmcnt(6)\n"
"v_xor_b32 v250, v250, v252\n"
"v_lshl_add_u32 v254, v250, 4, v251\n"
"v_mfma_f32_16x16x32_bf16 v[188:191], a[156:159], a[76:79], v[188:191]\n"
"ds_read_b64_tr_b16 a[140:141], v208 offset:6208\n"
"v_mfma_f32_16x16x32_bf16 v[184:187], a[156:159], a[72:75], v[184:187]\n"
"ds_read_b64_tr_b16 a[142:143], v208 offset:9280\n"
"v_mfma_f32_16x16x32_bf16 v[180:183], a[156:159], a[68:71], v[180:183]\n"
"v_lshrrev_b32 v250, 2, v236\n"
"v_lshlrev_b32 v251, 7, v250\n"
"v_mfma_f32_16x16x32_bf16 v[176:179], a[156:159], a[64:67], v[176:179]\n"
"s_waitcnt lgkmcnt(6)\n"
"v_and_b32 v252, 3, v236\n"
"v_lshl_add_u32 v251, v252, 3, v251\n"
"v_mfma_f32_16x16x32_bf16 v[192:195], a[128:131], a[64:67], v[192:195]\n"
"ds_read_b64_tr_b16 a[144:145], v209 offset:6208\n"
"v_mfma_f32_16x16x32_bf16 v[196:199], a[128:131], a[68:71], v[196:199]\n"
"ds_read_b64_tr_b16 a[146:147], v209 offset:9280\n"
"s_add_u32 m0, s83, 0xec00\n"
"v_mfma_f32_16x16x32_bf16 v[200:203], a[128:131], a[72:75], v[200:203]\n"
"buffer_load_dwordx4 v223, s[60:63], s80 offen lds\n"
"v_mfma_f32_16x16x32_bf16 v[204:207], a[128:131], a[76:79], v[204:207]\n"
"s_add_u32 s80, s80, 0x70000\n"
"s_waitcnt vmcnt(11) lgkmcnt(6)\n"
"v_mfma_f32_16x16x32_bf16 v[0:3], a[132:135], a[80:83], v[0:3]\n"
"ds_read_b64_tr_b16 a[148:149], v208 offset:6272\n"
"v_mfma_f32_16x16x32_bf16 v[4:7], a[132:135], a[84:87], v[4:7]\n"
"ds_read_b64_tr_b16 a[150:151], v208 offset:9344\n"
"v_mfma_f32_16x16x32_bf16 v[8:11], a[132:135], a[88:91], v[8:11]\n"
"buffer_load_dwordx4 a[112:115], v224, s[64:67], s81 offen sc1\n"
"v_mfma_f32_16x16x32_bf16 v[12:15], a[132:135], a[92:95], v[12:15]\n"
"s_waitcnt lgkmcnt(6)\n"
"v_add_u32 v251, 0x10000, v251\n"
"v_bfe_u32 v250, v236, 3, 2\n"
"v_mfma_f32_16x16x32_bf16 v[28:31], a[136:139], a[92:95], v[28:31]\n"
"ds_read_b64_tr_b16 a[152:153], v209 offset:6272\n"
"v_mfma_f32_16x16x32_bf16 v[24:27], a[136:139], a[88:91], v[24:27]\n"
"ds_read_b64_tr_b16 a[154:155], v209 offset:9344\n"
"v_mfma_f32_16x16x32_bf16 v[20:23], a[136:139], a[84:87], v[20:23]\n"
"buffer_load_dwordx4 a[116:119], v224, s[64:67], s81 offen offset:1024 sc1\n"
"v_mfma_f32_16x16x32_bf16 v[16:19], a[136:139], a[80:83], v[16:19]\n"
"s_waitcnt lgkmcnt(6)\n"
"v_xor_b32 v252, 3, v250\n"
"v_lshl_add_u32 v239, v252, 5, v251\n"
"v_mfma_f32_16x16x32_bf16 v[32:35], a[140:143], a[80:83], v[32:35]\n"
"ds_read_b64_tr_b16 a[156:157], v210 offset:20480\n"
"v_mfma_f32_16x16x32_bf16 v[36:39], a[140:143], a[84:87], v[36:39]\n"
"ds_read_b64_tr_b16 a[158:159], v210 offset:24576\n"
"v_mfma_f32_16x16x32_bf16 v[40:43], a[140:143], a[88:91], v[40:43]\n"
"buffer_load_dwordx4 a[120:123], v224, s[64:67], s81 offen offset:2048 sc1\n"
"v_mfma_f32_16x16x32_bf16 v[44:47], a[140:143], a[92:95], v[44:47]\n"
"s_waitcnt lgkmcnt(6)\n"
"v_mfma_f32_16x16x32_bf16 v[60:63], a[144:147], a[92:95], v[60:63]\n"
"ds_read_b64_tr_b16 a[128:129], v210 offset:20512\n"
"v_mfma_f32_16x16x32_bf16 v[56:59], a[144:147], a[88:91], v[56:59]\n"
"ds_read_b64_tr_b16 a[130:131], v210 offset:24608\n"
"v_mfma_f32_16x16x32_bf16 v[52:55], a[144:147], a[84:87], v[52:55]\n"
"buffer_load_dwordx4 a[124:127], v224, s[64:67], s81 offen offset:3072 sc1\n"
"v_mfma_f32_16x16x32_bf16 v[48:51], a[144:147], a[80:83], v[48:51]\n"
"s_add_u32 s81, s81, 0x1000\n"
"s_waitcnt lgkmcnt(6)\n"
"v_mfma_f32_16x16x32_bf16 v[64:67], a[80:83], a[148:151], v[64:67]\n"
"ds_read_b64_tr_b16 a[132:133], v210 offset:20544\n"
"v_mfma_f32_16x16x32_bf16 v[68:71], a[84:87], a[148:151], v[68:71]\n"
"ds_read_b64_tr_b16 a[134:135], v210 offset:24640\n"
"v_mfma_f32_16x16x32_bf16 v[72:75], a[88:91], a[148:151], v[72:75]\n"
"buffer_load_dwordx4 a[0:3], v224, s[64:67], s81 offen sc1\n"
"v_mfma_f32_16x16x32_bf16 v[76:79], a[92:95], a[148:151], v[76:79]\n"
"s_waitcnt lgkmcnt(6)\n"
"v_mfma_f32_16x16x32_bf16 v[92:95], a[92:95], a[152:155], v[92:95]\n"
"ds_read_b64_tr_b16 a[136:137], v210 offset:20576\n"
"v_mfma_f32_16x16x32_bf16 v[88:91], a[88:91], a[152:155], v[88:91]\n"
"ds_read_b64_tr_b16 a[138:139], v210 offset:24672\n"
"v_mfma_f32_16x16x32_bf16 v[84:87], a[84:87], a[152:155], v[84:87]\n"
"buffer_load_dwordx4 a[4:7], v224, s[64:67], s81 offen offset:1024 sc1\n"
"v_mfma_f32_16x16x32_bf16 v[80:83], a[80:83], a[152:155], v[80:83]\n"
"s_waitcnt lgkmcnt(6)\n"
"v_mfma_f32_16x16x32_bf16 v[96:99], a[156:159], a[80:83], v[96:99]\n"
"ds_read_b64_tr_b16 a[140:141], v210 offset:20608\n"
"v_mfma_f32_16x16x32_bf16 v[100:103], a[156:159], a[84:87], v[100:103]\n"
"ds_read_b64_tr_b16 a[142:143], v210 offset:24704\n"
"v_mfma_f32_16x16x32_bf16 v[104:107], a[156:159], a[88:91], v[104:107]\n"
"v_mfma_f32_16x16x32_bf16 v[108:111], a[156:159], a[92:95], v[108:111]\n"
"s_waitcnt lgkmcnt(6)\n"
"v_mfma_f32_16x16x32_bf16 v[124:127], a[128:131], a[92:95], v[124:127]\n"
"ds_read_b64_tr_b16 a[144:145], v210 offset:20640\n"
"v_mfma_f32_16x16x32_bf16 v[120:123], a[128:131], a[88:91], v[120:123]\n"
"ds_read_b64_tr_b16 a[146:147], v210 offset:24736\n"
"v_mfma_f32_16x16x32_bf16 v[116:119], a[128:131], a[84:87], v[116:119]\n"
"buffer_load_dwordx4 a[8:11], v224, s[64:67], s81 offen offset:2048 sc1\n"
"v_mfma_f32_16x16x32_bf16 v[112:115], a[128:131], a[80:83], v[112:115]\n"
"s_waitcnt lgkmcnt(6)\n"
"v_mfma_f32_16x16x32_bf16 v[128:131], a[132:135], a[80:83], v[128:131]\n"
"ds_read_b64_tr_b16 a[148:149], v210 offset:20672\n"
"v_mfma_f32_16x16x32_bf16 v[132:135], a[132:135], a[84:87], v[132:135]\n"
"ds_read_b64_tr_b16 a[150:151], v210 offset:24768\n"
"v_mfma_f32_16x16x32_bf16 v[136:139], a[132:135], a[88:91], v[136:139]\n"
"buffer_load_dwordx4 a[12:15], v224, s[64:67], s81 offen offset:3072 sc1\n"
"v_mfma_f32_16x16x32_bf16 v[140:143], a[132:135], a[92:95], v[140:143]\n"
"s_add_u32 s81, s81, 0x1000\n"
"s_barrier\n"
"s_waitcnt lgkmcnt(6)\n"
"v_mfma_f32_16x16x32_bf16 v[156:159], a[136:139], a[92:95], v[156:159]\n"
"ds_read_b64_tr_b16 a[152:153], v208 offset:28672\n"
"v_mfma_f32_16x16x32_bf16 v[152:155], a[136:139], a[88:91], v[152:155]\n"
"ds_read_b64_tr_b16 a[154:155], v208 offset:31744\n"
"v_mfma_f32_16x16x32_bf16 v[148:151], a[136:139], a[84:87], v[148:151]\n"
"v_mfma_f32_16x16x32_bf16 v[144:147], a[136:139], a[80:83], v[144:147]\n"
"s_waitcnt lgkmcnt(6)\n"
"v_mfma_f32_16x16x32_bf16 v[160:163], a[140:143], a[80:83], v[160:163]\n"
"ds_read_b64_tr_b16 a[156:157], v209 offset:28672\n"
"v_mfma_f32_16x16x32_bf16 v[164:167], a[140:143], a[84:87], v[164:167]\n"
"ds_read_b64_tr_b16 a[158:159], v209 offset:31744\n"
"s_add_u32 m0, s82, 0x15000\n"
"v_mfma_f32_16x16x32_bf16 v[168:171], a[140:143], a[88:91], v[168:171]\n"
"buffer_load_dwordx4 v217, s[56:59], s79 offen lds\n"
"v_mfma_f32_16x16x32_bf16 v[172:175], a[140:143], a[92:95], v[172:175]\n"
"s_waitcnt lgkmcnt(6)\n"
"v_mfma_f32_16x16x32_bf16 v[188:191], a[144:147], a[92:95], v[188:191]\n"
"ds_read_b64_tr_b16 a[128:129], v208 offset:28736\n"
"v_mfma_f32_16x16x32_bf16 v[184:187], a[144:147], a[88:91], v[184:187]\n"
"ds_read_b64_tr_b16 a[130:131], v208 offset:31808\n"
"v_mfma_f32_16x16x32_bf16 v[180:183], a[144:147], a[84:87], v[180:183]\n"
"v_mfma_f32_16x16x32_bf16 v[176:179], a[144:147], a[80:83], v[176:179]\n"
"s_waitcnt lgkmcnt(6)\n"
"v_mfma_f32_16x16x32_bf16 v[192:195], a[148:151], a[80:83], v[192:195]\n"
"ds_read_b64_tr_b16 a[132:133], v209 offset:28736\n"
"v_mfma_f32_16x16x32_bf16 v[196:199], a[148:151], a[84:87], v[196:199]\n"
"ds_read_b64_tr_b16 a[134:135], v209 offset:31808\n"
"s_add_u32 m0, s82, 0x15400\n"
"v_mfma_f32_16x16x32_bf16 v[200:203], a[148:151], a[88:91], v[200:203]\n"
"buffer_load_dwordx4 v218, s[56:59], s79 offen lds\n"
"v_mfma_f32_16x16x32_bf16 v[204:207], a[148:151], a[92:95], v[204:207]\n"
"s_waitcnt vmcnt(17) lgkmcnt(6)\n"
"v_mfma_f32_16x16x32_bf16 v[0:3], a[152:155], a[96:99], v[0:3]\n"
"ds_read_b64_tr_b16 a[136:137], v208 offset:28800\n"
"v_mfma_f32_16x16x32_bf16 v[4:7], a[152:155], a[100:103], v[4:7]\n"
"ds_read_b64_tr_b16 a[138:139], v208 offset:31872\n"
"v_mfma_f32_16x16x32_bf16 v[8:11], a[152:155], a[104:107], v[8:11]\n"
"v_mfma_f32_16x16x32_bf16 v[12:15], a[152:155], a[108:111], v[12:15]\n"
"s_waitcnt lgkmcnt(6)\n"
"v_mfma_f32_16x16x32_bf16 v[28:31], a[156:159], a[108:111], v[28:31]\n"
"ds_read_b64_tr_b16 a[140:141], v209 offset:28800\n"
"v_mfma_f32_16x16x32_bf16 v[24:27], a[156:159], a[104:107], v[24:27]\n"
"ds_read_b64_tr_b16 a[142:143], v209 offset:31872\n"
"s_add_u32 m0, s82, 0x15800\n"
"v_mfma_f32_16x16x32_bf16 v[20:23], a[156:159], a[100:103], v[20:23]\n"
"buffer_load_dwordx4 v219, s[56:59], s79 offen lds\n"
"v_mfma_f32_16x16x32_bf16 v[16:19], a[156:159], a[96:99], v[16:19]\n"
"s_add_u32 s79, s79, 0x60000\n"
"s_waitcnt lgkmcnt(6)\n"
"v_mfma_f32_16x16x32_bf16 v[32:35], a[128:131], a[96:99], v[32:35]\n"
"ds_read_b64_tr_b16 a[144:145], v210 offset:40960\n"
"v_mfma_f32_16x16x32_bf16 v[36:39], a[128:131], a[100:103], v[36:39]\n"
"ds_read_b64_tr_b16 a[146:147], v210 offset:45056\n"
"v_mfma_f32_16x16x32_bf16 v[40:43], a[128:131], a[104:107], v[40:43]\n"
"v_mfma_f32_16x16x32_bf16 v[44:47], a[128:131], a[108:111], v[44:47]\n"
"s_waitcnt lgkmcnt(6)\n"
"v_mfma_f32_16x16x32_bf16 v[60:63], a[132:135], a[108:111], v[60:63]\n"
"ds_read_b64_tr_b16 a[148:149], v210 offset:40992\n"
"v_mfma_f32_16x16x32_bf16 v[56:59], a[132:135], a[104:107], v[56:59]\n"
"ds_read_b64_tr_b16 a[150:151], v210 offset:45088\n"
"v_mfma_f32_16x16x32_bf16 v[52:55], a[132:135], a[100:103], v[52:55]\n"
"buffer_load_dword a162, v241, s[72:75], s27 offen\n"
"v_mfma_f32_16x16x32_bf16 v[48:51], a[132:135], a[96:99], v[48:51]\n"
"s_mov_b32 s72, s46\n"
"s_and_b32 s73, s47, 0xffff\n"
"s_waitcnt lgkmcnt(6)\n"
"v_mfma_f32_16x16x32_bf16 v[64:67], a[96:99], a[136:139], v[64:67]\n"
"ds_read_b64_tr_b16 a[152:153], v210 offset:41024\n"
"v_mfma_f32_16x16x32_bf16 v[68:71], a[100:103], a[136:139], v[68:71]\n"
"ds_read_b64_tr_b16 a[154:155], v210 offset:45120\n"
"v_mfma_f32_16x16x32_bf16 v[72:75], a[104:107], a[136:139], v[72:75]\n"
"buffer_load_dword a163, v243, s[64:67], 0 offen sc1\n"
"v_mfma_f32_16x16x32_bf16 v[76:79], a[108:111], a[136:139], v[76:79]\n"
".Lhop3_%=:\n"
".Lpad3_%=:\n"
"s_cbranch_execz .Lpad7_%=\n"
"s_mov_b32 s74, 0x700000\n"
"s_waitcnt lgkmcnt(6)\n"
"v_mfma_f32_16x16x32_bf16 v[92:95], a[108:111], a[140:143], v[92:95]\n"
"ds_read_b64_tr_b16 a[156:157], v210 offset:41056\n"
"v_mfma_f32_16x16x32_bf16 v[88:91], a[104:107], a[140:143], v[88:91]\n"
"ds_read_b64_tr_b16 a[158:159], v210 offset:45152\n"
"v_mfma_f32_16x16x32_bf16 v[84:87], a[100:103], a[140:143], v[84:87]\n"
"v_mfma_f32_16x16x32_bf16 v[80:83], a[96:99], a[140:143], v[80:83]\n"
"s_waitcnt lgkmcnt(6)\n"
"v_mfma_f32_16x16x32_bf16 v[96:99], a[144:147], a[96:99], v[96:99]\n"
"ds_read_b64_tr_b16 a[128:129], v210 offset:41088\n"
"v_mfma_f32_16x16x32_bf16 v[100:103], a[144:147], a[100:103], v[100:103]\n"
"ds_read_b64_tr_b16 a[130:131], v210 offset:45184\n"
"v_mfma_f32_16x16x32_bf16 v[104:107], a[144:147], a[104:107], v[104:107]\n"
"v_mfma_f32_16x16x32_bf16 v[108:111], a[144:147], a[108:111], v[108:111]\n"
"s_waitcnt lgkmcnt(6)\n"
"v_mfma_f32_16x16x32_bf16 v[124:127], a[148:151], a[108:111], v[124:127]\n"
"ds_read_b64_tr_b16 a[132:133], v210 offset:41120\n"
"v_mfma_f32_16x16x32_bf16 v[120:123], a[148:151], a[104:107], v[120:123]\n"
"ds_read_b64_tr_b16 a[134:135], v210 offset:45216\n"
"v_mfma_f32_16x16x32_bf16 v[116:119], a[148:151], a[100:103], v[116:119]\n"
"v_mfma_f32_16x16x32_bf16 v[112:115], a[148:151], a[96:99], v[112:115]\n"
"s_waitcnt lgkmcnt(6)\n"
"v_mfma_f32_16x16x32_bf16 v[128:131], a[152:155], a[96:99], v[128:131]\n"
"ds_read_b64_tr_b16 a[136:137], v210 offset:41152\n"
"v_mfma_f32_16x16x32_bf16 v[132:135], a[152:155], a[100:103], v[132:135]\n"
"ds_read_b64_tr_b16 a[138:139], v210 offset:45248\n"
"s_add_u32 m0, s83, 0x15000\n"
"v_mfma_f32_16x16x32_bf16 v[136:139], a[152:155], a[104:107], v[136:139]\n"
"buffer_load_dwordx4 v220, s[60:63], s80 offen lds\n"
"v_mfma_f32_16x16x32_bf16 v[140:143], a[152:155], a[108:111], v[140:143]\n"
"s_waitcnt lgkmcnt(6)\n"
"v_mfma_f32_16x16x32_bf16 v[156:159], a[156:159], a[108:111], v[156:159]\n"
"ds_read_b64_tr_b16 a[140:141], v208 offset:34816\n"
"v_mfma_f32_16x16x32_bf16 v[152:155], a[156:159], a[104:107], v[152:155]\n"
"ds_read_b64_tr_b16 a[142:143], v208 offset:37888\n"
"s_add_u32 m0, s83, 0x15400\n"
"v_mfma_f32_16x16x32_bf16 v[148:151], a[156:159], a[100:103], v[148:151]\n"
"buffer_load_dwordx4 v221, s[60:63], s80 offen lds\n"
"v_mfma_f32_16x16x32_bf16 v[144:147], a[156:159], a[96:99], v[144:147]\n"
"s_waitcnt lgkmcnt(6)\n"
"v_mfma_f32_16x16x32_bf16 v[160:163], a[128:131], a[96:99], v[160:163]\n"
"ds_read_b64_tr_b16 a[144:145], v209 offset:34816\n"
"v_mfma_f32_16x16x32_bf16 v[164:167], a[128:131], a[100:103], v[164:167]\n"
"ds_read_b64_tr_b16 a[146:147], v209 offset:37888\n"
"s_add_u32 m0, s83, 0x15800\n"
"v_mfma_f32_16x16x32_bf16 v[168:171], a[128:131], a[104:107], v[168:171]\n"
"buffer_load_dwordx4 v222, s[60:63], s80 offen lds\n"
"v_mfma_f32_16x16x32_bf16 v[172:175], a[128:131], a[108:111], v[172:175]\n"
"s_waitcnt lgkmcnt(6)\n"
"v_mfma_f32_16x16x32_bf16 v[188:191], a[132:135], a[108:111], v[188:191]\n"
"ds_read_b64_tr_b16 a[148:149], v208 offset:34880\n"
"v_mfma_f32_16x16x32_bf16 v[184:187], a[132:135], a[104:107], v[184:187]\n"
"ds_read_b64_tr_b16 a[150:151], v208 offset:37952\n"
"s_add_u32 m0, s83, 0x15c00\n"
"v_mfma_f32_16x16x32_bf16 v[180:183], a[132:135], a[100:103], v[180:183]\n"
"buffer_load_dwordx4 v223, s[60:63], s80 offen lds\n"
"v_mfma_f32_16x16x32_bf16 v[176:179], a[132:135], a[96:99], v[176:179]\n"
"s_add_u32 s80, s80, 0x70000\n"
"s_waitcnt lgkmcnt(6)\n"
"v_mfma_f32_16x16x32_bf16 v[192:195], a[136:139], a[96:99], v[192:195]\n"
"ds_read_b64_tr_b16 a[152:153], v209 offset:34880\n"
"v_mfma_f32_16x16x32_bf16 v[196:199], a[136:139], a[100:103], v[196:199]\n"
"ds_read_b64_tr_b16 a[154:155], v209 offset:37952\n"
"v_mfma_f32_16x16x32_bf16 v[200:203], a[136:139], a[104:107], v[200:203]\n"
"buffer_load_dwordx4 a[16:19], v224, s[64:67], s81 offen sc1\n"
"v_mfma_f32_16x16x32_bf16 v[204:207], a[136:139], a[108:111], v[204:207]\n"
"s_waitcnt vmcnt(14) lgkmcnt(6)\n"
"v_mfma_f32_16x16x32_bf16 v[0:3], a[140:143], a[112:115], v[0:3]\n"
"ds_read_b64_tr_b16 a[156:157], v208 offset:34944\n"
"v_mfma_f32_16x16x32_bf16 v[4:7], a[140:143], a[116:119], v[4:7]\n"
"ds_read_b64_tr_b16 a[158:159], v208 offset:38016\n"
"v_mfma_f32_16x16x32_bf16 v[8:11], a[140:143], a[120:123], v[8:11]\n"
"v_mfma_f32_16x16x32_bf16 v[12:15], a[140:143], a[124:127], v[12:15]\n"
"s_waitcnt lgkmcnt(6)\n"
"v_mfma_f32_16x16x32_bf16 v[28:31], a[144:147], a[124:127], v[28:31]\n"
"ds_read_b64_tr_b16 a[128:129], v209 offset:34944\n"
"v_mfma_f32_16x16x32_bf16 v[24:27], a[144:147], a[120:123], v[24:27]\n"
"ds_read_b64_tr_b16 a[130:131], v209 offset:38016\n"
"v_mfma_f32_16x16x32_bf16 v[20:23], a[144:147], a[116:119], v[20:23]\n"
"buffer_load_dwordx4 a[20:23], v224, s[64:67], s81 offen offset:1024 sc1\n"
"v_mfma_f32_16x16x32_bf16 v[16:19], a[144:147], a[112:115], v[16:19]\n"
"s_waitcnt lgkmcnt(6)\n"
"v_mfma_f32_16x16x32_bf16 v[32:35], a[148:151], a[112:115], v[32:35]\n"
"ds_read_b64_tr_b16 a[132:133], v210 offset:49152\n"
"v_mfma_f32_16x16x32_bf16 v[36:39], a[148:151], a[116:119], v[36:39]\n"
"ds_read_b64_tr_b16 a[134:135], v210 offset:53248\n"
"v_mfma_f32_16x16x32_bf16 v[40:43], a[148:151], a[120:123], v[40:43]\n"
"buffer_load_dwordx4 a[24:27], v224, s[64:67], s81 offen offset:2048 sc1\n"
"v_mfma_f32_16x16x32_bf16 v[44:47], a[148:151], a[124:127], v[44:47]\n"
"s_waitcnt lgkmcnt(6)\n"
"v_mfma_f32_16x16x32_bf16 v[60:63], a[152:155], a[124:127], v[60:63]\n"
"ds_read_b64_tr_b16 a[136:137], v210 offset:49184\n"
"v_mfma_f32_16x16x32_bf16 v[56:59], a[152:155], a[120:123], v[56:59]\n"
"ds_read_b64_tr_b16 a[138:139], v210 offset:53280\n"
"v_mfma_f32_16x16x32_bf16 v[52:55], a[152:155], a[116:119], v[52:55]\n"
"buffer_load_dwordx4 a[28:31], v224, s[64:67], s81 offen offset:3072 sc1\n"
"v_mfma_f32_16x16x32_bf16 v[48:51], a[152:155], a[112:115], v[48:51]\n"
"s_add_u32 s81, s81, 0x1000\n"
"s_waitcnt lgkmcnt(6)\n"
"v_mfma_f32_16x16x32_bf16 v[64:67], a[112:115], a[156:159], v[64:67]\n"
"ds_read_b64_tr_b16 a[140:141], v210 offset:49216\n"
"v_mfma_f32_16x16x32_bf16 v[68:71], a[116:119], a[156:159], v[68:71]\n"
"ds_read_b64_tr_b16 a[142:143], v210 offset:53312\n"
"v_mfma_f32_16x16x32_bf16 v[72:75], a[120:123], a[156:159], v[72:75]\n"
"buffer_load_dwordx4 a[32:35], v224, s[64:67], s81 offen sc1\n"
"v_mfma_f32_16x16x32_bf16 v[76:79], a[124:127], a[156:159], v[76:79]\n"
"s_waitcnt lgkmcnt(6)\n"
"v_mfma_f32_16x16x32_bf16 v[92:95], a[124:127], a[128:131], v[92:95]\n"
"ds_read_b64_tr_b16 a[144:145], v210 offset:49248\n"
"v_mfma_f32_16x16x32_bf16 v[88:91], a[120:123], a[128:131], v[88:91]\n"
"ds_read_b64_tr_b16 a[146:147], v210 offset:53344\n"
"v_mfma_f32_16x16x32_bf16 v[84:87], a[116:119], a[128:131], v[84:87]\n"
"buffer_load_dwordx4 a[36:39], v224, s[64:67], s81 offen offset:1024 sc1\n"
"v_mfma_f32_16x16x32_bf16 v[80:83], a[112:115], a[128:131], v[80:83]\n"
"s_waitcnt lgkmcnt(6)\n"
"v_mfma_f32_16x16x32_bf16 v[96:99], a[132:135], a[112:115], v[96:99]\n"
"ds_read_b64_tr_b16 a[148:149], v210 offset:49280\n"
"v_mfma_f32_16x16x32_bf16 v[100:103], a[132:135], a[116:119], v[100:103]\n"
"ds_read_b64_tr_b16 a[150:151], v210 offset:53376\n"
"v_mfma_f32_16x16x32_bf16 v[104:107], a[132:135], a[120:123], v[104:107]\n"
"buffer_load_dwordx4 a[40:43], v224, s[64:67], s81 offen offset:2048 sc1\n"
"v_mfma_f32_16x16x32_bf16 v[108:111], a[132:135], a[124:127], v[108:111]\n"
"s_waitcnt lgkmcnt(6)\n"
"v_mfma_f32_16x16x32_bf16 v[124:127], a[136:139], a[124:127], v[124:127]\n"
"ds_read_b64_tr_b16 a[152:153], v210 offset:49312\n"
"v_mfma_f32_16x16x32_bf16 v[120:123], a[136:139], a[120:123], v[120:123]\n"
"ds_read_b64_tr_b16 a[154:155], v210 offset:53408\n"
"v_mfma_f32_16x16x32_bf16 v[116:119], a[136:139], a[116:119], v[116:119]\n"
"buffer_load_dwordx4 a[44:47], v224, s[64:67], s81 offen offset:3072 sc1\n"
"v_mfma_f32_16x16x32_bf16 v[112:115], a[136:139], a[112:115], v[112:115]\n"
"s_add_u32 s81, s81, 0x1000\n"
"s_waitcnt lgkmcnt(6)\n"
"v_mfma_f32_16x16x32_bf16 v[128:131], a[140:143], a[112:115], v[128:131]\n"
"ds_read_b64_tr_b16 a[156:157], v210 offset:49344\n"
"v_mfma_f32_16x16x32_bf16 v[132:135], a[140:143], a[116:119], v[132:135]\n"
"ds_read_b64_tr_b16 a[158:159], v210 offset:53440\n"
"v_mfma_f32_16x16x32_bf16 v[136:139], a[140:143], a[120:123], v[136:139]\n"
"v_mfma_f32_16x16x32_bf16 v[140:143], a[140:143], a[124:127], v[140:143]\n"
"s_barrier\n"
"s_waitcnt lgkmcnt(6)\n"
"v_mfma_f32_16x16x32_bf16 v[156:159], a[144:147], a[124:127], v[156:159]\n"
"ds_read_b64_tr_b16 a[128:129], v211\n"
"v_mfma_f32_16x16x32_bf16 v[152:155], a[144:147], a[120:123], v[152:155]\n"
"ds_read_b64_tr_b16 a[130:131], v211 offset:3072\n"
"v_mfma_f32_16x16x32_bf16 v[148:151], a[144:147], a[116:119], v[148:151]\n"
"v_mfma_f32_16x16x32_bf16 v[144:147], a[144:147], a[112:115], v[144:147]\n"
"s_waitcnt lgkmcnt(6)\n"
"v_mfma_f32_16x16x32_bf16 v[160:163], a[148:151], a[112:115], v[160:163]\n"
"ds_read_b64_tr_b16 a[132:133], v212\n"
"v_mfma_f32_16x16x32_bf16 v[164:167], a[148:151], a[116:119], v[164:167]\n"
"ds_read_b64_tr_b16 a[134:135], v212 offset:3072\n"
"s_add_u32 m0, s82, 0x1c000\n"
"v_mfma_f32_16x16x32_bf16 v[168:171], a[148:151], a[120:123], v[168:171]\n"
"buffer_load_dwordx4 v217, s[56:59], s79 offen lds\n"
"v_mfma_f32_16x16x32_bf16 v[172:175], a[148:151], a[124:127], v[172:175]\n"
"s_waitcnt lgkmcnt(6)\n"
"v_mfma_f32_16x16x32_bf16 v[188:191], a[152:155], a[124:127], v[188:191]\n"
"ds_read_b64_tr_b16 a[136:137], v211 offset:64\n"
"v_mfma_f32_16x16x32_bf16 v[184:187], a[152:155], a[120:123], v[184:187]\n"
"ds_read_b64_tr_b16 a[138:139], v211 offset:3136\n"
"v_mfma_f32_16x16x32_bf16 v[180:183], a[152:155], a[116:119], v[180:183]\n"
"v_mfma_f32_16x16x32_bf16 v[176:179], a[152:155], a[112:115], v[176:179]\n"
"s_waitcnt lgkmcnt(6)\n"
"v_mfma_f32_16x16x32_bf16 v[192:195], a[156:159], a[112:115], v[192:195]\n"
"ds_read_b64_tr_b16 a[140:141], v212 offset:64\n"
"v_mfma_f32_16x16x32_bf16 v[196:199], a[156:159], a[116:119], v[196:199]\n"
"ds_read_b64_tr_b16 a[142:143], v212 offset:3136\n"
"s_add_u32 m0, s82, 0x1c400\n"
"v_mfma_f32_16x16x32_bf16 v[200:203], a[156:159], a[120:123], v[200:203]\n"
"buffer_load_dwordx4 v218, s[56:59], s79 offen lds\n"
"v_mfma_f32_16x16x32_bf16 v[204:207], a[156:159], a[124:127], v[204:207]\n"
"s_waitcnt vmcnt(19) lgkmcnt(6)\n"
"v_mfma_f32_16x16x32_bf16 v[0:3], a[128:131], a[0:3], v[0:3]\n"
"ds_read_b64_tr_b16 a[144:145], v211 offset:128\n"
"v_mfma_f32_16x16x32_bf16 v[4:7], a[128:131], a[4:7], v[4:7]\n"
"ds_read_b64_tr_b16 a[146:147], v211 offset:3200\n"
"v_mfma_f32_16x16x32_bf16 v[8:11], a[128:131], a[8:11], v[8:11]\n"
"v_mfma_f32_16x16x32_bf16 v[12:15], a[128:131], a[12:15], v[12:15]\n"
"s_waitcnt lgkmcnt(6)\n"
"v_mfma_f32_16x16x32_bf16 v[28:31], a[132:135], a[12:15], v[28:31]\n"
"ds_read_b64_tr_b16 a[148:149], v212 offset:128\n"
"v_mfma_f32_16x16x32_bf16 v[24:27], a[132:135], a[8:11], v[24:27]\n"
"ds_read_b64_tr_b16 a[150:151], v212 offset:3200\n"
"s_add_u32 m0, s82, 0x1c800\n"
"v_mfma_f32_16x16x32_bf16 v[20:23], a[132:135], a[4:7], v[20:23]\n"
"buffer_load_dwordx4 v219, s[56:59], s79 offen lds\n"
"v_mfma_f32_16x16x32_bf16 v[16:19], a[132:135], a[0:3], v[16:19]\n"
"s_add_u32 s79, s79, 0x60000\n"
"s_waitcnt lgkmcnt(6)\n"
"v_mfma_f32_16x16x32_bf16 v[32:35], a[136:139], a[0:3], v[32:35]\n"
"ds_read_b64_tr_b16 a[152:153], v213 offset:12288\n"
"v_mfma_f32_16x16x32_bf16 v[36:39], a[136:139], a[4:7], v[36:39]\n"
"ds_read_b64_tr_b16 a[154:155], v213 offset:16384\n"
"v_mfma_f32_16x16x32_bf16 v[40:43], a[136:139], a[8:11], v[40:43]\n"
"v_mfma_f32_16x16x32_bf16 v[44:47], a[136:139], a[12:15], v[44:47]\n"
"s_waitcnt lgkmcnt(6)\n"
"v_mfma_f32_16x16x32_bf16 v[60:63], a[140:143], a[12:15], v[60:63]\n"
"ds_read_b64_tr_b16 a[156:157], v213 offset:12320\n"
"v_mfma_f32_16x16x32_bf16 v[56:59], a[140:143], a[8:11], v[56:59]\n"
"ds_read_b64_tr_b16 a[158:159], v213 offset:16416\n"
"s_add_u32 m0, s83, 0x1c000\n"
"v_mfma_f32_16x16x32_bf16 v[52:55], a[140:143], a[4:7], v[52:55]\n"
"buffer_load_dwordx4 v220, s[60:63], s80 offen lds\n"
"v_mfma_f32_16x16x32_bf16 v[48:51], a[140:143], a[0:3], v[48:51]\n"
"s_waitcnt lgkmcnt(6)\n"
"v_mfma_f32_16x16x32_bf16 v[64:67], a[0:3], a[144:147], v[64:67]\n"
"ds_read_b64_tr_b16 a[128:129], v213 offset:12352\n"
"v_mfma_f32_16x16x32_bf16 v[68:71], a[4:7], a[144:147], v[68:71]\n"
"ds_read_b64_tr_b16 a[130:131], v213 offset:16448\n"
"s_add_u32 m0, s83, 0x1c400\n"
"v_mfma_f32_16x16x32_bf16 v[72:75], a[8:11], a[144:147], v[72:75]\n"
"buffer_load_dwordx4 v221, s[60:63], s80 offen lds\n"
"v_mfma_f32_16x16x32_bf16 v[76:79], a[12:15], a[144:147], v[76:79]\n"
"s_waitcnt lgkmcnt(6)\n"
"v_mfma_f32_16x16x32_bf16 v[92:95], a[12:15], a[148:151], v[92:95]\n"
"ds_read_b64_tr_b16 a[132:133], v213 offset:12384\n"
"v_mfma_f32_16x16x32_bf16 v[88:91], a[8:11], a[148:151], v[88:91]\n"
"ds_read_b64_tr_b16 a[134:135], v213 offset:16480\n"
"v_mfma_f32_16x16x32_bf16 v[84:87], a[4:7], a[148:151], v[84:87]\n"
"v_mfma_f32_16x16x32_bf16 v[80:83], a[0:3], a[148:151], v[80:83]\n"
"s_waitcnt lgkmcnt(6)\n"
"v_mfma_f32_16x16x32_bf16 v[96:99], a[152:155], a[0:3], v[96:99]\n"
"ds_read_b64_tr_b16 a[136:137], v213 offset:12416\n"
"v_mfma_f32_16x16x32_bf16 v[100:103], a[152:155], a[4:7], v[100:103]\n"
"ds_read_b64_tr_b16 a[138:139], v213 offset:16512\n"
"s_add_u32 m0, s83, 0x1c800\n"
"v_mfma_f32_16x16x32_bf16 v[104:107], a[152:155], a[8:11], v[104:107]\n"
"buffer_load_dwordx4 v222, s[60:63], s80 offen lds\n"
"v_mfma_f32_16x16x32_bf16 v[108:111], a[152:155], a[12:15], v[108:111]\n"
"s_waitcnt lgkmcnt(6)\n"
"v_mfma_f32_16x16x32_bf16 v[124:127], a[156:159], a[12:15], v[124:127]\n"
"ds_read_b64_tr_b16 a[140:141], v213 offset:12448\n"
"v_mfma_f32_16x16x32_bf16 v[120:123], a[156:159], a[8:11], v[120:123]\n"
"ds_read_b64_tr_b16 a[142:143], v213 offset:16544\n"
"s_add_u32 m0, s83, 0x1cc00\n"
"v_mfma_f32_16x16x32_bf16 v[116:119], a[156:159], a[4:7], v[116:119]\n"
"buffer_load_dwordx4 v223, s[60:63], s80 offen lds\n"
"v_mfma_f32_16x16x32_bf16 v[112:115], a[156:159], a[0:3], v[112:115]\n"
"s_add_u32 s80, s80, 0x70000\n"
"s_waitcnt lgkmcnt(6)\n"
"v_mfma_f32_16x16x32_bf16 v[128:131], a[128:131], a[0:3], v[128:131]\n"
"ds_read_b64_tr_b16 a[144:145], v213 offset:12480\n"
"v_mfma_f32_16x16x32_bf16 v[132:135], a[128:131], a[4:7], v[132:135]\n"
"ds_read_b64_tr_b16 a[146:147], v213 offset:16576\n"
"v_mfma_f32_16x16x32_bf16 v[136:139], a[128:131], a[8:11], v[136:139]\n"
"buffer_load_dwordx4 a[48:51], v224, s[64:67], s81 offen sc1\n"
"v_mfma_f32_16x16x32_bf16 v[140:143], a[128:131], a[12:15], v[140:143]\n"
"s_waitcnt lgkmcnt(6)\n"
"v_mfma_f32_16x16x32_bf16 v[156:159], a[132:135], a[12:15], v[156:159]\n"
"ds_read_b64_tr_b16 a[148:149], v211 offset:6144\n"
"v_mfma_f32_16x16x32_bf16 v[152:155], a[132:135], a[8:11], v[152:155]\n"
"ds_read_b64_tr_b16 a[150:151], v211 offset:9216\n"
"v_mfma_f32_16x16x32_bf16 v[148:151], a[132:135], a[4:7], v[148:151]\n"
"v_mfma_f32_16x16x32_bf16 v[144:147], a[132:135], a[0:3], v[144:147]\n"
"s_waitcnt lgkmcnt(6)\n"
"v_mfma_f32_16x16x32_bf16 v[160:163], a[136:139], a[0:3], v[160:163]\n"
"ds_read_b64_tr_b16 a[152:153], v212 offset:6144\n"
"v_mfma_f32_16x16x32_bf16 v[164:167], a[136:139], a[4:7], v[164:167]\n"
"ds_read_b64_tr_b16 a[154:155], v212 offset:9216\n"
"v_mfma_f32_16x16x32_bf16 v[168:171], a[136:139], a[8:11], v[168:171]\n"
"buffer_load_dwordx4 a[52:55], v224, s[64:67], s81 offen offset:1024 sc1\n"
"v_mfma_f32_16x16x32_bf16 v[172:175], a[136:139], a[12:15], v[172:175]\n"
"s_waitcnt lgkmcnt(6)\n"
"v_mfma_f32_16x16x32_bf16 v[188:191], a[140:143], a[12:15], v[188:191]\n"
"ds_read_b64_tr_b16 a[156:157], v211 offset:6208\n"
"v_mfma_f32_16x16x32_bf16 v[184:187], a[140:143], a[8:11], v[184:187]\n"
"ds_read_b64_tr_b16 a[158:159], v211 offset:9280\n"
"v_mfma_f32_16x16x32_bf16 v[180:183], a[140:143], a[4:7], v[180:183]\n"
"buffer_load_dwordx4 a[56:59], v224, s[64:67], s81 offen offset:2048 sc1\n"
"v_mfma_f32_16x16x32_bf16 v[176:179], a[140:143], a[0:3], v[176:179]\n"
"s_waitcnt lgkmcnt(6)\n"
"v_mfma_f32_16x16x32_bf16 v[192:195], a[144:147], a[0:3], v[192:195]\n"
"ds_read_b64_tr_b16 a[128:129], v212 offset:6208\n"
"v_mfma_f32_16x16x32_bf16 v[196:199], a[144:147], a[4:7], v[196:199]\n"
"ds_read_b64_tr_b16 a[130:131], v212 offset:9280\n"
"v_mfma_f32_16x16x32_bf16 v[200:203], a[144:147], a[8:11], v[200:203]\n"
"buffer_load_dwordx4 a[60:63], v224, s[64:67], s81 offen offset:3072 sc1\n"
"v_mfma_f32_16x16x32_bf16 v[204:207], a[144:147], a[12:15], v[204:207]\n"
"s_add_u32 s81, s81, 0x1000\n"
"s_waitcnt vmcnt(15) lgkmcnt(6)\n"
"v_mfma_f32_16x16x32_bf16 v[0:3], a[148:151], a[16:19], v[0:3]\n"
"ds_read_b64_tr_b16 a[132:133], v211 offset:6272\n"
"v_mfma_f32_16x16x32_bf16 v[4:7], a[148:151], a[20:23], v[4:7]\n"
"ds_read_b64_tr_b16 a[134:135], v211 offset:9344\n"
"v_mfma_f32_16x16x32_bf16 v[8:11], a[148:151], a[24:27], v[8:11]\n"
"buffer_load_dwordx4 a[64:67], v224, s[64:67], s81 offen sc1\n"
"v_mfma_f32_16x16x32_bf16 v[12:15], a[148:151], a[28:31], v[12:15]\n"
"s_waitcnt lgkmcnt(6)\n"
"v_mfma_f32_16x16x32_bf16 v[28:31], a[152:155], a[28:31], v[28:31]\n"
"ds_read_b64_tr_b16 a[136:137], v212 offset:6272\n"
"v_mfma_f32_16x16x32_bf16 v[24:27], a[152:155], a[24:27], v[24:27]\n"
"ds_read_b64_tr_b16 a[138:139], v212 offset:9344\n"
"v_mfma_f32_16x16x32_bf16 v[20:23], a[152:155], a[20:23], v[20:23]\n"
"buffer_load_dwordx4 a[68:71], v224, s[64:67], s81 offen offset:1024 sc1\n"
"v_mfma_f32_16x16x32_bf16 v[16:19], a[152:155], a[16:19], v[16:19]\n"
"s_waitcnt lgkmcnt(6)\n"
"v_mfma_f32_16x16x32_bf16 v[32:35], a[156:159], a[16:19], v[32:35]\n"
"ds_read_b64_tr_b16 a[140:141], v213 offset:20480\n"
"v_mfma_f32_16x16x32_bf16 v[36:39], a[156:159], a[20:23], v[36:39]\n"
"ds_read_b64_tr_b16 a[142:143], v213 offset:24576\n"
"v_mfma_f32_16x16x32_bf16 v[40:43], a[156:159], a[24:27], v[40:43]\n"
"buffer_load_dwordx4 a[72:75], v224, s[64:67], s81 offen offset:2048 sc1\n"
"v_mfma_f32_16x16x32_bf16 v[44:47], a[156:159], a[28:31], v[44:47]\n"
"s_waitcnt lgkmcnt(6)\n"
"v_mfma_f32_16x16x32_bf16 v[60:63], a[128:131], a[28:31], v[60:63]\n"
"ds_read_b64_tr_b16 a[144:145], v213 offset:20512\n"
"v_mfma_f32_16x16x32_bf16 v[56:59], a[128:131], a[24:27], v[56:59]\n"
"ds_read_b64_tr_b16 a[146:147], v213 offset:24608\n"
"v_mfma_f32_16x16x32_bf16 v[52:55], a[128:131], a[20:23], v[52:55]\n"
"buffer_load_dwordx4 a[76:79], v224, s[64:67], s81 offen offset:3072 sc1\n"
"v_mfma_f32_16x16x32_bf16 v[48:51], a[128:131], a[16:19], v[48:51]\n"
"s_add_u32 s81, s81, 0x1000\n"
"s_waitcnt lgkmcnt(6)\n"
"v_mfma_f32_16x16x32_bf16 v[64:67], a[16:19], a[132:135], v[64:67]\n"
"ds_read_b64_tr_b16 a[148:149], v213 offset:20544\n"
"v_mfma_f32_16x16x32_bf16 v[68:71], a[20:23], a[132:135], v[68:71]\n"
"ds_read_b64_tr_b16 a[150:151], v213 offset:24640\n"
"v_mfma_f32_16x16x32_bf16 v[72:75], a[24:27], a[132:135], v[72:75]\n"
"buffer_load_dword a160, v240, s[72:75], s26 offen\n"
"v_mfma_f32_16x16x32_bf16 v[76:79], a[28:31], a[132:135], v[76:79]\n"
"s_add_u32 s26, s26, 0x2000\n"
"s_waitcnt lgkmcnt(6)\n"
"v_mfma_f32_16x16x32_bf16 v[92:95], a[28:31], a[136:139], v[92:95]\n"
"ds_read_b64_tr_b16 a[152:153], v213 offset:20576\n"
"v_mfma_f32_16x16x32_bf16 v[88:91], a[24:27], a[136:139], v[88:91]\n"
"ds_read_b64_tr_b16 a[154:155], v213 offset:24672\n"
"v_mfma_f32_16x16x32_bf16 v[84:87], a[20:23], a[136:139], v[84:87]\n"
"v_mfma_f32_16x16x32_bf16 v[80:83], a[16:19], a[136:139], v[80:83]\n"
"s_waitcnt lgkmcnt(6)\n"
"v_mfma_f32_16x16x32_bf16 v[96:99], a[140:143], a[16:19], v[96:99]\n"
"ds_read_b64_tr_b16 a[156:157], v213 offset:20608\n"
"v_mfma_f32_16x16x32_bf16 v[100:103], a[140:143], a[20:23], v[100:103]\n"
"ds_read_b64_tr_b16 a[158:159], v213 offset:24704\n"
"v_mfma_f32_16x16x32_bf16 v[104:107], a[140:143], a[24:27], v[104:107]\n"
"v_mfma_f32_16x16x32_bf16 v[108:111], a[140:143], a[28:31], v[108:111]\n"
"s_waitcnt lgkmcnt(6)\n"
"v_mfma_f32_16x16x32_bf16 v[124:127], a[144:147], a[28:31], v[124:127]\n"
"ds_read_b64_tr_b16 a[128:129], v213 offset:20640\n"
"v_mfma_f32_16x16x32_bf16 v[120:123], a[144:147], a[24:27], v[120:123]\n"
"ds_read_b64_tr_b16 a[130:131], v213 offset:24736\n"
"v_mfma_f32_16x16x32_bf16 v[116:119], a[144:147], a[20:23], v[116:119]\n"
"v_mfma_f32_16x16x32_bf16 v[112:115], a[144:147], a[16:19], v[112:115]\n"
"s_waitcnt lgkmcnt(6)\n"
"v_mfma_f32_16x16x32_bf16 v[128:131], a[148:151], a[16:19], v[128:131]\n"
"ds_read_b64_tr_b16 a[132:133], v213 offset:20672\n"
"v_mfma_f32_16x16x32_bf16 v[132:135], a[148:151], a[20:23], v[132:135]\n"
"ds_read_b64_tr_b16 a[134:135], v213 offset:24768\n"
"v_mfma_f32_16x16x32_bf16 v[136:139], a[148:151], a[24:27], v[136:139]\n"
"v_mfma_f32_16x16x32_bf16 v[140:143], a[148:151], a[28:31], v[140:143]\n"
"s_barrier\n"
"s_waitcnt lgkmcnt(6)\n"
"v_mfma_f32_16x16x32_bf16 v[156:159], a[152:155], a[28:31], v[156:159]\n"
"ds_read_b64_tr_b16 a[136:137], v211 offset:28672\n"
"v_mfma_f32_16x16x32_bf16 v[152:155], a[152:155], a[24:27], v[152:155]\n"
"ds_read_b64_tr_b16 a[138:139], v211 offset:31744\n"
"v_mfma_f32_16x16x32_bf16 v[148:151], a[152:155], a[20:23], v[148:151]\n"
"v_mfma_f32_16x16x32_bf16 v[144:147], a[152:155], a[16:19], v[144:147]\n"
"s_waitcnt lgkmcnt(6)\n"
"v_mfma_f32_16x16x32_bf16 v[160:163], a[156:159], a[16:19], v[160:163]\n"
"ds_read_b64_tr_b16 a[140:141], v212 offset:28672\n"
"v_mfma_f32_16x16x32_bf16 v[164:167], a[156:159], a[20:23], v[164:167]\n"
"ds_read_b64_tr_b16 a[142:143], v212 offset:31744\n"
"s_add_u32 m0, s82, 0\n"
"v_mfma_f32_16x16x32_bf16 v[168:171], a[156:159], a[24:27], v[168:171]\n"
"buffer_load_dwordx4 v217, s[56:59], s79 offen lds\n"
"v_mfma_f32_16x16x32_bf16 v[172:175], a[156:159], a[28:31], v[172:175]\n"
"s_waitcnt lgkmcnt(6)\n"
"v_mfma_f32_16x16x32_bf16 v[188:191], a[128:131], a[28:31], v[188:191]\n"
"ds_read_b64_tr_b16 a[144:145], v211 offset:28736\n"
"v_mfma_f32_16x16x32_bf16 v[184:187], a[128:131], a[24:27], v[184:187]\n"
"ds_read_b64_tr_b16 a[146:147], v211 offset:31808\n"
"v_mfma_f32_16x16x32_bf16 v[180:183], a[128:131], a[20:23], v[180:183]\n"
"v_mfma_f32_16x16x32_bf16 v[176:179], a[128:131], a[16:19], v[176:179]\n"
"s_waitcnt lgkmcnt(6)\n"
"v_mfma_f32_16x16x32_bf16 v[192:195], a[132:135], a[16:19], v[192:195]\n"
"ds_read_b64_tr_b16 a[148:149], v212 offset:28736\n"
"v_mfma_f32_16x16x32_bf16 v[196:199], a[132:135], a[20:23], v[196:199]\n"
"ds_read_b64_tr_b16 a[150:151], v212 offset:31808\n"
"s_add_u32 m0, s82, 0x400\n"
"v_mfma_f32_16x16x32_bf16 v[200:203], a[132:135], a[24:27], v[200:203]\n"
"buffer_load_dwordx4 v218, s[56:59], s79 offen lds\n"
"v_mfma_f32_16x16x32_bf16 v[204:207], a[132:135], a[28:31], v[204:207]\n"
"s_waitcnt vmcnt(18) lgkmcnt(6)\n"
"v_mfma_f32_16x16x32_bf16 v[0:3], a[136:139], a[32:35], v[0:3]\n"
"ds_read_b64_tr_b16 a[152:153], v211 offset:28800\n"
"v_mfma_f32_16x16x32_bf16 v[4:7], a[136:139], a[36:39], v[4:7]\n"
"ds_read_b64_tr_b16 a[154:155], v211 offset:31872\n"
"v_mfma_f32_16x16x32_bf16 v[8:11], a[136:139], a[40:43], v[8:11]\n"
"v_mfma_f32_16x16x32_bf16 v[12:15], a[136:139], a[44:47], v[12:15]\n"
"s_waitcnt lgkmcnt(6)\n"
"v_mfma_f32_16x16x32_bf16 v[28:31], a[140:143], a[44:47], v[28:31]\n"
"ds_read_b64_tr_b16 a[156:157], v212 offset:28800\n"
"v_mfma_f32_16x16x32_bf16 v[24:27], a[140:143], a[40:43], v[24:27]\n"
"ds_read_b64_tr_b16 a[158:159], v212 offset:31872\n"
"s_add_u32 m0, s82, 0x800\n"
"v_mfma_f32_16x16x32_bf16 v[20:23], a[140:143], a[36:39], v[20:23]\n"
"buffer_load_dwordx4 v219, s[56:59], s79 offen lds\n"
"v_mfma_f32_16x16x32_bf16 v[16:19], a[140:143], a[32:35], v[16:19]\n"
"s_add_u32 s79, s79, 0x60000\n"
"s_waitcnt lgkmcnt(6)\n"
"v_mfma_f32_16x16x32_bf16 v[32:35], a[144:147], a[32:35], v[32:35]\n"
"ds_read_b64_tr_b16 a[128:129], v213 offset:40960\n"
"v_mfma_f32_16x16x32_bf16 v[36:39], a[144:147], a[36:39], v[36:39]\n"
"ds_read_b64_tr_b16 a[130:131], v213 offset:45056\n"
"s_add_u32 m0, s83, 0\n"
"v_mfma_f32_16x16x32_bf16 v[40:43], a[144:147], a[40:43], v[40:43]\n"
"buffer_load_dwordx4 v220, s[60:63], s80 offen lds\n"
"v_mfma_f32_16x16x32_bf16 v[44:47], a[144:147], a[44:47], v[44:47]\n"
"s_waitcnt lgkmcnt(6)\n"
"v_mfma_f32_16x16x32_bf16 v[60:63], a[148:151], a[44:47], v[60:63]\n"
"ds_read_b64_tr_b16 a[132:133], v213 offset:40992\n"
"v_mfma_f32_16x16x32_bf16 v[56:59], a[148:151], a[40:43], v[56:59]\n"
"ds_read_b64_tr_b16 a[134:135], v213 offset:45088\n"
"v_mfma_f32_16x16x32_bf16 v[52:55], a[148:151], a[36:39], v[52:55]\n"
"v_mfma_f32_16x16x32_bf16 v[48:51], a[148:151], a[32:35], v[48:51]\n"
"s_waitcnt lgkmcnt(6)\n"
"v_mfma_f32_16x16x32_bf16 v[64:67], a[32:35], a[152:155], v[64:67]\n"
"ds_read_b64_tr_b16 a[136:137], v213 offset:41024\n"
"v_mfma_f32_16x16x32_bf16 v[68:71], a[36:39], a[152:155], v[68:71]\n"
"ds_read_b64_tr_b16 a[138:139], v213 offset:45120\n"
"s_add_u32 m0, s83, 0x400\n"
"v_mfma_f32_16x16x32_bf16 v[72:75], a[40:43], a[152:155], v[72:75]\n"
"buffer_load_dwordx4 v221, s[60:63], s80 offen lds\n"
"v_mfma_f32_16x16x32_bf16 v[76:79], a[44:47], a[152:155], v[76:79]\n"
"s_waitcnt lgkmcnt(6)\n"
"v_mfma_f32_16x16x32_bf16 v[92:95], a[44:47], a[156:159], v[92:95]\n"
"ds_read_b64_tr_b16 a[140:141], v213 offset:41056\n"
"v_mfma_f32_16x16x32_bf16 v[88:91], a[40:43], a[156:159], v[88:91]\n"
"ds_read_b64_tr_b16 a[142:143], v213 offset:45152\n"
"v_mfma_f32_16x16x32_bf16 v[84:87], a[36:39], a[156:159], v[84:87]\n"
"v_mfma_f32_16x16x32_bf16 v[80:83], a[32:35], a[156:159], v[80:83]\n"
"s_waitcnt lgkmcnt(6)\n"
"v_mfma_f32_16x16x32_bf16 v[96:99], a[128:131], a[32:35], v[96:99]\n"
"ds_read_b64_tr_b16 a[144:145], v213 offset:41088\n"
"v_mfma_f32_16x16x32_bf16 v[100:103], a[128:131], a[36:39], v[100:103]\n"
"ds_read_b64_tr_b16 a[146:147], v213 offset:45184\n"
"s_add_u32 m0, s83, 0x800\n"
"v_mfma_f32_16x16x32_bf16 v[104:107], a[128:131], a[40:43], v[104:107]\n"
"buffer_load_dwordx4 v222, s[60:63], s80 offen lds\n"
"v_mfma_f32_16x16x32_bf16 v[108:111], a[128:131], a[44:47], v[108:111]\n"
"s_waitcnt lgkmcnt(6)\n"
"v_mfma_f32_16x16x32_bf16 v[124:127], a[132:135], a[44:47], v[124:127]\n"
"ds_read_b64_tr_b16 a[148:149], v213 offset:41120\n"
"v_mfma_f32_16x16x32_bf16 v[120:123], a[132:135], a[40:43], v[120:123]\n"
"ds_read_b64_tr_b16 a[150:151], v213 offset:45216\n"
"s_add_u32 m0, s83, 0xc00\n"
"v_mfma_f32_16x16x32_bf16 v[116:119], a[132:135], a[36:39], v[116:119]\n"
"buffer_load_dwordx4 v223, s[60:63], s80 offen lds\n"
"v_mfma_f32_16x16x32_bf16 v[112:115], a[132:135], a[32:35], v[112:115]\n"
"s_add_u32 s80, s80, 0x70000\n"
"s_waitcnt lgkmcnt(6)\n"
"v_mfma_f32_16x16x32_bf16 v[128:131], a[136:139], a[32:35], v[128:131]\n"
"ds_read_b64_tr_b16 a[152:153], v213 offset:41152\n"
"v_mfma_f32_16x16x32_bf16 v[132:135], a[136:139], a[36:39], v[132:135]\n"
"ds_read_b64_tr_b16 a[154:155], v213 offset:45248\n"
"v_mfma_f32_16x16x32_bf16 v[136:139], a[136:139], a[40:43], v[136:139]\n"
"buffer_load_dwordx4 a[80:83], v224, s[64:67], s81 offen sc1\n"
"v_mfma_f32_16x16x32_bf16 v[140:143], a[136:139], a[44:47], v[140:143]\n"
"s_waitcnt lgkmcnt(6)\n"
"v_mfma_f32_16x16x32_bf16 v[156:159], a[140:143], a[44:47], v[156:159]\n"
"ds_read_b64_tr_b16 a[156:157], v211 offset:34816\n"
"v_mfma_f32_16x16x32_bf16 v[152:155], a[140:143], a[40:43], v[152:155]\n"
"ds_read_b64_tr_b16 a[158:159], v211 offset:37888\n"
"v_mfma_f32_16x16x32_bf16 v[148:151], a[140:143], a[36:39], v[148:151]\n"
"buffer_load_dwordx4 a[84:87], v224, s[64:67], s81 offen offset:1024 sc1\n"
"v_mfma_f32_16x16x32_bf16 v[144:147], a[140:143], a[32:35], v[144:147]\n"
"s_waitcnt lgkmcnt(6)\n"
"v_mfma_f32_16x16x32_bf16 v[160:163], a[144:147], a[32:35], v[160:163]\n"
"ds_read_b64_tr_b16 a[128:129], v212 offset:34816\n"
"v_mfma_f32_16x16x32_bf16 v[164:167], a[144:147], a[36:39], v[164:167]\n"
"ds_read_b64_tr_b16 a[130:131], v212 offset:37888\n"
"v_mfma_f32_16x16x32_bf16 v[168:171], a[144:147], a[40:43], v[168:171]\n"
"buffer_load_dwordx4 a[88:91], v224, s[64:67], s81 offen offset:2048 sc1\n"
"v_mfma_f32_16x16x32_bf16 v[172:175], a[144:147], a[44:47], v[172:175]\n"
"s_waitcnt lgkmcnt(6)\n"
"v_mfma_f32_16x16x32_bf16 v[188:191], a[148:151], a[44:47], v[188:191]\n"
"ds_read_b64_tr_b16 a[132:133], v211 offset:34880\n"
"v_mfma_f32_16x16x32_bf16 v[184:187], a[148:151], a[40:43], v[184:187]\n"
"ds_read_b64_tr_b16 a[134:135], v211 offset:37952\n"
"v_mfma_f32_16x16x32_bf16 v[180:183], a[148:151], a[36:39], v[180:183]\n"
"v_mfma_f32_16x16x32_bf16 v[176:179], a[148:151], a[32:35], v[176:179]\n"
"s_waitcnt lgkmcnt(6)\n"
"v_mfma_f32_16x16x32_bf16 v[192:195], a[152:155], a[32:35], v[192:195]\n"
"ds_read_b64_tr_b16 a[136:137], v212 offset:34880\n"
"v_mfma_f32_16x16x32_bf16 v[196:199], a[152:155], a[36:39], v[196:199]\n"
"ds_read_b64_tr_b16 a[138:139], v212 offset:37952\n"
"v_mfma_f32_16x16x32_bf16 v[200:203], a[152:155], a[40:43], v[200:203]\n"
"buffer_load_dwordx4 a[92:95], v224, s[64:67], s81 offen offset:3072 sc1\n"
"v_mfma_f32_16x16x32_bf16 v[204:207], a[152:155], a[44:47], v[204:207]\n"
"s_add_u32 s81, s81, 0x1000\n"
"s_waitcnt vmcnt(16) lgkmcnt(6)\n"
"v_mfma_f32_16x16x32_bf16 v[0:3], a[156:159], a[48:51], v[0:3]\n"
"ds_read_b64_tr_b16 a[140:141], v211 offset:34944\n"
"v_mfma_f32_16x16x32_bf16 v[4:7], a[156:159], a[52:55], v[4:7]\n"
"ds_read_b64_tr_b16 a[142:143], v211 offset:38016\n"
"v_mfma_f32_16x16x32_bf16 v[8:11], a[156:159], a[56:59], v[8:11]\n"
"buffer_load_dwordx4 a[96:99], v224, s[64:67], s81 offen sc1\n"
"v_mfma_f32_16x16x32_bf16 v[12:15], a[156:159], a[60:63], v[12:15]\n"
"s_waitcnt lgkmcnt(6)\n"
"v_mfma_f32_16x16x32_bf16 v[28:31], a[128:131], a[60:63], v[28:31]\n"
"ds_read_b64_tr_b16 a[144:145], v212 offset:34944\n"
"v_mfma_f32_16x16x32_bf16 v[24:27], a[128:131], a[56:59], v[24:27]\n"
"ds_read_b64_tr_b16 a[146:147], v212 offset:38016\n"
"v_mfma_f32_16x16x32_bf16 v[20:23], a[128:131], a[52:55], v[20:23]\n"
"buffer_load_dwordx4 a[100:103], v224, s[64:67], s81 offen offset:1024 sc1\n"
"v_mfma_f32_16x16x32_bf16 v[16:19], a[128:131], a[48:51], v[16:19]\n"
"s_waitcnt lgkmcnt(6)\n"
"v_mfma_f32_16x16x32_bf16 v[32:35], a[132:135], a[48:51], v[32:35]\n"
"ds_read_b64_tr_b16 a[148:149], v213 offset:49152\n"
"v_mfma_f32_16x16x32_bf16 v[36:39], a[132:135], a[52:55], v[36:39]\n"
"ds_read_b64_tr_b16 a[150:151], v213 offset:53248\n"
"v_mfma_f32_16x16x32_bf16 v[40:43], a[132:135], a[56:59], v[40:43]\n"
"buffer_load_dwordx4 a[104:107], v224, s[64:67], s81 offen offset:2048 sc1\n"
"v_mfma_f32_16x16x32_bf16 v[44:47], a[132:135], a[60:63], v[44:47]\n"
"s_waitcnt lgkmcnt(6)\n"
"v_mfma_f32_16x16x32_bf16 v[60:63], a[136:139], a[60:63], v[60:63]\n"
"ds_read_b64_tr_b16 a[152:153], v213 offset:49184\n"
"v_mfma_f32_16x16x32_bf16 v[56:59], a[136:139], a[56:59], v[56:59]\n"
"ds_read_b64_tr_b16 a[154:155], v213 offset:53280\n"
"v_mfma_f32_16x16x32_bf16 v[52:55], a[136:139], a[52:55], v[52:55]\n"
"buffer_load_dwordx4 a[108:111], v224, s[64:67], s81 offen offset:3072 sc1\n"
"v_mfma_f32_16x16x32_bf16 v[48:51], a[136:139], a[48:51], v[48:51]\n"
"s_add_u32 s81, s81, 0x1000\n"
"s_waitcnt lgkmcnt(6)\n"
"v_mfma_f32_16x16x32_bf16 v[64:67], a[48:51], a[140:143], v[64:67]\n"
"ds_read_b64_tr_b16 a[156:157], v213 offset:49216\n"
"v_mfma_f32_16x16x32_bf16 v[68:71], a[52:55], a[140:143], v[68:71]\n"
"ds_read_b64_tr_b16 a[158:159], v213 offset:53312\n"
"v_mfma_f32_16x16x32_bf16 v[72:75], a[56:59], a[140:143], v[72:75]\n"
"v_mfma_f32_16x16x32_bf16 v[76:79], a[60:63], a[140:143], v[76:79]\n"
"s_waitcnt lgkmcnt(6)\n"
"v_mfma_f32_16x16x32_bf16 v[92:95], a[60:63], a[144:147], v[92:95]\n"
"ds_read_b64_tr_b16 a[128:129], v213 offset:49248\n"
"v_mfma_f32_16x16x32_bf16 v[88:91], a[56:59], a[144:147], v[88:91]\n"
"ds_read_b64_tr_b16 a[130:131], v213 offset:53344\n"
"s_add_u32 m0, s82, 0x7000\n"
"v_mfma_f32_16x16x32_bf16 v[84:87], a[52:55], a[144:147], v[84:87]\n"
"buffer_load_dwordx4 v217, s[56:59], s79 offen lds\n"
"v_mfma_f32_16x16x32_bf16 v[80:83], a[48:51], a[144:147], v[80:83]\n"
"s_waitcnt lgkmcnt(6)\n"
"v_mfma_f32_16x16x32_bf16 v[96:99], a[148:151], a[48:51], v[96:99]\n"
"ds_read_b64_tr_b16 a[132:133], v213 offset:49280\n"
"v_mfma_f32_16x16x32_bf16 v[100:103], a[148:151], a[52:55], v[100:103]\n"
"ds_read_b64_tr_b16 a[134:135], v213 offset:53376\n"
"v_mfma_f32_16x16x32_bf16 v[104:107], a[148:151], a[56:59], v[104:107]\n"
".Lpad4_%=:\n"
"s_cbranch_execz .Lpad8_%=\n"
"v_mfma_f32_16x16x32_bf16 v[108:111], a[148:151], a[60:63], v[108:111]\n"
"s_waitcnt lgkmcnt(6)\n"
"v_mfma_f32_16x16x32_bf16 v[124:127], a[152:155], a[60:63], v[124:127]\n"
"ds_read_b64_tr_b16 a[136:137], v213 offset:49312\n"
"v_mfma_f32_16x16x32_bf16 v[120:123], a[152:155], a[56:59], v[120:123]\n"
"ds_read_b64_tr_b16 a[138:139], v213 offset:53408\n"
"v_mfma_f32_16x16x32_bf16 v[116:119], a[152:155], a[52:55], v[116:119]\n"
"v_mfma_f32_16x16x32_bf16 v[112:115], a[152:155], a[48:51], v[112:115]\n"
"s_waitcnt lgkmcnt(6)\n"
"v_mfma_f32_16x16x32_bf16 v[128:131], a[156:159], a[48:51], v[128:131]\n"
"ds_read_b64_tr_b16 a[140:141], v213 offset:49344\n"
"v_mfma_f32_16x16x32_bf16 v[132:135], a[156:159], a[52:55], v[132:135]\n"
"ds_read_b64_tr_b16 a[142:143], v213 offset:53440\n"
"s_add_u32 m0, s82, 0x7400\n"
"v_mfma_f32_16x16x32_bf16 v[136:139], a[156:159], a[56:59], v[136:139]\n"
"buffer_load_dwordx4 v218, s[56:59], s79 offen lds\n"
"v_mfma_f32_16x16x32_bf16 v[140:143], a[156:159], a[60:63], v[140:143]\n"
"s_barrier\n"
"s_waitcnt lgkmcnt(6)\n"
"v_mfma_f32_16x16x32_bf16 v[156:159], a[128:131], a[60:63], v[156:159]\n"
"ds_read_b64_tr_b16 a[144:145], v214\n"
"v_mfma_f32_16x16x32_bf16 v[152:155], a[128:131], a[56:59], v[152:155]\n"
"ds_read_b64_tr_b16 a[146:147], v214 offset:3072\n"
"v_mfma_f32_16x16x32_bf16 v[148:151], a[128:131], a[52:55], v[148:151]\n"
"v_mfma_f32_16x16x32_bf16 v[144:147], a[128:131], a[48:51], v[144:147]\n"
"s_waitcnt lgkmcnt(6)\n"
"v_mfma_f32_16x16x32_bf16 v[160:163], a[132:135], a[48:51], v[160:163]\n"
"ds_read_b64_tr_b16 a[148:149], v215\n"
"v_mfma_f32_16x16x32_bf16 v[164:167], a[132:135], a[52:55], v[164:167]\n"
"ds_read_b64_tr_b16 a[150:151], v215 offset:3072\n"
"s_add_u32 m0, s82, 0x7800\n"
"v_mfma_f32_16x16x32_bf16 v[168:171], a[132:135], a[56:59], v[168:171]\n"
"buffer_load_dwordx4 v219, s[56:59], s79 offen lds\n"
"v_mfma_f32_16x16x32_bf16 v[172:175], a[132:135], a[60:63], v[172:175]\n"
"s_add_u32 s79, s79, 0x60000\n"
"s_waitcnt lgkmcnt(6)\n"
"v_mfma_f32_16x16x32_bf16 v[188:191], a[136:139], a[60:63], v[188:191]\n"
"ds_read_b64_tr_b16 a[152:153], v214 offset:64\n"
"v_mfma_f32_16x16x32_bf16 v[184:187], a[136:139], a[56:59], v[184:187]\n"
"ds_read_b64_tr_b16 a[154:155], v214 offset:3136\n"
"s_add_u32 m0, s83, 0x7000\n"
"v_mfma_f32_16x16x32_bf16 v[180:183], a[136:139], a[52:55], v[180:183]\n"
"buffer_load_dwordx4 v220, s[60:63], s80 offen lds\n"
"v_mfma_f32_16x16x32_bf16 v[176:179], a[136:139], a[48:51], v[176:179]\n"
"s_waitcnt lgkmcnt(6)\n"
"v_mfma_f32_16x16x32_bf16 v[192:195], a[140:143], a[48:51], v[192:195]\n"
"ds_read_b64_tr_b16 a[156:157], v215 offset:64\n"
"v_mfma_f32_16x16x32_bf16 v[196:199], a[140:143], a[52:55], v[196:199]\n"
"ds_read_b64_tr_b16 a[158:159], v215 offset:3136\n"
"v_mfma_f32_16x16x32_bf16 v[200:203], a[140:143], a[56:59], v[200:203]\n"
"v_mfma_f32_16x16x32_bf16 v[204:207], a[140:143], a[60:63], v[204:207]\n"
"s_waitcnt vmcnt(20) lgkmcnt(6)\n"
"v_mfma_f32_16x16x32_bf16 v[0:3], a[144:147], a[64:67], v[0:3]\n"
"ds_read_b64_tr_b16 a[128:129], v214 offset:128\n"
"v_mfma_f32_16x16x32_bf16 v[4:7], a[144:147], a[68:71], v[4:7]\n"
"ds_read_b64_tr_b16 a[130:131], v214 offset:3200\n"
"s_add_u32 m0, s83, 0x7400\n"
"v_mfma_f32_16x16x32_bf16 v[8:11], a[144:147], a[72:75], v[8:11]\n"
"buffer_load_dwordx4 v221, s[60:63], s80 offen lds\n"
"v_mfma_f32_16x16x32_bf16 v[12:15], a[144:147], a[76:79], v[12:15]\n"
"s_waitcnt lgkmcnt(6)\n"
"v_mfma_f32_16x16x32_bf16 v[28:31], a[148:151], a[76:79], v[28:31]\n"
"ds_read_b64_tr_b16 a[132:133], v215 offset:128\n"
"v_mfma_f32_16x16x32_bf16 v[24:27], a[148:151], a[72:75], v[24:27]\n"
"ds_read_b64_tr_b16 a[134:135], v215 offset:3200\n"
"v_mfma_f32_16x16x32_bf16 v[20:23], a[148:151], a[68:71], v[20:23]\n"
"v_mfma_f32_16x16x32_bf16 v[16:19], a[148:151], a[64:67], v[16:19]\n"
"s_waitcnt lgkmcnt(6)\n"
"v_mfma_f32_16x16x32_bf16 v[32:35], a[152:155], a[64:67], v[32:35]\n"
"ds_read_b64_tr_b16 a[136:137], v216 offset:12288\n"
"v_mfma_f32_16x16x32_bf16 v[36:39], a[152:155], a[68:71], v[36:39]\n"
"ds_read_b64_tr_b16 a[138:139], v216 offset:16384\n"
"s_add_u32 m0, s83, 0x7800\n"
"v_mfma_f32_16x16x32_bf16 v[40:43], a[152:155], a[72:75], v[40:43]\n"
"buffer_load_dwordx4 v222, s[60:63], s80 offen lds\n"
"v_mfma_f32_16x16x32_bf16 v[44:47], a[152:155], a[76:79], v[44:47]\n"
"s_waitcnt lgkmcnt(6)\n"
"v_mfma_f32_16x16x32_bf16 v[60:63], a[156:159], a[76:79], v[60:63]\n"
"ds_read_b64_tr_b16 a[140:141], v216 offset:12320\n"
"v_mfma_f32_16x16x32_bf16 v[56:59], a[156:159], a[72:75], v[56:59]\n"
"ds_read_b64_tr_b16 a[142:143], v216 offset:16416\n"
"s_add_u32 m0, s83, 0x7c00\n"
"v_mfma_f32_16x16x32_bf16 v[52:55], a[156:159], a[68:71], v[52:55]\n"
"buffer_load_dwordx4 v223, s[60:63], s80 offen lds\n"
"v_mfma_f32_16x16x32_bf16 v[48:51], a[156:159], a[64:67], v[48:51]\n"
"s_add_u32 s80, s80, 0x70000\n"
"s_waitcnt lgkmcnt(6)\n"
"v_mfma_f32_16x16x32_bf16 v[64:67], a[64:67], a[128:131], v[64:67]\n"
"ds_read_b64_tr_b16 a[144:145], v216 offset:12352\n"
"v_mfma_f32_16x16x32_bf16 v[68:71], a[68:71], a[128:131], v[68:71]\n"
"ds_read_b64_tr_b16 a[146:147], v216 offset:16448\n"
"v_mfma_f32_16x16x32_bf16 v[72:75], a[72:75], a[128:131], v[72:75]\n"
"buffer_load_dwordx4 a[112:115], v224, s[64:67], s81 offen sc1\n"
"v_mfma_f32_16x16x32_bf16 v[76:79], a[76:79], a[128:131], v[76:79]\n"
"s_waitcnt lgkmcnt(6)\n"
"v_mfma_f32_16x16x32_bf16 v[92:95], a[76:79], a[132:135], v[92:95]\n"
"ds_read_b64_tr_b16 a[148:149], v216 offset:12384\n"
"v_mfma_f32_16x16x32_bf16 v[88:91], a[72:75], a[132:135], v[88:91]\n"
"ds_read_b64_tr_b16 a[150:151], v216 offset:16480\n"
"v_mfma_f32_16x16x32_bf16 v[84:87], a[68:71], a[132:135], v[84:87]\n"
"buffer_load_dwordx4 a[116:119], v224, s[64:67], s81 offen offset:1024 sc1\n"
"v_mfma_f32_16x16x32_bf16 v[80:83], a[64:67], a[132:135], v[80:83]\n"
"s_waitcnt lgkmcnt(6)\n"
"v_mfma_f32_16x16x32_bf16 v[96:99], a[136:139], a[64:67], v[96:99]\n"
"ds_read_b64_tr_b16 a[152:153], v216 offset:12416\n"
"v_mfma_f32_16x16x32_bf16 v[100:103], a[136:139], a[68:71], v[100:103]\n"
"ds_read_b64_tr_b16 a[154:155], v216 offset:16512\n"
"v_mfma_f32_16x16x32_bf16 v[104:107], a[136:139], a[72:75], v[104:107]\n"
"buffer_load_dwordx4 a[120:123], v224, s[64:67], s81 offen offset:2048 sc1\n"
"v_mfma_f32_16x16x32_bf16 v[108:111], a[136:139], a[76:79], v[108:111]\n"
"s_waitcnt lgkmcnt(6)\n"
"v_mfma_f32_16x16x32_bf16 v[124:127], a[140:143], a[76:79], v[124:127]\n"
"ds_read_b64_tr_b16 a[156:157], v216 offset:12448\n"
"v_mfma_f32_16x16x32_bf16 v[120:123], a[140:143], a[72:75], v[120:123]\n"
"ds_read_b64_tr_b16 a[158:159], v216 offset:16544\n"
"v_mfma_f32_16x16x32_bf16 v[116:119], a[140:143], a[68:71], v[116:119]\n"
"v_mfma_f32_16x16x32_bf16 v[112:115], a[140:143], a[64:67], v[112:115]\n"
"s_waitcnt lgkmcnt(6)\n"
"v_mfma_f32_16x16x32_bf16 v[128:131], a[144:147], a[64:67], v[128:131]\n"
"ds_read_b64_tr_b16 a[128:129], v216 offset:12480\n"
"v_mfma_f32_16x16x32_bf16 v[132:135], a[144:147], a[68:71], v[132:135]\n"
"ds_read_b64_tr_b16 a[130:131], v216 offset:16576\n"
"v_mfma_f32_16x16x32_bf16 v[136:139], a[144:147], a[72:75], v[136:139]\n"
"buffer_load_dwordx4 a[124:127], v224, s[64:67], s81 offen offset:3072 sc1\n"
"v_mfma_f32_16x16x32_bf16 v[140:143], a[144:147], a[76:79], v[140:143]\n"
"s_add_u32 s81, s81, 0x1000\n"
"s_waitcnt lgkmcnt(6)\n"
"v_mfma_f32_16x16x32_bf16 v[156:159], a[148:151], a[76:79], v[156:159]\n"
"ds_read_b64_tr_b16 a[132:133], v214 offset:6144\n"
"v_mfma_f32_16x16x32_bf16 v[152:155], a[148:151], a[72:75], v[152:155]\n"
"ds_read_b64_tr_b16 a[134:135], v214 offset:9216\n"
"v_mfma_f32_16x16x32_bf16 v[148:151], a[148:151], a[68:71], v[148:151]\n"
"buffer_load_dwordx4 a[0:3], v224, s[64:67], s81 offen sc1\n"
"v_mfma_f32_16x16x32_bf16 v[144:147], a[148:151], a[64:67], v[144:147]\n"
"s_waitcnt lgkmcnt(6)\n"
"v_mfma_f32_16x16x32_bf16 v[160:163], a[152:155], a[64:67], v[160:163]\n"
"ds_read_b64_tr_b16 a[136:137], v215 offset:6144\n"
"v_mfma_f32_16x16x32_bf16 v[164:167], a[152:155], a[68:71], v[164:167]\n"
"ds_read_b64_tr_b16 a[138:139], v215 offset:9216\n"
"v_mfma_f32_16x16x32_bf16 v[168:171], a[152:155], a[72:75], v[168:171]\n"
"buffer_load_dwordx4 a[4:7], v224, s[64:67], s81 offen offset:1024 sc1\n"
"v_mfma_f32_16x16x32_bf16 v[172:175], a[152:155], a[76:79], v[172:175]\n"
"s_waitcnt lgkmcnt(6)\n"
"v_mfma_f32_16x16x32_bf16 v[188:191], a[156:159], a[76:79], v[188:191]\n"
"ds_read_b64_tr_b16 a[140:141], v214 offset:6208\n"
"v_mfma_f32_16x16x32_bf16 v[184:187], a[156:159], a[72:75], v[184:187]\n"
"ds_read_b64_tr_b16 a[142:143], v214 offset:9280\n"
"v_mfma_f32_16x16x32_bf16 v[180:183], a[156:159], a[68:71], v[180:183]\n"
"buffer_load_dwordx4 a[8:11], v224, s[64:67], s81 offen offset:2048 sc1\n"
"v_mfma_f32_16x16x32_bf16 v[176:179], a[156:159], a[64:67], v[176:179]\n"
"s_waitcnt lgkmcnt(6)\n"
"v_mfma_f32_16x16x32_bf16 v[192:195], a[128:131], a[64:67], v[192:195]\n"
"ds_read_b64_tr_b16 a[144:145], v215 offset:6208\n"
"v_mfma_f32_16x16x32_bf16 v[196:199], a[128:131], a[68:71], v[196:199]\n"
"ds_read_b64_tr_b16 a[146:147], v215 offset:9280\n"
"v_mfma_f32_16x16x32_bf16 v[200:203], a[128:131], a[72:75], v[200:203]\n"
"buffer_load_dwordx4 a[12:15], v224, s[64:67], s81 offen offset:3072 sc1\n"
"v_mfma_f32_16x16x32_bf16 v[204:207], a[128:131], a[76:79], v[204:207]\n"
"s_add_u32 s81, s81, 0x1000\n"
"s_waitcnt vmcnt(19) lgkmcnt(6)\n"
"v_mfma_f32_16x16x32_bf16 v[0:3], a[132:135], a[80:83], v[0:3]\n"
"ds_read_b64_tr_b16 a[148:149], v214 offset:6272\n"
"v_mfma_f32_16x16x32_bf16 v[4:7], a[132:135], a[84:87], v[4:7]\n"
"ds_read_b64_tr_b16 a[150:151], v214 offset:9344\n"
"v_mfma_f32_16x16x32_bf16 v[8:11], a[132:135], a[88:91], v[8:11]\n"
"v_mfma_f32_16x16x32_bf16 v[12:15], a[132:135], a[92:95], v[12:15]\n"
"s_waitcnt lgkmcnt(6)\n"
"v_mfma_f32_16x16x32_bf16 v[28:31], a[136:139], a[92:95], v[28:31]\n"
"ds_read_b64_tr_b16 a[152:153], v215 offset:6272\n"
"v_mfma_f32_16x16x32_bf16 v[24:27], a[136:139], a[88:91], v[24:27]\n"
"ds_read_b64_tr_b16 a[154:155], v215 offset:9344\n"
"s_add_u32 m0, s82, 0xe000\n"
"v_mfma_f32_16x16x32_bf16 v[20:23], a[136:139], a[84:87], v[20:23]\n"
"buffer_load_dwordx4 v217, s[56:59], s79 offen lds\n"
"v_mfma_f32_16x16x32_bf16 v[16:19], a[136:139], a[80:83], v[16:19]\n"
"s_waitcnt lgkmcnt(6)\n"
"v_mfma_f32_16x16x32_bf16 v[32:35], a[140:143], a[80:83], v[32:35]\n"
"ds_read_b64_tr_b16 a[156:157], v216 offset:20480\n"
"v_mfma_f32_16x16x32_bf16 v[36:39], a[140:143], a[84:87], v[36:39]\n"
"ds_read_b64_tr_b16 a[158:159], v216 offset:24576\n"
"v_mfma_f32_16x16x32_bf16 v[40:43], a[140:143], a[88:91], v[40:43]\n"
"v_mfma_f32_16x16x32_bf16 v[44:47], a[140:143], a[92:95], v[44:47]\n"
"s_waitcnt lgkmcnt(6)\n"
"v_mfma_f32_16x16x32_bf16 v[60:63], a[144:147], a[92:95], v[60:63]\n"
"ds_read_b64_tr_b16 a[128:129], v216 offset:20512\n"
"v_mfma_f32_16x16x32_bf16 v[56:59], a[144:147], a[88:91], v[56:59]\n"
"ds_read_b64_tr_b16 a[130:131], v216 offset:24608\n"
"v_mfma_f32_16x16x32_bf16 v[52:55], a[144:147], a[84:87], v[52:55]\n"
"v_mfma_f32_16x16x32_bf16 v[48:51], a[144:147], a[80:83], v[48:51]\n"
"s_waitcnt lgkmcnt(6)\n"
"v_mfma_f32_16x16x32_bf16 v[64:67], a[80:83], a[148:151], v[64:67]\n"
"ds_read_b64_tr_b16 a[132:133], v216 offset:20544\n"
"v_mfma_f32_16x16x32_bf16 v[68:71], a[84:87], a[148:151], v[68:71]\n"
"ds_read_b64_tr_b16 a[134:135], v216 offset:24640\n"
"s_add_u32 m0, s82, 0xe400\n"
"v_mfma_f32_16x16x32_bf16 v[72:75], a[88:91], a[148:151], v[72:75]\n"
"buffer_load_dwordx4 v218, s[56:59], s79 offen lds\n"
"v_mfma_f32_16x16x32_bf16 v[76:79], a[92:95], a[148:151], v[76:79]\n"
"s_waitcnt lgkmcnt(6)\n"
"v_mfma_f32_16x16x32_bf16 v[92:95], a[92:95], a[152:155], v[92:95]\n"
"ds_read_b64_tr_b16 a[136:137], v216 offset:20576\n"
"v_mfma_f32_16x16x32_bf16 v[88:91], a[88:91], a[152:155], v[88:91]\n"
"ds_read_b64_tr_b16 a[138:139], v216 offset:24672\n"
"v_mfma_f32_16x16x32_bf16 v[84:87], a[84:87], a[152:155], v[84:87]\n"
"v_mfma_f32_16x16x32_bf16 v[80:83], a[80:83], a[152:155], v[80:83]\n"
"s_waitcnt lgkmcnt(6)\n"
"v_mfma_f32_16x16x32_bf16 v[96:99], a[156:159], a[80:83], v[96:99]\n"
"ds_read_b64_tr_b16 a[140:141], v216 offset:20608\n"
"v_mfma_f32_16x16x32_bf16 v[100:103], a[156:159], a[84:87], v[100:103]\n"
"ds_read_b64_tr_b16 a[142:143], v216 offset:24704\n"
"s_add_u32 m0, s82, 0xe800\n"
"v_mfma_f32_16x16x32_bf16 v[104:107], a[156:159], a[88:91], v[104:107]\n"
"buffer_load_dwordx4 v219, s[56:59], s79 offen lds\n"
"v_mfma_f32_16x16x32_bf16 v[108:111], a[156:159], a[92:95], v[108:111]\n"
"s_add_u32 s79, s79, 0x60000\n"
"s_waitcnt lgkmcnt(6)\n"
"v_mfma_f32_16x16x32_bf16 v[124:127], a[128:131], a[92:95], v[124:127]\n"
"ds_read_b64_tr_b16 a[144:145], v216 offset:20640\n"
"v_mfma_f32_16x16x32_bf16 v[120:123], a[128:131], a[88:91], v[120:123]\n"
"ds_read_b64_tr_b16 a[146:147], v216 offset:24736\n"
"v_mfma_f32_16x16x32_bf16 v[116:119], a[128:131], a[84:87], v[116:119]\n"
"buffer_load_dword a160, v240, s[72:75], s26 offen\n"
"v_mfma_f32_16x16x32_bf16 v[112:115], a[128:131], a[80:83], v[112:115]\n"
"s_add_u32 s26, s26, 0x2000\n"
"s_waitcnt lgkmcnt(6)\n"
"v_mfma_f32_16x16x32_bf16 v[128:131], a[132:135], a[80:83], v[128:131]\n"
"ds_read_b64_tr_b16 a[148:149], v216 offset:20672\n"
"v_mfma_f32_16x16x32_bf16 v[132:135], a[132:135], a[84:87], v[132:135]\n"
"ds_read_b64_tr_b16 a[150:151], v216 offset:24768\n"
"v_mfma_f32_16x16x32_bf16 v[136:139], a[132:135], a[88:91], v[136:139]\n"
"v_mfma_f32_16x16x32_bf16 v[140:143], a[132:135], a[92:95], v[140:143]\n"
"s_barrier\n"
"s_waitcnt lgkmcnt(6)\n"
"v_mfma_f32_16x16x32_bf16 v[156:159], a[136:139], a[92:95], v[156:159]\n"
"ds_read_b64_tr_b16 a[152:153], v208\n"
"v_mfma_f32_16x16x32_bf16 v[152:155], a[136:139], a[88:91], v[152:155]\n"
"ds_read_b64_tr_b16 a[154:155], v208 offset:3072\n"
"v_mfma_f32_16x16x32_bf16 v[148:151], a[136:139], a[84:87], v[148:151]\n"
"v_mfma_f32_16x16x32_bf16 v[144:147], a[136:139], a[80:83], v[144:147]\n"
"s_waitcnt lgkmcnt(6)\n"
"v_mfma_f32_16x16x32_bf16 v[160:163], a[140:143], a[80:83], v[160:163]\n"
"ds_read_b64_tr_b16 a[156:157], v209\n"
"v_mfma_f32_16x16x32_bf16 v[164:167], a[140:143], a[84:87], v[164:167]\n"
"ds_read_b64_tr_b16 a[158:159], v209 offset:3072\n"
"v_mfma_f32_16x16x32_bf16 v[168:171], a[140:143], a[88:91], v[168:171]\n"
"v_mfma_f32_16x16x32_bf16 v[172:175], a[140:143], a[92:95], v[172:175]\n"
"s_waitcnt lgkmcnt(6)\n"
"v_mfma_f32_16x16x32_bf16 v[188:191], a[144:147], a[92:95], v[188:191]\n"
"ds_read_b64_tr_b16 a[128:129], v208 offset:64\n"
"v_mfma_f32_16x16x32_bf16 v[184:187], a[144:147], a[88:91], v[184:187]\n"
"ds_read_b64_tr_b16 a[130:131], v208 offset:3136\n"
"v_mfma_f32_16x16x32_bf16 v[180:183], a[144:147], a[84:87], v[180:183]\n"
"v_mfma_f32_16x16x32_bf16 v[176:179], a[144:147], a[80:83], v[176:179]\n"
"s_waitcnt lgkmcnt(6)\n"
"v_mfma_f32_16x16x32_bf16 v[192:195], a[148:151], a[80:83], v[192:195]\n"
"ds_read_b64_tr_b16 a[132:133], v209 offset:64\n"
"v_mfma_f32_16x16x32_bf16 v[196:199], a[148:151], a[84:87], v[196:199]\n"
"ds_read_b64_tr_b16 a[134:135], v209 offset:3136\n"
"v_mfma_f32_16x16x32_bf16 v[200:203], a[148:151], a[88:91], v[200:203]\n"
"v_mfma_f32_16x16x32_bf16 v[204:207], a[148:151], a[92:95], v[204:207]\n"
"s_waitcnt vmcnt(19) lgkmcnt(6)\n"
"v_mfma_f32_16x16x32_bf16 v[0:3], a[152:155], a[96:99], v[0:3]\n"
"ds_read_b64_tr_b16 a[136:137], v208 offset:128\n"
"v_mfma_f32_16x16x32_bf16 v[4:7], a[152:155], a[100:103], v[4:7]\n"
"ds_read_b64_tr_b16 a[138:139], v208 offset:3200\n"
"v_mfma_f32_16x16x32_bf16 v[8:11], a[152:155], a[104:107], v[8:11]\n"
"v_mfma_f32_16x16x32_bf16 v[12:15], a[152:155], a[108:111], v[12:15]\n"
"s_waitcnt lgkmcnt(6)\n"
"v_mfma_f32_16x16x32_bf16 v[28:31], a[156:159], a[108:111], v[28:31]\n"
"ds_read_b64_tr_b16 a[140:141], v209 offset:128\n"
"v_mfma_f32_16x16x32_bf16 v[24:27], a[156:159], a[104:107], v[24:27]\n"
"ds_read_b64_tr_b16 a[142:143], v209 offset:3200\n"
"v_mfma_f32_16x16x32_bf16 v[20:23], a[156:159], a[100:103], v[20:23]\n"
"v_mfma_f32_16x16x32_bf16 v[16:19], a[156:159], a[96:99], v[16:19]\n"
"s_waitcnt lgkmcnt(6)\n"
"v_mfma_f32_16x16x32_bf16 v[32:35], a[128:131], a[96:99], v[32:35]\n"
"ds_read_b64_tr_b16 a[144:145], v210 offset:12288\n"
"v_mfma_f32_16x16x32_bf16 v[36:39], a[128:131], a[100:103], v[36:39]\n"
"ds_read_b64_tr_b16 a[146:147], v210 offset:16384\n"
"s_add_u32 m0, s83, 0xe000\n"
"v_mfma_f32_16x16x32_bf16 v[40:43], a[128:131], a[104:107], v[40:43]\n"
"buffer_load_dwordx4 v220, s[60:63], s80 offen lds\n"
"v_mfma_f32_16x16x32_bf16 v[44:47], a[128:131], a[108:111], v[44:47]\n"
"s_waitcnt lgkmcnt(6)\n"
"v_mfma_f32_16x16x32_bf16 v[60:63], a[132:135], a[108:111], v[60:63]\n"
"ds_read_b64_tr_b16 a[148:149], v210 offset:12320\n"
"v_mfma_f32_16x16x32_bf16 v[56:59], a[132:135], a[104:107], v[56:59]\n"
"ds_read_b64_tr_b16 a[150:151], v210 offset:16416\n"
"s_add_u32 m0, s83, 0xe400\n"
"v_mfma_f32_16x16x32_bf16 v[52:55], a[132:135], a[100:103], v[52:55]\n"
"buffer_load_dwordx4 v221, s[60:63], s80 offen lds\n"
"v_mfma_f32_16x16x32_bf16 v[48:51], a[132:135], a[96:99], v[48:51]\n"
"s_waitcnt lgkmcnt(6)\n"
"v_mfma_f32_16x16x32_bf16 v[64:67], a[96:99], a[136:139], v[64:67]\n"
"ds_read_b64_tr_b16 a[152:153], v210 offset:12352\n"
"v_mfma_f32_16x16x32_bf16 v[68:71], a[100:103], a[136:139], v[68:71]\n"
"ds_read_b64_tr_b16 a[154:155], v210 offset:16448\n"
"s_add_u32 m0, s83, 0xe800\n"
"v_mfma_f32_16x16x32_bf16 v[72:75], a[104:107], a[136:139], v[72:75]\n"
"buffer_load_dwordx4 v222, s[60:63], s80 offen lds\n"
"v_mfma_f32_16x16x32_bf16 v[76:79], a[108:111], a[136:139], v[76:79]\n"
"s_waitcnt lgkmcnt(6)\n"
"v_mfma_f32_16x16x32_bf16 v[92:95], a[108:111], a[140:143], v[92:95]\n"
"ds_read_b64_tr_b16 a[156:157], v210 offset:12384\n"
"v_mfma_f32_16x16x32_bf16 v[88:91], a[104:107], a[140:143], v[88:91]\n"
"ds_read_b64_tr_b16 a[158:159], v210 offset:16480\n"
"s_add_u32 m0, s83, 0xec00\n"
"v_mfma_f32_16x16x32_bf16 v[84:87], a[100:103], a[140:143], v[84:87]\n"
"buffer_load_dwordx4 v223, s[60:63], s80 offen lds\n"
"v_mfma_f32_16x16x32_bf16 v[80:83], a[96:99], a[140:143], v[80:83]\n"
"s_add_u32 s80, s80, 0x70000\n"
"s_waitcnt lgkmcnt(6)\n"
"v_mfma_f32_16x16x32_bf16 v[96:99], a[144:147], a[96:99], v[96:99]\n"
"ds_read_b64_tr_b16 a[128:129], v210 offset:12416\n"
"v_mfma_f32_16x16x32_bf16 v[100:103], a[144:147], a[100:103], v[100:103]\n"
"ds_read_b64_tr_b16 a[130:131], v210 offset:16512\n"
"v_mfma_f32_16x16x32_bf16 v[104:107], a[144:147], a[104:107], v[104:107]\n"
"buffer_load_dwordx4 a[16:19], v224, s[64:67], s81 offen sc1\n"
"v_mfma_f32_16x16x32_bf16 v[108:111], a[144:147], a[108:111], v[108:111]\n"
"s_waitcnt lgkmcnt(6)\n"
"v_mfma_f32_16x16x32_bf16 v[124:127], a[148:151], a[108:111], v[124:127]\n"
"ds_read_b64_tr_b16 a[132:133], v210 offset:12448\n"
"v_mfma_f32_16x16x32_bf16 v[120:123], a[148:151], a[104:107], v[120:123]\n"
"ds_read_b64_tr_b16 a[134:135], v210 offset:16544\n"
"v_mfma_f32_16x16x32_bf16 v[116:119], a[148:151], a[100:103], v[116:119]\n"
"v_mfma_f32_16x16x32_bf16 v[112:115], a[148:151], a[96:99], v[112:115]\n"
"s_waitcnt lgkmcnt(6)\n"
"v_mfma_f32_16x16x32_bf16 v[128:131], a[152:155], a[96:99], v[128:131]\n"
"ds_read_b64_tr_b16 a[136:137], v210 offset:12480\n"
"v_mfma_f32_16x16x32_bf16 v[132:135], a[152:155], a[100:103], v[132:135]\n"
"ds_read_b64_tr_b16 a[138:139], v210 offset:16576\n"
"v_mfma_f32_16x16x32_bf16 v[136:139], a[152:155], a[104:107], v[136:139]\n"
"buffer_load_dwordx4 a[20:23], v224, s[64:67], s81 offen offset:1024 sc1\n"
"v_mfma_f32_16x16x32_bf16 v[140:143], a[152:155], a[108:111], v[140:143]\n"
"s_waitcnt lgkmcnt(6)\n"
"v_mfma_f32_16x16x32_bf16 v[156:159], a[156:159], a[108:111], v[156:159]\n"
"ds_read_b64_tr_b16 a[140:141], v208 offset:6144\n"
"v_mfma_f32_16x16x32_bf16 v[152:155], a[156:159], a[104:107], v[152:155]\n"
"ds_read_b64_tr_b16 a[142:143], v208 offset:9216\n"
"v_mfma_f32_16x16x32_bf16 v[148:151], a[156:159], a[100:103], v[148:151]\n"
"buffer_load_dwordx4 a[24:27], v224, s[64:67], s81 offen offset:2048 sc1\n"
"v_mfma_f32_16x16x32_bf16 v[144:147], a[156:159], a[96:99], v[144:147]\n"
"s_waitcnt lgkmcnt(6)\n"
"v_mfma_f32_16x16x32_bf16 v[160:163], a[128:131], a[96:99], v[160:163]\n"
"ds_read_b64_tr_b16 a[144:145], v209 offset:6144\n"
"v_mfma_f32_16x16x32_bf16 v[164:167], a[128:131], a[100:103], v[164:167]\n"
"ds_read_b64_tr_b16 a[146:147], v209 offset:9216\n"
"v_mfma_f32_16x16x32_bf16 v[168:171], a[128:131], a[104:107], v[168:171]\n"
"buffer_load_dwordx4 a[28:31], v224, s[64:67], s81 offen offset:3072 sc1\n"
"v_mfma_f32_16x16x32_bf16 v[172:175], a[128:131], a[108:111], v[172:175]\n"
"s_add_u32 s81, s81, 0x1000\n"
"s_waitcnt lgkmcnt(6)\n"
"v_mfma_f32_16x16x32_bf16 v[188:191], a[132:135], a[108:111], v[188:191]\n"
"ds_read_b64_tr_b16 a[148:149], v208 offset:6208\n"
"v_mfma_f32_16x16x32_bf16 v[184:187], a[132:135], a[104:107], v[184:187]\n"
"ds_read_b64_tr_b16 a[150:151], v208 offset:9280\n"
"v_mfma_f32_16x16x32_bf16 v[180:183], a[132:135], a[100:103], v[180:183]\n"
"buffer_load_dwordx4 a[32:35], v224, s[64:67], s81 offen sc1\n"
"v_mfma_f32_16x16x32_bf16 v[176:179], a[132:135], a[96:99], v[176:179]\n"
"s_waitcnt lgkmcnt(6)\n"
"v_mfma_f32_16x16x32_bf16 v[192:195], a[136:139], a[96:99], v[192:195]\n"
"ds_read_b64_tr_b16 a[152:153], v209 offset:6208\n"
"v_mfma_f32_16x16x32_bf16 v[196:199], a[136:139], a[100:103], v[196:199]\n"
"ds_read_b64_tr_b16 a[154:155], v209 offset:9280\n"
"v_mfma_f32_16x16x32_bf16 v[200:203], a[136:139], a[104:107], v[200:203]\n"
"buffer_load_dwordx4 a[36:39], v224, s[64:67], s81 offen offset:1024 sc1\n"
"v_mfma_f32_16x16x32_bf16 v[204:207], a[136:139], a[108:111], v[204:207]\n"
"s_waitcnt vmcnt(18) lgkmcnt(6)\n"
"v_mfma_f32_16x16x32_bf16 v[0:3], a[140:143], a[112:115], v[0:3]\n"
"ds_read_b64_tr_b16 a[156:157], v208 offset:6272\n"
"v_mfma_f32_16x16x32_bf16 v[4:7], a[140:143], a[116:119], v[4:7]\n"
"ds_read_b64_tr_b16 a[158:159], v208 offset:9344\n"
"v_mfma_f32_16x16x32_bf16 v[8:11], a[140:143], a[120:123], v[8:11]\n"
"buffer_load_dwordx4 a[40:43], v224, s[64:67], s81 offen offset:2048 sc1\n"
"v_mfma_f32_16x16x32_bf16 v[12:15], a[140:143], a[124:127], v[12:15]\n"
"s_waitcnt lgkmcnt(6)\n"
"v_mfma_f32_16x16x32_bf16 v[28:31], a[144:147], a[124:127], v[28:31]\n"
"ds_read_b64_tr_b16 a[128:129], v209 offset:6272\n"
"v_mfma_f32_16x16x32_bf16 v[24:27], a[144:147], a[120:123], v[24:27]\n"
"ds_read_b64_tr_b16 a[130:131], v209 offset:9344\n"
"v_mfma_f32_16x16x32_bf16 v[20:23], a[144:147], a[116:119], v[20:23]\n"
"buffer_load_dwordx4 a[44:47], v224, s[64:67], s81 offen offset:3072 sc1\n"
"v_mfma_f32_16x16x32_bf16 v[16:19], a[144:147], a[112:115], v[16:19]\n"
"s_add_u32 s81, s81, 0x1000\n"
"s_waitcnt lgkmcnt(6)\n"
"v_mfma_f32_16x16x32_bf16 v[32:35], a[148:151], a[112:115], v[32:35]\n"
"ds_read_b64_tr_b16 a[132:133], v210 offset:20480\n"
"v_mfma_f32_16x16x32_bf16 v[36:39], a[148:151], a[116:119], v[36:39]\n"
"ds_read_b64_tr_b16 a[134:135], v210 offset:24576\n"
"v_mfma_f32_16x16x32_bf16 v[40:43], a[148:151], a[120:123], v[40:43]\n"
"v_mfma_f32_16x16x32_bf16 v[44:47], a[148:151], a[124:127], v[44:47]\n"
"s_lshr_b32 s44, s54, 3\n"
"s_lshl_b32 s44, s44, 2\n"
"s_waitcnt lgkmcnt(6)\n"
"v_mfma_f32_16x16x32_bf16 v[60:63], a[152:155], a[124:127], v[60:63]\n"
"ds_read_b64_tr_b16 a[136:137], v210 offset:20512\n"
"v_mfma_f32_16x16x32_bf16 v[56:59], a[152:155], a[120:123], v[56:59]\n"
"ds_read_b64_tr_b16 a[138:139], v210 offset:24608\n"
"v_mfma_f32_16x16x32_bf16 v[52:55], a[152:155], a[116:119], v[52:55]\n"
"v_mfma_f32_16x16x32_bf16 v[48:51], a[152:155], a[112:115], v[48:51]\n"
"s_add_u32 s44, s44, s78\n"
"s_mul_i32 s44, s44, 256\n"
"s_waitcnt lgkmcnt(6)\n"
"v_mfma_f32_16x16x32_bf16 v[64:67], a[112:115], a[156:159], v[64:67]\n"
"ds_read_b64_tr_b16 a[140:141], v210 offset:20544\n"
"v_mfma_f32_16x16x32_bf16 v[68:71], a[116:119], a[156:159], v[68:71]\n"
"ds_read_b64_tr_b16 a[142:143], v210 offset:24640\n"
"s_add_u32 m0, s82, 0x15000\n"
"v_mfma_f32_16x16x32_bf16 v[72:75], a[120:123], a[156:159], v[72:75]\n"
"buffer_load_dwordx4 v217, s[56:59], s79 offen lds\n"
"v_mfma_f32_16x16x32_bf16 v[76:79], a[124:127], a[156:159], v[76:79]\n"
"s_add_u32 s87, s44, 3072\n"
"s_getpc_b64 s[44:45]\n"
"s_waitcnt lgkmcnt(6)\n"
"v_mfma_f32_16x16x32_bf16 v[92:95], a[124:127], a[128:131], v[92:95]\n"
"ds_read_b64_tr_b16 a[144:145], v210 offset:20576\n"
"v_mfma_f32_16x16x32_bf16 v[88:91], a[120:123], a[128:131], v[88:91]\n"
"ds_read_b64_tr_b16 a[146:147], v210 offset:24672\n"
"v_mfma_f32_16x16x32_bf16 v[84:87], a[116:119], a[128:131], v[84:87]\n"
"v_mfma_f32_16x16x32_bf16 v[80:83], a[112:115], a[128:131], v[80:83]\n"
"s_add_u32 s44, s44, s87\n"
"s_addc_u32 s45, s45, 0\n"
"s_load_dword s33, s[44:45], 0x0\n"
"s_waitcnt lgkmcnt(6)\n"
"v_mfma_f32_16x16x32_bf16 v[96:99], a[132:135], a[112:115], v[96:99]\n"
"ds_read_b64_tr_b16 a[148:149], v210 offset:20608\n"
"v_mfma_f32_16x16x32_bf16 v[100:103], a[132:135], a[116:119], v[100:103]\n"
"ds_read_b64_tr_b16 a[150:151], v210 offset:24704\n"
"s_add_u32 m0, s82, 0x15400\n"
"v_mfma_f32_16x16x32_bf16 v[104:107], a[132:135], a[120:123], v[104:107]\n"
"buffer_load_dwordx4 v218, s[56:59], s79 offen lds\n"
"v_mfma_f32_16x16x32_bf16 v[108:111], a[132:135], a[124:127], v[108:111]\n"
"s_load_dword s34, s[44:45], 0x40\n"
"s_load_dword s33, s[44:45], 0x80\n"
"s_waitcnt lgkmcnt(6)\n"
"v_mfma_f32_16x16x32_bf16 v[124:127], a[136:139], a[124:127], v[124:127]\n"
"ds_read_b64_tr_b16 a[152:153], v210 offset:20640\n"
"v_mfma_f32_16x16x32_bf16 v[120:123], a[136:139], a[120:123], v[120:123]\n"
"ds_read_b64_tr_b16 a[154:155], v210 offset:24736\n"
"v_mfma_f32_16x16x32_bf16 v[116:119], a[136:139], a[116:119], v[116:119]\n"
"v_mfma_f32_16x16x32_bf16 v[112:115], a[136:139], a[112:115], v[112:115]\n"
"s_load_dword s34, s[44:45], 0xc0\n"
"s_waitcnt lgkmcnt(6)\n"
"v_mfma_f32_16x16x32_bf16 v[128:131], a[140:143], a[112:115], v[128:131]\n"
"ds_read_b64_tr_b16 a[156:157], v210 offset:20672\n"
"v_mfma_f32_16x16x32_bf16 v[132:135], a[140:143], a[116:119], v[132:135]\n"
"ds_read_b64_tr_b16 a[158:159], v210 offset:24768\n"
"s_add_u32 m0, s82, 0x15800\n"
"v_mfma_f32_16x16x32_bf16 v[136:139], a[140:143], a[120:123], v[136:139]\n"
"buffer_load_dwordx4 v219, s[56:59], s79 offen lds\n"
"v_mfma_f32_16x16x32_bf16 v[140:143], a[140:143], a[124:127], v[140:143]\n"
"s_add_u32 s79, s79, 0x60000\n"
"s_barrier\n"
"s_waitcnt lgkmcnt(6)\n"
"v_mfma_f32_16x16x32_bf16 v[156:159], a[144:147], a[124:127], v[156:159]\n"
"ds_read_b64_tr_b16 a[128:129], v208 offset:28736\n"
"v_mfma_f32_16x16x32_bf16 v[152:155], a[144:147], a[120:123], v[152:155]\n"
"ds_read_b64_tr_b16 a[130:131], v208 offset:31808\n"
"v_mfma_f32_16x16x32_bf16 v[148:151], a[144:147], a[116:119], v[148:151]\n"
"v_mfma_f32_16x16x32_bf16 v[144:147], a[144:147], a[112:115], v[144:147]\n"
"s_waitcnt lgkmcnt(6)\n"
"v_mfma_f32_16x16x32_bf16 v[160:163], a[148:151], a[112:115], v[160:163]\n"
"ds_read_b64_tr_b16 a[132:133], v209 offset:28736\n"
"v_mfma_f32_16x16x32_bf16 v[164:167], a[148:151], a[116:119], v[164:167]\n"
"ds_read_b64_tr_b16 a[134:135], v209 offset:31808\n"
"s_add_u32 m0, s83, 0x15000\n"
"v_mfma_f32_16x16x32_bf16 v[168:171], a[148:151], a[120:123], v[168:171]\n"
"buffer_load_dwordx4 v220, s[60:63], s80 offen lds\n"
"v_mfma_f32_16x16x32_bf16 v[172:175], a[148:151], a[124:127], v[172:175]\n"
"s_waitcnt lgkmcnt(6)\n"
"v_mfma_f32_16x16x32_bf16 v[188:191], a[152:155], a[124:127], v[188:191]\n"
"ds_read_b64_tr_b16 a[136:137], v208 offset:28800\n"
"v_mfma_f32_16x16x32_bf16 v[184:187], a[152:155], a[120:123], v[184:187]\n"
"ds_read_b64_tr_b16 a[138:139], v208 offset:31872\n"
"s_add_u32 m0, s83, 0x15400\n"
"v_mfma_f32_16x16x32_bf16 v[180:183], a[152:155], a[116:119], v[180:183]\n"
"buffer_load_dwordx4 v221, s[60:63], s80 offen lds\n"
"v_mfma_f32_16x16x32_bf16 v[176:179], a[152:155], a[112:115], v[176:179]\n"
"s_waitcnt lgkmcnt(6)\n"
"v_mfma_f32_16x16x32_bf16 v[192:195], a[156:159], a[112:115], v[192:195]\n"
"ds_read_b64_tr_b16 a[140:141], v209 offset:28800\n"
"v_mfma_f32_16x16x32_bf16 v[196:199], a[156:159], a[116:119], v[196:199]\n"
"ds_read_b64_tr_b16 a[142:143], v209 offset:31872\n"
"v_mfma_f32_16x16x32_bf16 v[200:203], a[156:159], a[120:123], v[200:203]\n"
"v_mfma_f32_16x16x32_bf16 v[204:207], a[156:159], a[124:127], v[204:207]\n"
"s_waitcnt vmcnt(21) lgkmcnt(6)\n"
"v_mfma_f32_16x16x32_bf16 v[32:35], a[128:131], a[0:3], v[32:35]\n"
"ds_read_b64_tr_b16 a[144:145], v210 offset:40960\n"
"v_mfma_f32_16x16x32_bf16 v[36:39], a[128:131], a[4:7], v[36:39]\n"
"ds_read_b64_tr_b16 a[146:147], v210 offset:45056\n"
"s_add_u32 m0, s83, 0x15800\n"
"v_mfma_f32_16x16x32_bf16 v[40:43], a[128:131], a[8:11], v[40:43]\n"
"buffer_load_dwordx4 v222, s[60:63], s80 offen lds\n"
"v_mfma_f32_16x16x32_bf16 v[44:47], a[128:131], a[12:15], v[44:47]\n"
"ds_read_b64_tr_b16 a[208:209], v208 offset:28672\n"
"s_waitcnt lgkmcnt(7)\n"
"v_mfma_f32_16x16x32_bf16 v[60:63], a[132:135], a[12:15], v[60:63]\n"
"ds_read_b64_tr_b16 a[148:149], v210 offset:40992\n"
"v_mfma_f32_16x16x32_bf16 v[56:59], a[132:135], a[8:11], v[56:59]\n"
"ds_read_b64_tr_b16 a[150:151], v210 offset:45088\n"
"s_add_u32 m0, s83, 0x15c00\n"
"v_mfma_f32_16x16x32_bf16 v[52:55], a[132:135], a[4:7], v[52:55]\n"
"buffer_load_dwordx4 v223, s[60:63], s80 offen lds\n"
"v_mfma_f32_16x16x32_bf16 v[48:51], a[132:135], a[0:3], v[48:51]\n"
"ds_read_b64_tr_b16 a[210:211], v208 offset:31744\n"
"s_add_u32 s80, s80, 0x70000\n"
"s_waitcnt lgkmcnt(8)\n"
"v_mfma_f32_16x16x32_bf16 v[64:67], a[0:3], a[136:139], v[64:67]\n"
"ds_read_b64_tr_b16 a[152:153], v210 offset:41024\n"
"v_mfma_f32_16x16x32_bf16 v[68:71], a[4:7], a[136:139], v[68:71]\n"
"ds_read_b64_tr_b16 a[154:155], v210 offset:45120\n"
"v_mfma_f32_16x16x32_bf16 v[72:75], a[8:11], a[136:139], v[72:75]\n"
"v_mfma_f32_16x16x32_bf16 v[76:79], a[12:15], a[136:139], v[76:79]\n"
"ds_read_b64_tr_b16 a[212:213], v208 offset:34816\n"
"s_waitcnt lgkmcnt(9)\n"
"v_mfma_f32_16x16x32_bf16 v[92:95], a[12:15], a[140:143], v[92:95]\n"
"ds_read_b64_tr_b16 a[156:157], v210 offset:41056\n"
"v_mfma_f32_16x16x32_bf16 v[88:91], a[8:11], a[140:143], v[88:91]\n"
"ds_read_b64_tr_b16 a[158:159], v210 offset:45152\n"
"v_mfma_f32_16x16x32_bf16 v[84:87], a[4:7], a[140:143], v[84:87]\n"
"v_mfma_f32_16x16x32_bf16 v[80:83], a[0:3], a[140:143], v[80:83]\n"
"ds_read_b64_tr_b16 a[214:215], v208 offset:37888\n"
"s_waitcnt lgkmcnt(10)\n"
"v_mfma_f32_16x16x32_bf16 v[96:99], a[144:147], a[0:3], v[96:99]\n"
"ds_read_b64_tr_b16 a[128:129], v210 offset:41088\n"
"v_mfma_f32_16x16x32_bf16 v[100:103], a[144:147], a[4:7], v[100:103]\n"
"ds_read_b64_tr_b16 a[130:131], v210 offset:45184\n"
"s_add_u32 m0, s82, 0x1c000\n"
"v_mfma_f32_16x16x32_bf16 v[104:107], a[144:147], a[8:11], v[104:107]\n"
"buffer_load_dwordx4 v217, s[56:59], s79 offen lds\n"
"v_mfma_f32_16x16x32_bf16 v[108:111], a[144:147], a[12:15], v[108:111]\n"
"ds_read_b64_tr_b16 a[224:225], v209 offset:28672\n"
"s_waitcnt lgkmcnt(10)\n"
"v_mfma_f32_16x16x32_bf16 v[124:127], a[148:151], a[12:15], v[124:127]\n"
"ds_read_b64_tr_b16 a[132:133], v210 offset:41120\n"
"v_mfma_f32_16x16x32_bf16 v[120:123], a[148:151], a[8:11], v[120:123]\n"
"ds_read_b64_tr_b16 a[134:135], v210 offset:45216\n"
"v_mfma_f32_16x16x32_bf16 v[116:119], a[148:151], a[4:7], v[116:119]\n"
"v_mfma_f32_16x16x32_bf16 v[112:115], a[148:151], a[0:3], v[112:115]\n"
"ds_read_b64_tr_b16 a[226:227], v209 offset:31744\n"
"s_waitcnt lgkmcnt(10)\n"
"v_mfma_f32_16x16x32_bf16 v[128:131], a[152:155], a[0:3], v[128:131]\n"
"ds_read_b64_tr_b16 a[136:137], v208 offset:34880\n"
"v_mfma_f32_16x16x32_bf16 v[132:135], a[152:155], a[4:7], v[132:135]\n"
"ds_read_b64_tr_b16 a[138:139], v208 offset:37952\n"
"s_add_u32 m0, s82, 0x1c400\n"
"v_mfma_f32_16x16x32_bf16 v[136:139], a[152:155], a[8:11], v[136:139]\n"
"buffer_load_dwordx4 v218, s[56:59], s79 offen lds\n"
"v_mfma_f32_16x16x32_bf16 v[140:143], a[152:155], a[12:15], v[140:143]\n"
".Lpad5_%=:\n"
"s_cbranch_execz .Lpad9_%=\n"
"ds_read_b64_tr_b16 a[228:229], v209 offset:34816\n"
"s_waitcnt lgkmcnt(10)\n"
"v_mfma_f32_16x16x32_bf16 v[156:159], a[156:159], a[12:15], v[156:159]\n"
"ds_read_b64_tr_b16 a[140:141], v209 offset:34880\n"
"v_mfma_f32_16x16x32_bf16 v[152:155], a[156:159], a[8:11], v[152:155]\n"
"ds_read_b64_tr_b16 a[142:143], v209 offset:37952\n"
"v_mfma_f32_16x16x32_bf16 v[148:151], a[156:159], a[4:7], v[148:151]\n"
"v_mfma_f32_16x16x32_bf16 v[144:147], a[156:159], a[0:3], v[144:147]\n"
"ds_read_b64_tr_b16 a[230:231], v209 offset:37888\n"
"s_waitcnt lgkmcnt(10)\n"
"v_mfma_f32_16x16x32_bf16 v[160:163], a[128:131], a[0:3], v[160:163]\n"
"ds_read_b64_tr_b16 a[144:145], v208 offset:34944\n"
"v_mfma_f32_16x16x32_bf16 v[164:167], a[128:131], a[4:7], v[164:167]\n"
"ds_read_b64_tr_b16 a[146:147], v208 offset:38016\n"
"s_add_u32 m0, s82, 0x1c800\n"
"v_mfma_f32_16x16x32_bf16 v[168:171], a[128:131], a[8:11], v[168:171]\n"
"buffer_load_dwordx4 v219, s[56:59], s79 offen lds\n"
"v_mfma_f32_16x16x32_bf16 v[172:175], a[128:131], a[12:15], v[172:175]\n"
"ds_read_b64_tr_b16 a[192:193], v210 offset:41152\n"
"s_add_u32 s79, s79, 0x60000\n"
"s_waitcnt lgkmcnt(10)\n"
"v_mfma_f32_16x16x32_bf16 v[188:191], a[132:135], a[12:15], v[188:191]\n"
"ds_read_b64_tr_b16 a[148:149], v209 offset:34944\n"
"v_mfma_f32_16x16x32_bf16 v[184:187], a[132:135], a[8:11], v[184:187]\n"
"ds_read_b64_tr_b16 a[150:151], v209 offset:38016\n"
"v_mfma_f32_16x16x32_bf16 v[180:183], a[132:135], a[4:7], v[180:183]\n"
"buffer_load_dwordx4 a[48:51], v224, s[64:67], s81 offen sc1\n"
"v_mfma_f32_16x16x32_bf16 v[176:179], a[132:135], a[0:3], v[176:179]\n"
"ds_read_b64_tr_b16 a[194:195], v210 offset:45248\n"
"s_waitcnt vmcnt(15) lgkmcnt(10)\n"
"v_mfma_f32_16x16x32_bf16 v[32:35], a[136:139], a[16:19], v[32:35]\n"
"ds_read_b64_tr_b16 a[152:153], v210 offset:49152\n"
"v_mfma_f32_16x16x32_bf16 v[36:39], a[136:139], a[20:23], v[36:39]\n"
"ds_read_b64_tr_b16 a[154:155], v210 offset:53248\n"
"v_mfma_f32_16x16x32_bf16 v[40:43], a[136:139], a[24:27], v[40:43]\n"
"buffer_load_dwordx4 a[52:55], v224, s[64:67], s81 offen offset:1024 sc1\n"
"v_mfma_f32_16x16x32_bf16 v[44:47], a[136:139], a[28:31], v[44:47]\n"
"ds_read_b64_tr_b16 a[196:197], v210 offset:49344\n"
"s_waitcnt lgkmcnt(10)\n"
"v_mfma_f32_16x16x32_bf16 v[60:63], a[140:143], a[28:31], v[60:63]\n"
"ds_read_b64_tr_b16 a[156:157], v210 offset:49184\n"
"v_mfma_f32_16x16x32_bf16 v[56:59], a[140:143], a[24:27], v[56:59]\n"
"ds_read_b64_tr_b16 a[158:159], v210 offset:53280\n"
"v_mfma_f32_16x16x32_bf16 v[52:55], a[140:143], a[20:23], v[52:55]\n"
"buffer_load_dwordx4 a[56:59], v224, s[64:67], s81 offen offset:2048 sc1\n"
"v_mfma_f32_16x16x32_bf16 v[48:51], a[140:143], a[16:19], v[48:51]\n"
"ds_read_b64_tr_b16 a[198:199], v210 offset:53440\n"
"s_waitcnt lgkmcnt(10)\n"
"v_mfma_f32_16x16x32_bf16 v[64:67], a[16:19], a[144:147], v[64:67]\n"
"ds_read_b64_tr_b16 a[128:129], v210 offset:49216\n"
"v_mfma_f32_16x16x32_bf16 v[68:71], a[20:23], a[144:147], v[68:71]\n"
"ds_read_b64_tr_b16 a[130:131], v210 offset:53312\n"
"v_mfma_f32_16x16x32_bf16 v[72:75], a[24:27], a[144:147], v[72:75]\n"
"v_mfma_f32_16x16x32_bf16 v[76:79], a[28:31], a[144:147], v[76:79]\n"
"s_waitcnt lgkmcnt(9)\n"
"v_mfma_f32_16x16x32_bf16 v[92:95], a[28:31], a[148:151], v[92:95]\n"
"ds_read_b64_tr_b16 a[132:133], v210 offset:49248\n"
"v_mfma_f32_16x16x32_bf16 v[88:91], a[24:27], a[148:151], v[88:91]\n"
"ds_read_b64_tr_b16 a[134:135], v210 offset:53344\n"
"v_mfma_f32_16x16x32_bf16 v[84:87], a[20:23], a[148:151], v[84:87]\n"
"buffer_load_dwordx4 a[60:63], v224, s[64:67], s81 offen offset:3072 sc1\n"
"v_mfma_f32_16x16x32_bf16 v[80:83], a[16:19], a[148:151], v[80:83]\n"
"s_add_u32 s81, s81, 0x1000\n"
"s_waitcnt lgkmcnt(8)\n"
"v_mfma_f32_16x16x32_bf16 v[96:99], a[152:155], a[16:19], v[96:99]\n"
"ds_read_b64_tr_b16 a[136:137], v210 offset:49280\n"
"v_mfma_f32_16x16x32_bf16 v[100:103], a[152:155], a[20:23], v[100:103]\n"
"ds_read_b64_tr_b16 a[138:139], v210 offset:53376\n"
"v_mfma_f32_16x16x32_bf16 v[104:107], a[152:155], a[24:27], v[104:107]\n"
"buffer_load_dwordx4 a[64:67], v224, s[64:67], s81 offen sc1\n"
"v_mfma_f32_16x16x32_bf16 v[108:111], a[152:155], a[28:31], v[108:111]\n"
"s_waitcnt lgkmcnt(7)\n"
"v_mfma_f32_16x16x32_bf16 v[124:127], a[156:159], a[28:31], v[124:127]\n"
"ds_read_b64_tr_b16 a[140:141], v210 offset:49312\n"
"v_mfma_f32_16x16x32_bf16 v[120:123], a[156:159], a[24:27], v[120:123]\n"
"ds_read_b64_tr_b16 a[142:143], v210 offset:53408\n"
"v_mfma_f32_16x16x32_bf16 v[116:119], a[156:159], a[20:23], v[116:119]\n"
"buffer_load_dwordx4 a[68:71], v224, s[64:67], s81 offen offset:1024 sc1\n"
"v_mfma_f32_16x16x32_bf16 v[112:115], a[156:159], a[16:19], v[112:115]\n"
"s_waitcnt lgkmcnt(8)\n"
"s_barrier\n"
"s_waitcnt lgkmcnt(6)\n"
"v_mfma_f32_16x16x32_bf16 v[128:131], a[128:131], a[16:19], v[128:131]\n"
"ds_read_b64_tr_b16 a[144:145], v211 offset:64\n"
"v_mfma_f32_16x16x32_bf16 v[132:135], a[128:131], a[20:23], v[132:135]\n"
"ds_read_b64_tr_b16 a[146:147], v211 offset:3136\n"
"v_mfma_f32_16x16x32_bf16 v[136:139], a[128:131], a[24:27], v[136:139]\n"
"buffer_load_dwordx4 a[72:75], v224, s[64:67], s81 offen offset:2048 sc1\n"
"v_mfma_f32_16x16x32_bf16 v[140:143], a[128:131], a[28:31], v[140:143]\n"
"s_waitcnt lgkmcnt(6)\n"
"v_mfma_f32_16x16x32_bf16 v[156:159], a[132:135], a[28:31], v[156:159]\n"
"ds_read_b64_tr_b16 a[148:149], v212 offset:64\n"
"v_mfma_f32_16x16x32_bf16 v[152:155], a[132:135], a[24:27], v[152:155]\n"
"ds_read_b64_tr_b16 a[150:151], v212 offset:3136\n"
"v_mfma_f32_16x16x32_bf16 v[148:151], a[132:135], a[20:23], v[148:151]\n"
"buffer_load_dwordx4 a[76:79], v224, s[64:67], s81 offen offset:3072 sc1\n"
"v_mfma_f32_16x16x32_bf16 v[144:147], a[132:135], a[16:19], v[144:147]\n"
"s_add_u32 s81, s81, 0x1000\n"
"s_waitcnt lgkmcnt(6)\n"
"v_mfma_f32_16x16x32_bf16 v[160:163], a[136:139], a[16:19], v[160:163]\n"
"ds_read_b64_tr_b16 a[152:153], v211 offset:128\n"
"v_mfma_f32_16x16x32_bf16 v[164:167], a[136:139], a[20:23], v[164:167]\n"
"ds_read_b64_tr_b16 a[154:155], v211 offset:3200\n"
"v_mfma_f32_16x16x32_bf16 v[168:171], a[136:139], a[24:27], v[168:171]\n"
"buffer_load_dwordx4 a[80:83], v224, s[64:67], s81 offen sc1\n"
"v_mfma_f32_16x16x32_bf16 v[172:175], a[136:139], a[28:31], v[172:175]\n"
"s_waitcnt lgkmcnt(6)\n"
"v_mfma_f32_16x16x32_bf16 v[188:191], a[140:143], a[28:31], v[188:191]\n"
"ds_read_b64_tr_b16 a[156:157], v212 offset:128\n"
"v_mfma_f32_16x16x32_bf16 v[184:187], a[140:143], a[24:27], v[184:187]\n"
"ds_read_b64_tr_b16 a[158:159], v212 offset:3200\n"
"v_mfma_f32_16x16x32_bf16 v[180:183], a[140:143], a[20:23], v[180:183]\n"
"buffer_load_dwordx4 a[84:87], v224, s[64:67], s81 offen offset:1024 sc1\n"
"v_mfma_f32_16x16x32_bf16 v[176:179], a[140:143], a[16:19], v[176:179]\n"
"s_waitcnt vmcnt(20) lgkmcnt(6)\n"
"v_mfma_f32_16x16x32_bf16 v[32:35], a[144:147], a[32:35], v[32:35]\n"
"ds_read_b64_tr_b16 a[128:129], v213 offset:12288\n"
"v_mfma_f32_16x16x32_bf16 v[36:39], a[144:147], a[36:39], v[36:39]\n"
"ds_read_b64_tr_b16 a[130:131], v213 offset:16384\n"
"v_mfma_f32_16x16x32_bf16 v[40:43], a[144:147], a[40:43], v[40:43]\n"
"buffer_load_dwordx4 a[88:91], v224, s[64:67], s81 offen offset:2048 sc1\n"
"v_mfma_f32_16x16x32_bf16 v[44:47], a[144:147], a[44:47], v[44:47]\n"
"ds_read_b64_tr_b16 a[216:217], v211\n"
"s_waitcnt lgkmcnt(7)\n"
"v_mfma_f32_16x16x32_bf16 v[60:63], a[148:151], a[44:47], v[60:63]\n"
"ds_read_b64_tr_b16 a[132:133], v213 offset:12320\n"
"v_mfma_f32_16x16x32_bf16 v[56:59], a[148:151], a[40:43], v[56:59]\n"
"ds_read_b64_tr_b16 a[134:135], v213 offset:16416\n"
"v_mfma_f32_16x16x32_bf16 v[52:55], a[148:151], a[36:39], v[52:55]\n"
"v_mfma_f32_16x16x32_bf16 v[48:51], a[148:151], a[32:35], v[48:51]\n"
"ds_read_b64_tr_b16 a[218:219], v211 offset:3072\n"
"s_waitcnt lgkmcnt(8)\n"
"v_mfma_f32_16x16x32_bf16 v[64:67], a[32:35], a[152:155], v[64:67]\n"
"ds_read_b64_tr_b16 a[136:137], v213 offset:12352\n"
"v_mfma_f32_16x16x32_bf16 v[68:71], a[36:39], a[152:155], v[68:71]\n"
"ds_read_b64_tr_b16 a[138:139], v213 offset:16448\n"
"v_mfma_f32_16x16x32_bf16 v[72:75], a[40:43], a[152:155], v[72:75]\n"
"buffer_load_dwordx4 a[92:95], v224, s[64:67], s81 offen offset:3072 sc1\n"
"v_mfma_f32_16x16x32_bf16 v[76:79], a[44:47], a[152:155], v[76:79]\n"
"ds_read_b64_tr_b16 a[220:221], v211 offset:6144\n"
"s_add_u32 s81, s81, 0x1000\n"
"s_waitcnt lgkmcnt(9)\n"
"v_mfma_f32_16x16x32_bf16 v[92:95], a[44:47], a[156:159], v[92:95]\n"
"ds_read_b64_tr_b16 a[140:141], v213 offset:12384\n"
"v_mfma_f32_16x16x32_bf16 v[88:91], a[40:43], a[156:159], v[88:91]\n"
"ds_read_b64_tr_b16 a[142:143], v213 offset:16480\n"
"v_mfma_f32_16x16x32_bf16 v[84:87], a[36:39], a[156:159], v[84:87]\n"
"buffer_load_dwordx4 a[96:99], v224, s[64:67], s81 offen sc1\n"
"v_mfma_f32_16x16x32_bf16 v[80:83], a[32:35], a[156:159], v[80:83]\n"
"ds_read_b64_tr_b16 a[222:223], v211 offset:9216\n"
"s_waitcnt lgkmcnt(10)\n"
"v_mfma_f32_16x16x32_bf16 v[96:99], a[128:131], a[32:35], v[96:99]\n"
"ds_read_b64_tr_b16 a[144:145], v213 offset:12416\n"
"v_mfma_f32_16x16x32_bf16 v[100:103], a[128:131], a[36:39], v[100:103]\n"
"ds_read_b64_tr_b16 a[146:147], v213 offset:16512\n"
"v_mfma_f32_16x16x32_bf16 v[104:107], a[128:131], a[40:43], v[104:107]\n"
"buffer_load_dwordx4 a[100:103], v224, s[64:67], s81 offen offset:1024 sc1\n"
"v_mfma_f32_16x16x32_bf16 v[108:111], a[128:131], a[44:47], v[108:111]\n"
"ds_read_b64_tr_b16 a[232:233], v212\n"
"s_waitcnt lgkmcnt(10)\n"
"v_mfma_f32_16x16x32_bf16 v[124:127], a[132:135], a[44:47], v[124:127]\n"
"ds_read_b64_tr_b16 a[148:149], v213 offset:12448\n"
"v_mfma_f32_16x16x32_bf16 v[120:123], a[132:135], a[40:43], v[120:123]\n"
"ds_read_b64_tr_b16 a[150:151], v213 offset:16544\n"
"v_mfma_f32_16x16x32_bf16 v[116:119], a[132:135], a[36:39], v[116:119]\n"
"buffer_load_dwordx4 a[104:107], v224, s[64:67], s81 offen offset:2048 sc1\n"
"v_mfma_f32_16x16x32_bf16 v[112:115], a[132:135], a[32:35], v[112:115]\n"
"ds_read_b64_tr_b16 a[234:235], v212 offset:3072\n"
"s_waitcnt lgkmcnt(10)\n"
"v_mfma_f32_16x16x32_bf16 v[128:131], a[136:139], a[32:35], v[128:131]\n"
"ds_read_b64_tr_b16 a[152:153], v211 offset:6208\n"
"v_mfma_f32_16x16x32_bf16 v[132:135], a[136:139], a[36:39], v[132:135]\n"
"ds_read_b64_tr_b16 a[154:155], v211 offset:9280\n"
"v_mfma_f32_16x16x32_bf16 v[136:139], a[136:139], a[40:43], v[136:139]\n"
"buffer_load_dwordx4 a[108:111], v224, s[64:67], s81 offen offset:3072 sc1\n"
"v_mfma_f32_16x16x32_bf16 v[140:143], a[136:139], a[44:47], v[140:143]\n"
"ds_read_b64_tr_b16 a[236:237], v212 offset:6144\n"
"s_add_u32 s81, s81, 0x1000\n"
"s_waitcnt lgkmcnt(10)\n"
"v_mfma_f32_16x16x32_bf16 v[156:159], a[140:143], a[44:47], v[156:159]\n"
"ds_read_b64_tr_b16 a[156:157], v212 offset:6208\n"
"v_mfma_f32_16x16x32_bf16 v[152:155], a[140:143], a[40:43], v[152:155]\n"
"ds_read_b64_tr_b16 a[158:159], v212 offset:9280\n"
"v_mfma_f32_16x16x32_bf16 v[148:151], a[140:143], a[36:39], v[148:151]\n"
"buffer_load_dwordx4 a[112:115], v224, s[64:67], s81 offen sc1\n"
"v_mfma_f32_16x16x32_bf16 v[144:147], a[140:143], a[32:35], v[144:147]\n"
"ds_read_b64_tr_b16 a[238:239], v212 offset:9216\n"
"s_waitcnt lgkmcnt(10)\n"
"v_mfma_f32_16x16x32_bf16 v[160:163], a[144:147], a[32:35], v[160:163]\n"
"ds_read_b64_tr_b16 a[128:129], v211 offset:6272\n"
"v_mfma_f32_16x16x32_bf16 v[164:167], a[144:147], a[36:39], v[164:167]\n"
"ds_read_b64_tr_b16 a[130:131], v211 offset:9344\n"
"v_mfma_f32_16x16x32_bf16 v[168:171], a[144:147], a[40:43], v[168:171]\n"
"buffer_load_dwordx4 a[116:119], v224, s[64:67], s81 offen offset:1024 sc1\n"
"v_mfma_f32_16x16x32_bf16 v[172:175], a[144:147], a[44:47], v[172:175]\n"
"ds_read_b64_tr_b16 a[200:201], v213 offset:12480\n"
"s_waitcnt lgkmcnt(10)\n"
"v_mfma_f32_16x16x32_bf16 v[188:191], a[148:151], a[44:47], v[188:191]\n"
"ds_read_b64_tr_b16 a[132:133], v212 offset:6272\n"
"v_mfma_f32_16x16x32_bf16 v[184:187], a[148:151], a[40:43], v[184:187]\n"
"ds_read_b64_tr_b16 a[134:135], v212 offset:9344\n"
"v_mfma_f32_16x16x32_bf16 v[180:183], a[148:151], a[36:39], v[180:183]\n"
"buffer_load_dwordx4 a[120:123], v224, s[64:67], s81 offen offset:2048 sc1\n"
"v_mfma_f32_16x16x32_bf16 v[176:179], a[148:151], a[32:35], v[176:179]\n"
"ds_read_b64_tr_b16 a[202:203], v213 offset:16576\n"
"s_waitcnt vmcnt(15) lgkmcnt(10)\n"
"v_mfma_f32_16x16x32_bf16 v[32:35], a[152:155], a[48:51], v[32:35]\n"
"ds_read_b64_tr_b16 a[136:137], v213 offset:20480\n"
"v_mfma_f32_16x16x32_bf16 v[36:39], a[152:155], a[52:55], v[36:39]\n"
"ds_read_b64_tr_b16 a[138:139], v213 offset:24576\n"
"v_mfma_f32_16x16x32_bf16 v[40:43], a[152:155], a[56:59], v[40:43]\n"
"v_mfma_f32_16x16x32_bf16 v[44:47], a[152:155], a[60:63], v[44:47]\n"
"ds_read_b64_tr_b16 a[204:205], v213 offset:20672\n"
"s_waitcnt lgkmcnt(10)\n"
"v_mfma_f32_16x16x32_bf16 v[60:63], a[156:159], a[60:63], v[60:63]\n"
"ds_read_b64_tr_b16 a[140:141], v213 offset:20512\n"
"v_mfma_f32_16x16x32_bf16 v[56:59], a[156:159], a[56:59], v[56:59]\n"
"ds_read_b64_tr_b16 a[142:143], v213 offset:24608\n"
"v_mfma_f32_16x16x32_bf16 v[52:55], a[156:159], a[52:55], v[52:55]\n"
"buffer_load_dwordx4 a[124:127], v224, s[64:67], s81 offen offset:3072 sc1\n"
"v_mfma_f32_16x16x32_bf16 v[48:51], a[156:159], a[48:51], v[48:51]\n"
"ds_read_b64_tr_b16 a[206:207], v213 offset:24768\n"
"s_add_u32 s81, s81, 0x1000\n"
"s_waitcnt lgkmcnt(10)\n"
"v_mfma_f32_16x16x32_bf16 v[64:67], a[48:51], a[128:131], v[64:67]\n"
"ds_read_b64_tr_b16 a[144:145], v213 offset:20544\n"
"v_mfma_f32_16x16x32_bf16 v[68:71], a[52:55], a[128:131], v[68:71]\n"
"ds_read_b64_tr_b16 a[146:147], v213 offset:24640\n"
"s_add_u32 m0, s83, 0x1c000\n"
"v_mfma_f32_16x16x32_bf16 v[72:75], a[56:59], a[128:131], v[72:75]\n"
"buffer_load_dwordx4 v220, s[60:63], s80 offen lds\n"
"v_mfma_f32_16x16x32_bf16 v[76:79], a[60:63], a[128:131], v[76:79]\n"
"s_waitcnt lgkmcnt(9)\n"
"v_mfma_f32_16x16x32_bf16 v[92:95], a[60:63], a[132:135], v[92:95]\n"
"ds_read_b64_tr_b16 a[148:149], v213 offset:20576\n"
"v_mfma_f32_16x16x32_bf16 v[88:91], a[56:59], a[132:135], v[88:91]\n"
"ds_read_b64_tr_b16 a[150:151], v213 offset:24672\n"
"v_mfma_f32_16x16x32_bf16 v[84:87], a[52:55], a[132:135], v[84:87]\n"
"s_mul_i32 s92, s30, 0x1c000\n"
"s_add_u32 s81, s95, s92\n"
"v_mfma_f32_16x16x32_bf16 v[80:83], a[48:51], a[132:135], v[80:83]\n"
"s_add_u32 s81, s81, 0x10000\n"
"s_mov_b32 s56, s40\n"
"s_waitcnt lgkmcnt(8)\n"
"v_mfma_f32_16x16x32_bf16 v[96:99], a[136:139], a[48:51], v[96:99]\n"
"ds_read_b64_tr_b16 a[152:153], v213 offset:20608\n"
"v_mfma_f32_16x16x32_bf16 v[100:103], a[136:139], a[52:55], v[100:103]\n"
"ds_read_b64_tr_b16 a[154:155], v213 offset:24704\n"
"s_add_u32 m0, s83, 0x1c400\n"
"v_mfma_f32_16x16x32_bf16 v[104:107], a[136:139], a[56:59], v[104:107]\n"
"buffer_load_dwordx4 v221, s[60:63], s80 offen lds\n"
"v_mfma_f32_16x16x32_bf16 v[108:111], a[136:139], a[60:63], v[108:111]\n"
"s_waitcnt lgkmcnt(7)\n"
"v_mfma_f32_16x16x32_bf16 v[124:127], a[140:143], a[60:63], v[124:127]\n"
"ds_read_b64_tr_b16 a[156:157], v213 offset:20640\n"
"v_mfma_f32_16x16x32_bf16 v[120:123], a[140:143], a[56:59], v[120:123]\n"
"ds_read_b64_tr_b16 a[158:159], v213 offset:24736\n"
"s_add_u32 m0, s83, 0x1c800\n"
"v_mfma_f32_16x16x32_bf16 v[116:119], a[140:143], a[52:55], v[116:119]\n"
"buffer_load_dwordx4 v222, s[60:63], s80 offen lds\n"
"v_mfma_f32_16x16x32_bf16 v[112:115], a[140:143], a[48:51], v[112:115]\n"
"s_waitcnt lgkmcnt(8)\n"
"s_barrier\n"
"s_waitcnt lgkmcnt(6)\n"
"v_mfma_f32_16x16x32_bf16 v[128:131], a[144:147], a[48:51], v[128:131]\n"
"ds_read_b64_tr_b16 a[128:129], v211 offset:28736\n"
"v_mfma_f32_16x16x32_bf16 v[132:135], a[144:147], a[52:55], v[132:135]\n"
"ds_read_b64_tr_b16 a[130:131], v211 offset:31808\n"
"v_mfma_f32_16x16x32_bf16 v[136:139], a[144:147], a[56:59], v[136:139]\n"
"s_and_b32 s57, s41, 0xffff\n"
"s_mov_b32 s58, 0x200000\n"
"v_mfma_f32_16x16x32_bf16 v[140:143], a[144:147], a[60:63], v[140:143]\n"
"s_add_u32 s79, s78, 16\n"
"s_lshl_b32 s79, s79, 16\n"
"s_waitcnt lgkmcnt(6)\n"
"v_mfma_f32_16x16x32_bf16 v[156:159], a[148:151], a[60:63], v[156:159]\n"
"ds_read_b64_tr_b16 a[132:133], v211 offset:34880\n"
"v_mfma_f32_16x16x32_bf16 v[152:155], a[148:151], a[56:59], v[152:155]\n"
"ds_read_b64_tr_b16 a[134:135], v211 offset:37952\n"
"s_add_u32 m0, s83, 0x1cc00\n"
"v_mfma_f32_16x16x32_bf16 v[148:151], a[148:151], a[52:55], v[148:151]\n"
"buffer_load_dwordx4 v223, s[60:63], s80 offen lds\n"
"v_mfma_f32_16x16x32_bf16 v[144:147], a[148:151], a[48:51], v[144:147]\n"
"s_add_u32 s80, s80, 0x70000\n"
"s_waitcnt lgkmcnt(6)\n"
"v_mfma_f32_16x16x32_bf16 v[160:163], a[152:155], a[48:51], v[160:163]\n"
"ds_read_b64_tr_b16 a[136:137], v214 offset:64\n"
"v_mfma_f32_16x16x32_bf16 v[164:167], a[152:155], a[52:55], v[164:167]\n"
"ds_read_b64_tr_b16 a[138:139], v214 offset:3136\n"
"v_mfma_f32_16x16x32_bf16 v[168:171], a[152:155], a[56:59], v[168:171]\n"
"s_add_u32 s79, s79, s39\n"
"s_lshl_b32 s82, s78, 12\n"
"v_mfma_f32_16x16x32_bf16 v[172:175], a[152:155], a[60:63], v[172:175]\n"
"s_and_b32 s90, s30, 1\n"
"s_lshl_b32 s90, s90, 11\n"
"s_waitcnt lgkmcnt(6)\n"
"v_mfma_f32_16x16x32_bf16 v[188:191], a[156:159], a[60:63], v[188:191]\n"
"ds_read_b64_tr_b16 a[140:141], v214 offset:6208\n"
"v_mfma_f32_16x16x32_bf16 v[184:187], a[156:159], a[56:59], v[184:187]\n"
"ds_read_b64_tr_b16 a[142:143], v214 offset:9280\n"
"v_mfma_f32_16x16x32_bf16 v[180:183], a[156:159], a[52:55], v[180:183]\n"
"v_add_u32 v208, s90, v224\n"
"v_xor_b32 v209, 0x800, v208\n"
"v_mfma_f32_16x16x32_bf16 v[176:179], a[156:159], a[48:51], v[176:179]\n"
"s_waitcnt vmcnt(19) lgkmcnt(6)\n"
"v_add_u32 v217, 0, v254\n"
"v_add_u32 v218, 0x7c00, v254\n"
"v_mfma_f32_16x16x32_bf16 v[32:35], a[128:131], a[64:67], v[32:35]\n"
"ds_read_b64_tr_b16 a[144:145], v212 offset:28736\n"
"v_add_u32 v219, 0x1800, v254\n"
"s_waitcnt vmcnt(18)\n"
"v_mfma_f32_16x16x32_bf16 v[36:39], a[128:131], a[68:71], v[36:39]\n"
"ds_read_b64_tr_b16 a[146:147], v212 offset:31808\n"
"s_lshl_b32 s90, s99, 13\n"
"s_waitcnt vmcnt(17)\n"
"v_mfma_f32_16x16x32_bf16 v[40:43], a[128:131], a[72:75], v[40:43]\n"
"s_add_u32 s90, s90, 0x1800000\n"
"s_cmp_lt_u32 s76, 7\n"
"s_cselect_b32 s55, s90, 0x40000000\n"
"s_waitcnt vmcnt(16)\n"
"v_mfma_f32_16x16x32_bf16 v[44:47], a[128:131], a[76:79], v[44:47]\n"
"s_waitcnt vmcnt(15) lgkmcnt(6)\n"
"s_mov_b32 s88, 0xbdd2d3e8\n"
"s_add_u32 s28, s55, 0x1000\n"
"v_mfma_f32_16x16x32_bf16 v[32:35], a[132:135], a[80:83], v[32:35]\n"
"ds_read_b64_tr_b16 a[148:149], v212 offset:34880\n"
"s_lshl_b32 s91, s99, 6\n"
"s_waitcnt vmcnt(14)\n"
"v_mfma_f32_16x16x32_bf16 v[36:39], a[132:135], a[84:87], v[36:39]\n"
"ds_read_b64_tr_b16 a[150:151], v212 offset:37952\n"
"s_add_u32 s90, s91, 0x2002000\n"
"s_waitcnt vmcnt(13)\n"
"v_mfma_f32_16x16x32_bf16 v[40:43], a[132:135], a[88:91], v[40:43]\n"
"s_cmp_lt_u32 s76, 7\n"
"s_cselect_b32 s90, s90, 0x40000000\n"
"s_add_u32 s91, s91, 0x2000000\n"
"s_waitcnt vmcnt(12)\n"
"v_mfma_f32_16x16x32_bf16 v[44:47], a[132:135], a[92:95], v[44:47]\n"
"s_waitcnt vmcnt(11) lgkmcnt(6)\n"
"s_lshl_b32 s92, s99, 13\n"
"s_add_u32 s92, s92, 0x1700000\n"
"v_mfma_f32_16x16x32_bf16 v[32:35], a[136:139], a[96:99], v[32:35]\n"
"ds_read_b64_tr_b16 a[152:153], v215 offset:64\n"
"s_cmp_gt_u32 s76, 0\n"
"s_cselect_b32 s92, s92, 0x40000000\n"
"s_cselect_b32 s91, s91, 0x40000000\n"
"s_waitcnt vmcnt(10)\n"
"v_mfma_f32_16x16x32_bf16 v[36:39], a[136:139], a[100:103], v[36:39]\n"
"ds_read_b64_tr_b16 a[154:155], v215 offset:3136\n"
"s_add_u32 s93, s92, 0x1000\n"
"s_waitcnt vmcnt(9)\n"
"v_mfma_f32_16x16x32_bf16 v[40:43], a[136:139], a[104:107], v[40:43]\n"
"v_mov_b32 v238, 0\n"
"s_waitcnt vmcnt(8)\n"
"v_mfma_f32_16x16x32_bf16 v[44:47], a[136:139], a[108:111], v[44:47]\n"
"s_waitcnt vmcnt(7) lgkmcnt(6)\n"
"s_mov_b32 s60, s46\n"
"s_and_b32 s61, s47, 0xffff\n"
"v_mfma_f32_16x16x32_bf16 v[32:35], a[140:143], a[112:115], v[32:35]\n"
"ds_read_b64_tr_b16 a[156:157], v215 offset:6208\n"
"s_mov_b32 s62, 0x700000\n"
"s_waitcnt vmcnt(6)\n"
"v_mfma_f32_16x16x32_bf16 v[36:39], a[140:143], a[116:119], v[36:39]\n"
"ds_read_b64_tr_b16 a[158:159], v215 offset:9280\n"
"s_xor_b32 s80, s36, 0x80\n"
"s_waitcnt vmcnt(5)\n"
"v_mfma_f32_16x16x32_bf16 v[40:43], a[140:143], a[120:123], v[40:43]\n"
"v_add_u32 v220, 0x9400, v254\n"
"v_xor_b32 v221, 0x60, v239\n"
"s_waitcnt vmcnt(4)\n"
"v_mfma_f32_16x16x32_bf16 v[44:47], a[140:143], a[124:127], v[44:47]\n"
"s_waitcnt lgkmcnt(6)\n"
"v_xor_b32 v222, 64, v239\n"
"v_xor_b32 v223, 32, v239\n"
"v_mfma_f32_16x16x32_bf16 v[48:51], a[144:147], a[64:67], v[48:51]\n"
"ds_read_b64_tr_b16 a[128:129], v211 offset:28800\n"
"v_mfma_f32_16x16x32_bf16 v[52:55], a[144:147], a[68:71], v[52:55]\n"
"ds_read_b64_tr_b16 a[130:131], v211 offset:31872\n"
"v_mfma_f32_16x16x32_bf16 v[56:59], a[144:147], a[72:75], v[56:59]\n"
"v_mfma_f32_16x16x32_bf16 v[60:63], a[144:147], a[76:79], v[60:63]\n"
"s_waitcnt lgkmcnt(6)\n"
"v_mfma_f32_16x16x32_bf16 v[48:51], a[148:151], a[80:83], v[48:51]\n"
"ds_read_b64_tr_b16 a[132:133], v211 offset:34944\n"
"v_cvt_pk_bf16_f32 v32, v32, v33\n"
"v_mfma_f32_16x16x32_bf16 v[52:55], a[148:151], a[84:87], v[52:55]\n"
"ds_read_b64_tr_b16 a[134:135], v211 offset:38016\n"
"v_cvt_pk_bf16_f32 v33, v34, v35\n"
"v_mfma_f32_16x16x32_bf16 v[56:59], a[148:151], a[88:91], v[56:59]\n"
"v_cvt_pk_bf16_f32 v36, v36, v37\n"
"v_cvt_pk_bf16_f32 v37, v38, v39\n"
"v_mfma_f32_16x16x32_bf16 v[60:63], a[148:151], a[92:95], v[60:63]\n"
"s_waitcnt lgkmcnt(6)\n"
"v_cvt_pk_bf16_f32 v40, v40, v41\n"
"v_cvt_pk_bf16_f32 v41, v42, v43\n"
"v_mfma_f32_16x16x32_bf16 v[48:51], a[152:155], a[96:99], v[48:51]\n"
"ds_read_b64_tr_b16 a[136:137], v214 offset:128\n"
"v_cvt_pk_bf16_f32 v44, v44, v45\n"
"v_mfma_f32_16x16x32_bf16 v[52:55], a[152:155], a[100:103], v[52:55]\n"
"ds_read_b64_tr_b16 a[138:139], v214 offset:3200\n"
"v_cvt_pk_bf16_f32 v45, v46, v47\n"
"v_mfma_f32_16x16x32_bf16 v[56:59], a[152:155], a[104:107], v[56:59]\n"
"v_mfma_f32_16x16x32_bf16 v[60:63], a[152:155], a[108:111], v[60:63]\n"
"s_waitcnt lgkmcnt(6)\n"
"v_mfma_f32_16x16x32_bf16 v[48:51], a[156:159], a[112:115], v[48:51]\n"
"ds_read_b64_tr_b16 a[140:141], v214 offset:6272\n"
"v_mfma_f32_16x16x32_bf16 v[52:55], a[156:159], a[116:119], v[52:55]\n"
"ds_read_b64_tr_b16 a[142:143], v214 offset:9344\n"
"v_mfma_f32_16x16x32_bf16 v[56:59], a[156:159], a[120:123], v[56:59]\n"
"v_mfma_f32_16x16x32_bf16 v[60:63], a[156:159], a[124:127], v[60:63]\n"
"s_waitcnt lgkmcnt(6)\n"
"v_mfma_f32_16x16x32_bf16 v[64:67], a[64:67], a[128:131], v[64:67]\n"
"ds_read_b64_tr_b16 a[144:145], v212 offset:28800\n"
"v_mfma_f32_16x16x32_bf16 v[68:71], a[68:71], a[128:131], v[68:71]\n"
"ds_read_b64_tr_b16 a[146:147], v212 offset:31872\n"
"v_mfma_f32_16x16x32_bf16 v[72:75], a[72:75], a[128:131], v[72:75]\n"
"v_mfma_f32_16x16x32_bf16 v[76:79], a[76:79], a[128:131], v[76:79]\n"
"s_waitcnt lgkmcnt(6)\n"
"v_mfma_f32_16x16x32_bf16 v[64:67], a[80:83], a[132:135], v[64:67]\n"
"ds_read_b64_tr_b16 a[148:149], v212 offset:34944\n"
"v_cvt_pk_bf16_f32 v34, v48, v49\n"
"v_mfma_f32_16x16x32_bf16 v[68:71], a[84:87], a[132:135], v[68:71]\n"
"ds_read_b64_tr_b16 a[150:151], v212 offset:38016\n"
"v_cvt_pk_bf16_f32 v35, v50, v51\n"
"v_mfma_f32_16x16x32_bf16 v[72:75], a[88:91], a[132:135], v[72:75]\n"
"buffer_store_dwordx4 v[32:35], v224, s[64:67], s55 offen sc1\n"
"v_cvt_pk_bf16_f32 v38, v52, v53\n"
"v_mfma_f32_16x16x32_bf16 v[76:79], a[92:95], a[132:135], v[76:79]\n"
"s_waitcnt lgkmcnt(6)\n"
"ds_write_b128 v237, v[32:35] offset:16384\n"
"v_cvt_pk_bf16_f32 v39, v54, v55\n"
"v_mfma_f32_16x16x32_bf16 v[64:67], a[96:99], a[136:139], v[64:67]\n"
"ds_read_b64_tr_b16 a[152:153], v215 offset:128\n"
"v_cvt_pk_bf16_f32 v42, v56, v57\n"
"v_mfma_f32_16x16x32_bf16 v[68:71], a[100:103], a[136:139], v[68:71]\n"
"ds_read_b64_tr_b16 a[154:155], v215 offset:3200\n"
"v_cvt_pk_bf16_f32 v43, v58, v59\n"
"v_mfma_f32_16x16x32_bf16 v[72:75], a[104:107], a[136:139], v[72:75]\n"
"buffer_store_dwordx4 v[36:39], v224, s[64:67], s55 offen offset:1024 sc1\n"
"v_cvt_pk_bf16_f32 v46, v60, v61\n"
"v_mfma_f32_16x16x32_bf16 v[76:79], a[108:111], a[136:139], v[76:79]\n"
"s_waitcnt lgkmcnt(7)\n"
"ds_write_b128 v237, v[36:39] offset:17408\n"
"v_cvt_pk_bf16_f32 v47, v62, v63\n"
"v_mfma_f32_16x16x32_bf16 v[64:67], a[112:115], a[140:143], v[64:67]\n"
"ds_read_b64_tr_b16 a[156:157], v215 offset:6272\n"
"v_mfma_f32_16x16x32_bf16 v[68:71], a[116:119], a[140:143], v[68:71]\n"
"ds_read_b64_tr_b16 a[158:159], v215 offset:9344\n"
"v_mfma_f32_16x16x32_bf16 v[72:75], a[120:123], a[140:143], v[72:75]\n"
"buffer_store_dwordx4 v[40:43], v224, s[64:67], s55 offen offset:2048 sc1\n"
"v_mfma_f32_16x16x32_bf16 v[76:79], a[124:127], a[140:143], v[76:79]\n"
"s_waitcnt lgkmcnt(8)\n"
"ds_write_b128 v237, v[40:43] offset:18432\n"
"s_waitcnt vmcnt(3)\n"
"s_barrier\n"
"v_mfma_f32_16x16x32_bf16 v[80:83], a[64:67], a[144:147], v[80:83]\n"
"ds_read_b64_tr_b16 a[128:129], v213 offset:40960\n"
"v_mfma_f32_16x16x32_bf16 v[84:87], a[68:71], a[144:147], v[84:87]\n"
"ds_read_b64_tr_b16 a[130:131], v213 offset:45056\n"
"v_mfma_f32_16x16x32_bf16 v[88:91], a[72:75], a[144:147], v[88:91]\n"
"buffer_store_dwordx4 v[44:47], v224, s[64:67], s55 offen offset:3072 sc1\n"
"v_mfma_f32_16x16x32_bf16 v[92:95], a[76:79], a[144:147], v[92:95]\n"
"s_waitcnt lgkmcnt(9)\n"
"ds_write_b128 v237, v[44:47] offset:19456\n"
"v_mfma_f32_16x16x32_bf16 v[80:83], a[80:83], a[148:151], v[80:83]\n"
"ds_read_b64_tr_b16 a[132:133], v213 offset:49152\n"
"v_cvt_pk_bf16_f32 v64, v64, v65\n"
"v_mfma_f32_16x16x32_bf16 v[84:87], a[84:87], a[148:151], v[84:87]\n"
"ds_read_b64_tr_b16 a[134:135], v213 offset:53248\n"
"v_cvt_pk_bf16_f32 v65, v66, v67\n"
"v_mfma_f32_16x16x32_bf16 v[88:91], a[88:91], a[148:151], v[88:91]\n"
"v_cvt_pk_bf16_f32 v66, v68, v69\n"
"v_cvt_pk_bf16_f32 v67, v70, v71\n"
"v_mfma_f32_16x16x32_bf16 v[92:95], a[92:95], a[148:151], v[92:95]\n"
"s_waitcnt lgkmcnt(9)\n"
"buffer_store_dwordx4 v[64:67], v224, s[64:67], s28 offen sc1\n"
"v_cvt_pk_bf16_f32 v72, v72, v73\n"
"v_mfma_f32_16x16x32_bf16 v[80:83], a[96:99], a[152:155], v[80:83]\n"
"ds_read_b64_tr_b16 a[136:137], v216 offset:12288\n"
"v_cvt_pk_bf16_f32 v73, v74, v75\n"
"v_mfma_f32_16x16x32_bf16 v[84:87], a[100:103], a[152:155], v[84:87]\n"
"ds_read_b64_tr_b16 a[138:139], v216 offset:16384\n"
"v_cvt_pk_bf16_f32 v74, v76, v77\n"
"v_mfma_f32_16x16x32_bf16 v[88:91], a[104:107], a[152:155], v[88:91]\n"
"ds_write_b128 v237, v[64:67] offset:49152\n"
"v_cvt_pk_bf16_f32 v75, v78, v79\n"
"v_mfma_f32_16x16x32_bf16 v[92:95], a[108:111], a[152:155], v[92:95]\n"
"s_waitcnt lgkmcnt(9)\n"
"buffer_store_dwordx4 v[72:75], v224, s[64:67], s28 offen offset:2048 sc1\n"
"v_mfma_f32_16x16x32_bf16 v[80:83], a[112:115], a[156:159], v[80:83]\n"
"ds_read_b64_tr_b16 a[140:141], v216 offset:20480\n"
"v_mfma_f32_16x16x32_bf16 v[84:87], a[116:119], a[156:159], v[84:87]\n"
"ds_read_b64_tr_b16 a[142:143], v216 offset:24576\n"
"v_mfma_f32_16x16x32_bf16 v[88:91], a[120:123], a[156:159], v[88:91]\n"
"ds_write_b128 v237, v[72:75] offset:51200\n"
"v_mfma_f32_16x16x32_bf16 v[92:95], a[124:127], a[156:159], v[92:95]\n"
"s_waitcnt lgkmcnt(9)\n"
"v_mfma_f32_16x16x32_bf16 v[96:99], a[128:131], a[64:67], v[96:99]\n"
"ds_read_b64_tr_b16 a[144:145], v213 offset:40992\n"
"v_mfma_f32_16x16x32_bf16 v[100:103], a[128:131], a[68:71], v[100:103]\n"
"ds_read_b64_tr_b16 a[146:147], v213 offset:45088\n"
"v_mfma_f32_16x16x32_bf16 v[104:107], a[128:131], a[72:75], v[104:107]\n"
"v_mfma_f32_16x16x32_bf16 v[108:111], a[128:131], a[76:79], v[108:111]\n"
"s_waitcnt lgkmcnt(8)\n"
"v_mfma_f32_16x16x32_bf16 v[96:99], a[132:135], a[80:83], v[96:99]\n"
"ds_read_b64_tr_b16 a[148:149], v213 offset:49184\n"
"v_cvt_pk_bf16_f32 v80, v80, v81\n"
"v_mfma_f32_16x16x32_bf16 v[100:103], a[132:135], a[84:87], v[100:103]\n"
"ds_read_b64_tr_b16 a[150:151], v213 offset:53280\n"
"v_cvt_pk_bf16_f32 v81, v82, v83\n"
"v_mfma_f32_16x16x32_bf16 v[104:107], a[132:135], a[88:91], v[104:107]\n"
"v_cvt_pk_bf16_f32 v82, v84, v85\n"
"v_cvt_pk_bf16_f32 v83, v86, v87\n"
"v_mfma_f32_16x16x32_bf16 v[108:111], a[132:135], a[92:95], v[108:111]\n"
"s_waitcnt lgkmcnt(8)\n"
"buffer_store_dwordx4 v[80:83], v224, s[64:67], s28 offen offset:1024 sc1\n"
"v_cvt_pk_bf16_f32 v88, v88, v89\n"
"v_mfma_f32_16x16x32_bf16 v[96:99], a[136:139], a[96:99], v[96:99]\n"
"ds_read_b64_tr_b16 a[152:153], v216 offset:12320\n"
"v_cvt_pk_bf16_f32 v89, v90, v91\n"
"v_mfma_f32_16x16x32_bf16 v[100:103], a[136:139], a[100:103], v[100:103]\n"
"ds_read_b64_tr_b16 a[154:155], v216 offset:16416\n"
"v_cvt_pk_bf16_f32 v90, v92, v93\n"
"v_mfma_f32_16x16x32_bf16 v[104:107], a[136:139], a[104:107], v[104:107]\n"
"ds_write_b128 v237, v[80:83] offset:50176\n"
"v_cvt_pk_bf16_f32 v91, v94, v95\n"
"v_mfma_f32_16x16x32_bf16 v[108:111], a[136:139], a[108:111], v[108:111]\n"
"s_waitcnt lgkmcnt(8)\n"
"buffer_store_dwordx4 v[88:91], v224, s[64:67], s28 offen offset:3072 sc1\n"
"v_mfma_f32_16x16x32_bf16 v[96:99], a[140:143], a[112:115], v[96:99]\n"
"ds_read_b64_tr_b16 a[156:157], v216 offset:20512\n"
"v_mfma_f32_16x16x32_bf16 v[100:103], a[140:143], a[116:119], v[100:103]\n"
"ds_read_b64_tr_b16 a[158:159], v216 offset:24608\n"
"v_mfma_f32_16x16x32_bf16 v[104:107], a[140:143], a[120:123], v[104:107]\n"
"ds_write_b128 v237, v[88:91] offset:52224\n"
"v_mfma_f32_16x16x32_bf16 v[108:111], a[140:143], a[124:127], v[108:111]\n"
"s_waitcnt lgkmcnt(8)\n"
"s_nop 0\n"
"v_pk_mul_f32 v[240:241], v[96:97], v[96:97]\n"
"v_pk_mul_f32 v[242:243], v[98:99], v[98:99]\n"
"v_pk_mul_f32 v[244:245], v[100:101], v[100:101]\n"
"v_pk_mul_f32 v[246:247], v[102:103], v[102:103]\n"
"v_pk_mul_f32 v[248:249], v[104:105], v[104:105]\n"
"v_pk_mul_f32 v[250:251], v[106:107], v[106:107]\n"
"v_pk_mul_f32 v[252:253], v[108:109], v[108:109]\n"
"v_pk_mul_f32 v[254:255], v[110:111], v[110:111]\n"
"v_pk_fma_f32 v[240:241], v[240:241], s[88:89], v[232:233] op_sel_hi:[1,0,1]\n"
"v_pk_fma_f32 v[242:243], v[242:243], s[88:89], v[232:233] op_sel_hi:[1,0,1]\n"
"v_pk_fma_f32 v[244:245], v[244:245], s[88:89], v[232:233] op_sel_hi:[1,0,1]\n"
"v_pk_fma_f32 v[246:247], v[246:247], s[88:89], v[232:233] op_sel_hi:[1,0,1]\n"
"v_pk_fma_f32 v[248:249], v[248:249], s[88:89], v[232:233] op_sel_hi:[1,0,1]\n"
"v_pk_fma_f32 v[250:251], v[250:251], s[88:89], v[232:233] op_sel_hi:[1,0,1]\n"
"v_pk_fma_f32 v[252:253], v[252:253], s[88:89], v[232:233] op_sel_hi:[1,0,1]\n"
"v_pk_fma_f32 v[254:255], v[254:255], s[88:89], v[232:233] op_sel_hi:[1,0,1]\n"
"v_pk_mul_f32 v[240:241], v[240:241], v[96:97]\n"
"v_pk_mul_f32 v[242:243], v[242:243], v[98:99]\n"
"v_pk_mul_f32 v[244:245], v[244:245], v[100:101]\n"
"v_pk_mul_f32 v[246:247], v[246:247], v[102:103]\n"
"v_pk_mul_f32 v[248:249], v[248:249], v[104:105]\n"
"v_pk_mul_f32 v[250:251], v[250:251], v[106:107]\n"
"v_pk_mul_f32 v[252:253], v[252:253], v[108:109]\n"
"v_pk_mul_f32 v[254:255], v[254:255], v[110:111]\n"
"v_mfma_f32_16x16x32_bf16 v[112:115], a[144:147], a[64:67], v[112:115]\n"
".Lpad6_%=:\n"
"s_cbranch_execz .Lpad10_%=\n"
"ds_read_b64_tr_b16 a[128:129], v213 offset:41024\n"
"v_exp_f32 v240, v240\n"
"v_mfma_f32_16x16x32_bf16 v[116:119], a[144:147], a[68:71], v[116:119]\n"
"ds_read_b64_tr_b16 a[130:131], v213 offset:45120\n"
"v_exp_f32 v241, v241\n"
"v_mfma_f32_16x16x32_bf16 v[120:123], a[144:147], a[72:75], v[120:123]\n"
"v_exp_f32 v242, v242\n"
"v_mfma_f32_16x16x32_bf16 v[124:127], a[144:147], a[76:79], v[124:127]\n"
"s_waitcnt lgkmcnt(8)\n"
"v_exp_f32 v243, v243\n"
"v_mfma_f32_16x16x32_bf16 v[112:115], a[148:151], a[80:83], v[112:115]\n"
"ds_read_b64_tr_b16 a[132:133], v213 offset:49216\n"
"v_exp_f32 v244, v244\n"
"v_mfma_f32_16x16x32_bf16 v[116:119], a[148:151], a[84:87], v[116:119]\n"
"ds_read_b64_tr_b16 a[134:135], v213 offset:53312\n"
"v_exp_f32 v245, v245\n"
"v_mfma_f32_16x16x32_bf16 v[120:123], a[148:151], a[88:91], v[120:123]\n"
"v_exp_f32 v246, v246\n"
"v_mfma_f32_16x16x32_bf16 v[124:127], a[148:151], a[92:95], v[124:127]\n"
"s_waitcnt lgkmcnt(8)\n"
"v_exp_f32 v247, v247\n"
"v_mfma_f32_16x16x32_bf16 v[112:115], a[152:155], a[96:99], v[112:115]\n"
"ds_read_b64_tr_b16 a[136:137], v216 offset:12352\n"
"v_exp_f32 v248, v248\n"
"v_mfma_f32_16x16x32_bf16 v[116:119], a[152:155], a[100:103], v[116:119]\n"
"ds_read_b64_tr_b16 a[138:139], v216 offset:16448\n"
"v_exp_f32 v249, v249\n"
"v_mfma_f32_16x16x32_bf16 v[120:123], a[152:155], a[104:107], v[120:123]\n"
"v_exp_f32 v250, v250\n"
"v_mfma_f32_16x16x32_bf16 v[124:127], a[152:155], a[108:111], v[124:127]\n"
"s_waitcnt lgkmcnt(7)\n"
"v_exp_f32 v251, v251\n"
"v_mfma_f32_16x16x32_bf16 v[112:115], a[156:159], a[112:115], v[112:115]\n"
"ds_read_b64_tr_b16 a[140:141], v216 offset:20544\n"
"v_exp_f32 v252, v252\n"
"v_exp_f32 v253, v253\n"
"v_exp_f32 v254, v254\n"
"v_exp_f32 v255, v255\n"
"v_pk_add_f32 v[240:241], v[240:241], 1.0 op_sel_hi:[1,0]\n"
"v_pk_add_f32 v[242:243], v[242:243], 1.0 op_sel_hi:[1,0]\n"
"v_pk_add_f32 v[244:245], v[244:245], 1.0 op_sel_hi:[1,0]\n"
"v_pk_add_f32 v[246:247], v[246:247], 1.0 op_sel_hi:[1,0]\n"
"v_pk_add_f32 v[248:249], v[248:249], 1.0 op_sel_hi:[1,0]\n"
"v_pk_add_f32 v[250:251], v[250:251], 1.0 op_sel_hi:[1,0]\n"
"v_pk_add_f32 v[252:253], v[252:253], 1.0 op_sel_hi:[1,0]\n"
"v_pk_add_f32 v[254:255], v[254:255], 1.0 op_sel_hi:[1,0]\n"
"v_mfma_f32_16x16x32_bf16 v[116:119], a[156:159], a[116:119], v[116:119]\n"
"ds_read_b64_tr_b16 a[142:143], v216 offset:24640\n"
"v_rcp_f32 v240, v240\n"
"v_mfma_f32_16x16x32_bf16 v[120:123], a[156:159], a[120:123], v[120:123]\n"
"v_rcp_f32 v241, v241\n"
"v_mfma_f32_16x16x32_bf16 v[124:127], a[156:159], a[124:127], v[124:127]\n"
"v_rcp_f32 v242, v242\n"
"v_mfma_f32_16x16x32_bf16 v[192:195], a[192:195], a[0:3], v[192:195]\n"
"v_rcp_f32 v243, v243\n"
"v_mfma_f32_16x16x32_bf16 v[196:199], a[192:195], a[4:7], v[196:199]\n"
"v_rcp_f32 v244, v244\n"
"v_mfma_f32_16x16x32_bf16 v[200:203], a[192:195], a[8:11], v[200:203]\n"
"v_rcp_f32 v245, v245\n"
"v_mfma_f32_16x16x32_bf16 v[204:207], a[192:195], a[12:15], v[204:207]\n"
"v_rcp_f32 v246, v246\n"
"v_mfma_f32_16x16x32_bf16 v[192:195], a[196:199], a[16:19], v[192:195]\n"
"v_rcp_f32 v247, v247\n"
"v_mfma_f32_16x16x32_bf16 v[196:199], a[196:199], a[20:23], v[196:199]\n"
"v_rcp_f32 v248, v248\n"
"v_mfma_f32_16x16x32_bf16 v[200:203], a[196:199], a[24:27], v[200:203]\n"
"v_rcp_f32 v249, v249\n"
"v_mfma_f32_16x16x32_bf16 v[204:207], a[196:199], a[28:31], v[204:207]\n"
"v_rcp_f32 v250, v250\n"
"v_mfma_f32_16x16x32_bf16 v[192:195], a[200:203], a[32:35], v[192:195]\n"
"v_rcp_f32 v251, v251\n"
"v_mfma_f32_16x16x32_bf16 v[196:199], a[200:203], a[36:39], v[196:199]\n"
"v_rcp_f32 v252, v252\n"
"v_rcp_f32 v253, v253\n"
"v_rcp_f32 v254, v254\n"
"v_rcp_f32 v255, v255\n"
"v_pk_mul_f32 v[240:241], v[240:241], v[96:97]\n"
"v_pk_mul_f32 v[242:243], v[242:243], v[98:99]\n"
"v_pk_mul_f32 v[244:245], v[244:245], v[100:101]\n"
"v_pk_mul_f32 v[246:247], v[246:247], v[102:103]\n"
"v_pk_mul_f32 v[248:249], v[248:249], v[104:105]\n"
"v_pk_mul_f32 v[250:251], v[250:251], v[106:107]\n"
"v_pk_mul_f32 v[252:253], v[252:253], v[108:109]\n"
"v_pk_mul_f32 v[254:255], v[254:255], v[110:111]\n"
"v_cvt_pk_bf16_f32 v96, v240, v241\n"
"v_cvt_pk_bf16_f32 v97, v242, v243\n"
"buffer_store_dwordx2 v[96:97], v227, s[64:67], s98 offen sc1\n"
"v_cvt_pk_bf16_f32 v100, v244, v245\n"
"v_cvt_pk_bf16_f32 v101, v246, v247\n"
"v_cvt_pk_bf16_f32 v104, v248, v249\n"
"v_cvt_pk_bf16_f32 v105, v250, v251\n"
"v_cvt_pk_bf16_f32 v108, v252, v253\n"
"v_cvt_pk_bf16_f32 v109, v254, v255\n"
"v_pk_mul_f32 v[240:241], v[112:113], v[112:113]\n"
"v_pk_mul_f32 v[242:243], v[114:115], v[114:115]\n"
"v_pk_mul_f32 v[244:245], v[116:117], v[116:117]\n"
"v_pk_mul_f32 v[246:247], v[118:119], v[118:119]\n"
"v_pk_mul_f32 v[248:249], v[120:121], v[120:121]\n"
"v_pk_mul_f32 v[250:251], v[122:123], v[122:123]\n"
"v_pk_mul_f32 v[252:253], v[124:125], v[124:125]\n"
"v_pk_mul_f32 v[254:255], v[126:127], v[126:127]\n"
"buffer_store_dwordx2 v[100:101], v227, s[64:67], s98 offen offset:1024 sc1\n"
"v_pk_fma_f32 v[240:241], v[240:241], s[88:89], v[232:233] op_sel_hi:[1,0,1]\n"
"v_pk_fma_f32 v[242:243], v[242:243], s[88:89], v[232:233] op_sel_hi:[1,0,1]\n"
"v_pk_fma_f32 v[244:245], v[244:245], s[88:89], v[232:233] op_sel_hi:[1,0,1]\n"
"v_pk_fma_f32 v[246:247], v[246:247], s[88:89], v[232:233] op_sel_hi:[1,0,1]\n"
"v_pk_fma_f32 v[248:249], v[248:249], s[88:89], v[232:233] op_sel_hi:[1,0,1]\n"
"v_pk_fma_f32 v[250:251], v[250:251], s[88:89], v[232:233] op_sel_hi:[1,0,1]\n"
"v_pk_fma_f32 v[252:253], v[252:253], s[88:89], v[232:233] op_sel_hi:[1,0,1]\n"
"v_pk_fma_f32 v[254:255], v[254:255], s[88:89], v[232:233] op_sel_hi:[1,0,1]\n"
"v_pk_mul_f32 v[240:241], v[240:241], v[112:113]\n"
"v_pk_mul_f32 v[242:243], v[242:243], v[114:115]\n"
"v_pk_mul_f32 v[244:245], v[244:245], v[116:117]\n"
"v_pk_mul_f32 v[246:247], v[246:247], v[118:119]\n"
"v_pk_mul_f32 v[248:249], v[248:249], v[120:121]\n"
"v_pk_mul_f32 v[250:251], v[250:251], v[122:123]\n"
"v_pk_mul_f32 v[252:253], v[252:253], v[124:125]\n"
"v_pk_mul_f32 v[254:255], v[254:255], v[126:127]\n"
"s_waitcnt lgkmcnt(6)\n"
"v_mfma_f32_16x16x32_bf16 v[128:131], a[128:131], a[64:67], v[128:131]\n"
"ds_read_b64_tr_b16 a[144:145], v213 offset:41056\n"
"v_exp_f32 v240, v240\n"
"v_mfma_f32_16x16x32_bf16 v[132:135], a[128:131], a[68:71], v[132:135]\n"
"ds_read_b64_tr_b16 a[146:147], v213 offset:45152\n"
"v_exp_f32 v241, v241\n"
"v_mfma_f32_16x16x32_bf16 v[136:139], a[128:131], a[72:75], v[136:139]\n"
"buffer_store_dwordx2 v[104:105], v227, s[64:67], s98 offen offset:2048 sc1\n"
"v_exp_f32 v242, v242\n"
"v_mfma_f32_16x16x32_bf16 v[140:143], a[128:131], a[76:79], v[140:143]\n"
"s_waitcnt lgkmcnt(6)\n"
"v_exp_f32 v243, v243\n"
"v_mfma_f32_16x16x32_bf16 v[128:131], a[132:135], a[80:83], v[128:131]\n"
"ds_read_b64_tr_b16 a[148:149], v213 offset:49248\n"
"v_exp_f32 v244, v244\n"
"v_mfma_f32_16x16x32_bf16 v[132:135], a[132:135], a[84:87], v[132:135]\n"
"ds_read_b64_tr_b16 a[150:151], v213 offset:53344\n"
"v_exp_f32 v245, v245\n"
"v_mfma_f32_16x16x32_bf16 v[136:139], a[132:135], a[88:91], v[136:139]\n"
"buffer_store_dwordx2 v[108:109], v227, s[64:67], s98 offen offset:3072 sc1\n"
"s_add_u32 s98, s98, s96\n"
"v_exp_f32 v246, v246\n"
"v_mfma_f32_16x16x32_bf16 v[140:143], a[132:135], a[92:95], v[140:143]\n"
"s_waitcnt lgkmcnt(6)\n"
"v_exp_f32 v247, v247\n"
"v_mfma_f32_16x16x32_bf16 v[128:131], a[136:139], a[96:99], v[128:131]\n"
"ds_read_b64_tr_b16 a[152:153], v216 offset:12384\n"
"v_exp_f32 v248, v248\n"
"v_mfma_f32_16x16x32_bf16 v[132:135], a[136:139], a[100:103], v[132:135]\n"
"ds_read_b64_tr_b16 a[154:155], v216 offset:16480\n"
"v_exp_f32 v249, v249\n"
"v_mfma_f32_16x16x32_bf16 v[136:139], a[136:139], a[104:107], v[136:139]\n"
"v_exp_f32 v250, v250\n"
"v_mfma_f32_16x16x32_bf16 v[140:143], a[136:139], a[108:111], v[140:143]\n"
"s_waitcnt lgkmcnt(6)\n"
"v_exp_f32 v251, v251\n"
"v_mfma_f32_16x16x32_bf16 v[128:131], a[140:143], a[112:115], v[128:131]\n"
"ds_read_b64_tr_b16 a[156:157], v216 offset:20576\n"
"v_exp_f32 v252, v252\n"
"v_exp_f32 v253, v253\n"
"v_exp_f32 v254, v254\n"
"v_exp_f32 v255, v255\n"
"v_pk_add_f32 v[240:241], v[240:241], 1.0 op_sel_hi:[1,0]\n"
"v_pk_add_f32 v[242:243], v[242:243], 1.0 op_sel_hi:[1,0]\n"
"v_pk_add_f32 v[244:245], v[244:245], 1.0 op_sel_hi:[1,0]\n"
"v_pk_add_f32 v[246:247], v[246:247], 1.0 op_sel_hi:[1,0]\n"
"v_pk_add_f32 v[248:249], v[248:249], 1.0 op_sel_hi:[1,0]\n"
"v_pk_add_f32 v[250:251], v[250:251], 1.0 op_sel_hi:[1,0]\n"
"v_pk_add_f32 v[252:253], v[252:253], 1.0 op_sel_hi:[1,0]\n"
"v_pk_add_f32 v[254:255], v[254:255], 1.0 op_sel_hi:[1,0]\n"
"v_mfma_f32_16x16x32_bf16 v[132:135], a[140:143], a[116:119], v[132:135]\n"
"ds_read_b64_tr_b16 a[158:159], v216 offset:24672\n"
"v_rcp_f32 v240, v240\n"
"v_mfma_f32_16x16x32_bf16 v[136:139], a[140:143], a[120:123], v[136:139]\n"
"v_rcp_f32 v241, v241\n"
"v_mfma_f32_16x16x32_bf16 v[140:143], a[140:143], a[124:127], v[140:143]\n"
"v_rcp_f32 v242, v242\n"
"v_mfma_f32_16x16x32_bf16 v[200:203], a[200:203], a[40:43], v[200:203]\n"
"v_rcp_f32 v243, v243\n"
"v_mfma_f32_16x16x32_bf16 v[204:207], a[200:203], a[44:47], v[204:207]\n"
"v_rcp_f32 v244, v244\n"
"v_mfma_f32_16x16x32_bf16 v[192:195], a[204:207], a[48:51], v[192:195]\n"
"v_rcp_f32 v245, v245\n"
"v_mfma_f32_16x16x32_bf16 v[196:199], a[204:207], a[52:55], v[196:199]\n"
"v_rcp_f32 v246, v246\n"
"v_mfma_f32_16x16x32_bf16 v[200:203], a[204:207], a[56:59], v[200:203]\n"
"v_rcp_f32 v247, v247\n"
"v_mfma_f32_16x16x32_bf16 v[204:207], a[204:207], a[60:63], v[204:207]\n"
"v_rcp_f32 v248, v248\n"
"v_mfma_f32_16x16x32_bf16 v[0:3], a[208:211], a[0:3], v[0:3]\n"
"v_rcp_f32 v249, v249\n"
"v_mfma_f32_16x16x32_bf16 v[4:7], a[208:211], a[4:7], v[4:7]\n"
"v_rcp_f32 v250, v250\n"
"v_mfma_f32_16x16x32_bf16 v[8:11], a[208:211], a[8:11], v[8:11]\n"
"v_rcp_f32 v251, v251\n"
"v_mfma_f32_16x16x32_bf16 v[12:15], a[208:211], a[12:15], v[12:15]\n"
"v_rcp_f32 v252, v252\n"
"v_rcp_f32 v253, v253\n"
"v_rcp_f32 v254, v254\n"
"v_rcp_f32 v255, v255\n"
"v_pk_mul_f32 v[240:241], v[240:241], v[112:113]\n"
"v_pk_mul_f32 v[242:243], v[242:243], v[114:115]\n"
"v_pk_mul_f32 v[244:245], v[244:245], v[116:117]\n"
"v_pk_mul_f32 v[246:247], v[246:247], v[118:119]\n"
"v_pk_mul_f32 v[248:249], v[248:249], v[120:121]\n"
"v_pk_mul_f32 v[250:251], v[250:251], v[122:123]\n"
"v_pk_mul_f32 v[252:253], v[252:253], v[124:125]\n"
"v_pk_mul_f32 v[254:255], v[254:255], v[126:127]\n"
"v_cvt_pk_bf16_f32 v112, v240, v241\n"
"v_cvt_pk_bf16_f32 v113, v242, v243\n"
"buffer_store_dwordx2 v[112:113], v227, s[64:67], s98 offen sc1\n"
"v_cvt_pk_bf16_f32 v116, v244, v245\n"
"v_cvt_pk_bf16_f32 v117, v246, v247\n"
"v_cvt_pk_bf16_f32 v120, v248, v249\n"
"v_cvt_pk_bf16_f32 v121, v250, v251\n"
"v_cvt_pk_bf16_f32 v124, v252, v253\n"
"v_cvt_pk_bf16_f32 v125, v254, v255\n"
"v_pk_mul_f32 v[240:241], v[128:129], v[128:129]\n"
"v_pk_mul_f32 v[242:243], v[130:131], v[130:131]\n"
"v_pk_mul_f32 v[244:245], v[132:133], v[132:133]\n"
"v_pk_mul_f32 v[246:247], v[134:135], v[134:135]\n"
"v_pk_mul_f32 v[248:249], v[136:137], v[136:137]\n"
"v_pk_mul_f32 v[250:251], v[138:139], v[138:139]\n"
"v_pk_mul_f32 v[252:253], v[140:141], v[140:141]\n"
"v_pk_mul_f32 v[254:255], v[142:143], v[142:143]\n"
"s_waitcnt vmcnt(5)\n"
"buffer_store_dwordx2 v[228:229], v225, s[64:67], s90 offen sc1\n"
"v_pk_fma_f32 v[240:241], v[240:241], s[88:89], v[232:233] op_sel_hi:[1,0,1]\n"
"v_pk_fma_f32 v[242:243], v[242:243], s[88:89], v[232:233] op_sel_hi:[1,0,1]\n"
"v_pk_fma_f32 v[244:245], v[244:245], s[88:89], v[232:233] op_sel_hi:[1,0,1]\n"
"v_pk_fma_f32 v[246:247], v[246:247], s[88:89], v[232:233] op_sel_hi:[1,0,1]\n"
"v_pk_fma_f32 v[248:249], v[248:249], s[88:89], v[232:233] op_sel_hi:[1,0,1]\n"
"v_pk_fma_f32 v[250:251], v[250:251], s[88:89], v[232:233] op_sel_hi:[1,0,1]\n"
"v_pk_fma_f32 v[252:253], v[252:253], s[88:89], v[232:233] op_sel_hi:[1,0,1]\n"
"v_pk_fma_f32 v[254:255], v[254:255], s[88:89], v[232:233] op_sel_hi:[1,0,1]\n"
"v_pk_mul_f32 v[240:241], v[240:241], v[128:129]\n"
"v_pk_mul_f32 v[242:243], v[242:243], v[130:131]\n"
"v_pk_mul_f32 v[244:245], v[244:245], v[132:133]\n"
"v_pk_mul_f32 v[246:247], v[246:247], v[134:135]\n"
"v_pk_mul_f32 v[248:249], v[248:249], v[136:137]\n"
"v_pk_mul_f32 v[250:251], v[250:251], v[138:139]\n"
"v_pk_mul_f32 v[252:253], v[252:253], v[140:141]\n"
"v_pk_mul_f32 v[254:255], v[254:255], v[142:143]\n"
"s_waitcnt lgkmcnt(6)\n"
"v_mfma_f32_16x16x32_bf16 v[144:147], a[144:147], a[64:67], v[144:147]\n"
"ds_read_b64_tr_b16 a[128:129], v213 offset:41088\n"
"v_exp_f32 v240, v240\n"
"v_mfma_f32_16x16x32_bf16 v[148:151], a[144:147], a[68:71], v[148:151]\n"
"ds_read_b64_tr_b16 a[130:131], v213 offset:45184\n"
"v_exp_f32 v241, v241\n"
"v_mfma_f32_16x16x32_bf16 v[152:155], a[144:147], a[72:75], v[152:155]\n"
"buffer_store_dwordx2 v[116:117], v227, s[64:67], s98 offen offset:1024 sc1\n"
"v_exp_f32 v242, v242\n"
"v_mfma_f32_16x16x32_bf16 v[156:159], a[144:147], a[76:79], v[156:159]\n"
"s_waitcnt lgkmcnt(6)\n"
"v_exp_f32 v243, v243\n"
"v_mfma_f32_16x16x32_bf16 v[144:147], a[148:151], a[80:83], v[144:147]\n"
"ds_read_b64_tr_b16 a[132:133], v213 offset:49280\n"
"v_exp_f32 v244, v244\n"
"v_mfma_f32_16x16x32_bf16 v[148:151], a[148:151], a[84:87], v[148:151]\n"
"ds_read_b64_tr_b16 a[134:135], v213 offset:53376\n"
"v_exp_f32 v245, v245\n"
"v_mfma_f32_16x16x32_bf16 v[152:155], a[148:151], a[88:91], v[152:155]\n"
"buffer_store_dwordx2 v[120:121], v227, s[64:67], s98 offen offset:2048 sc1\n"
"v_exp_f32 v246, v246\n"
"v_mfma_f32_16x16x32_bf16 v[156:159], a[148:151], a[92:95], v[156:159]\n"
"s_waitcnt lgkmcnt(6)\n"
"v_exp_f32 v247, v247\n"
"v_mfma_f32_16x16x32_bf16 v[144:147], a[152:155], a[96:99], v[144:147]\n"
"ds_read_b64_tr_b16 a[136:137], v216 offset:12416\n"
"v_exp_f32 v248, v248\n"
"v_mfma_f32_16x16x32_bf16 v[148:151], a[152:155], a[100:103], v[148:151]\n"
"ds_read_b64_tr_b16 a[138:139], v216 offset:16512\n"
"v_exp_f32 v249, v249\n"
"v_mfma_f32_16x16x32_bf16 v[152:155], a[152:155], a[104:107], v[152:155]\n"
"buffer_store_dwordx2 v[124:125], v227, s[64:67], s98 offen offset:3072 sc1\n"
"s_add_u32 s98, s98, s97\n"
"v_exp_f32 v250, v250\n"
"v_mfma_f32_16x16x32_bf16 v[156:159], a[152:155], a[108:111], v[156:159]\n"
"s_waitcnt lgkmcnt(6)\n"
"v_exp_f32 v251, v251\n"
"v_mfma_f32_16x16x32_bf16 v[144:147], a[156:159], a[112:115], v[144:147]\n"
"ds_read_b64_tr_b16 a[140:141], v216 offset:20608\n"
"v_exp_f32 v252, v252\n"
"v_exp_f32 v253, v253\n"
"v_exp_f32 v254, v254\n"
"v_exp_f32 v255, v255\n"
"v_pk_add_f32 v[240:241], v[240:241], 1.0 op_sel_hi:[1,0]\n"
"v_pk_add_f32 v[242:243], v[242:243], 1.0 op_sel_hi:[1,0]\n"
"v_pk_add_f32 v[244:245], v[244:245], 1.0 op_sel_hi:[1,0]\n"
"v_pk_add_f32 v[246:247], v[246:247], 1.0 op_sel_hi:[1,0]\n"
"v_pk_add_f32 v[248:249], v[248:249], 1.0 op_sel_hi:[1,0]\n"
"v_pk_add_f32 v[250:251], v[250:251], 1.0 op_sel_hi:[1,0]\n"
"v_pk_add_f32 v[252:253], v[252:253], 1.0 op_sel_hi:[1,0]\n"
"v_pk_add_f32 v[254:255], v[254:255], 1.0 op_sel_hi:[1,0]\n"
"v_mfma_f32_16x16x32_bf16 v[148:151], a[156:159], a[116:119], v[148:151]\n"
"ds_read_b64_tr_b16 a[142:143], v216 offset:24704\n"
"v_rcp_f32 v240, v240\n"
"v_mfma_f32_16x16x32_bf16 v[152:155], a[156:159], a[120:123], v[152:155]\n"
"v_rcp_f32 v241, v241\n"
"v_mfma_f32_16x16x32_bf16 v[156:159], a[156:159], a[124:127], v[156:159]\n"
"v_rcp_f32 v242, v242\n"
"v_mfma_f32_16x16x32_bf16 v[0:3], a[212:215], a[16:19], v[0:3]\n"
"v_rcp_f32 v243, v243\n"
"v_mfma_f32_16x16x32_bf16 v[4:7], a[212:215], a[20:23], v[4:7]\n"
"v_rcp_f32 v244, v244\n"
"v_mfma_f32_16x16x32_bf16 v[8:11], a[212:215], a[24:27], v[8:11]\n"
"v_rcp_f32 v245, v245\n"
"v_mfma_f32_16x16x32_bf16 v[12:15], a[212:215], a[28:31], v[12:15]\n"
"v_rcp_f32 v246, v246\n"
"v_mfma_f32_16x16x32_bf16 v[0:3], a[216:219], a[32:35], v[0:3]\n"
"v_rcp_f32 v247, v247\n"
"v_mfma_f32_16x16x32_bf16 v[4:7], a[216:219], a[36:39], v[4:7]\n"
"v_rcp_f32 v248, v248\n"
"v_mfma_f32_16x16x32_bf16 v[8:11], a[216:219], a[40:43], v[8:11]\n"
"v_rcp_f32 v249, v249\n"
"v_mfma_f32_16x16x32_bf16 v[12:15], a[216:219], a[44:47], v[12:15]\n"
"v_rcp_f32 v250, v250\n"
"v_mfma_f32_16x16x32_bf16 v[0:3], a[220:223], a[48:51], v[0:3]\n"
"v_rcp_f32 v251, v251\n"
"v_mfma_f32_16x16x32_bf16 v[4:7], a[220:223], a[52:55], v[4:7]\n"
"v_rcp_f32 v252, v252\n"
"v_rcp_f32 v253, v253\n"
"v_rcp_f32 v254, v254\n"
"v_rcp_f32 v255, v255\n"
"v_pk_mul_f32 v[240:241], v[240:241], v[128:129]\n"
"v_pk_mul_f32 v[242:243], v[242:243], v[130:131]\n"
"v_pk_mul_f32 v[244:245], v[244:245], v[132:133]\n"
"v_pk_mul_f32 v[246:247], v[246:247], v[134:135]\n"
"v_pk_mul_f32 v[248:249], v[248:249], v[136:137]\n"
"v_pk_mul_f32 v[250:251], v[250:251], v[138:139]\n"
"v_pk_mul_f32 v[252:253], v[252:253], v[140:141]\n"
"v_pk_mul_f32 v[254:255], v[254:255], v[142:143]\n"
"v_cvt_pk_bf16_f32 v128, v240, v241\n"
"v_cvt_pk_bf16_f32 v129, v242, v243\n"
"buffer_store_dwordx2 v[128:129], v227, s[64:67], s98 offen sc1\n"
"v_cvt_pk_bf16_f32 v132, v244, v245\n"
"v_cvt_pk_bf16_f32 v133, v246, v247\n"
"v_cvt_pk_bf16_f32 v136, v248, v249\n"
"v_cvt_pk_bf16_f32 v137, v250, v251\n"
"v_cvt_pk_bf16_f32 v140, v252, v253\n"
"v_cvt_pk_bf16_f32 v141, v254, v255\n"
"v_pk_mul_f32 v[240:241], v[144:145], v[144:145]\n"
"v_pk_mul_f32 v[242:243], v[146:147], v[146:147]\n"
"v_pk_mul_f32 v[244:245], v[148:149], v[148:149]\n"
"v_pk_mul_f32 v[246:247], v[150:151], v[150:151]\n"
"v_pk_mul_f32 v[248:249], v[152:153], v[152:153]\n"
"v_pk_mul_f32 v[250:251], v[154:155], v[154:155]\n"
"v_pk_mul_f32 v[252:253], v[156:157], v[156:157]\n"
"v_pk_mul_f32 v[254:255], v[158:159], v[158:159]\n"
"buffer_store_dwordx2 v[132:133], v227, s[64:67], s98 offen offset:1024 sc1\n"
"v_pk_fma_f32 v[240:241], v[240:241], s[88:89], v[232:233] op_sel_hi:[1,0,1]\n"
"v_pk_fma_f32 v[242:243], v[242:243], s[88:89], v[232:233] op_sel_hi:[1,0,1]\n"
"v_pk_fma_f32 v[244:245], v[244:245], s[88:89], v[232:233] op_sel_hi:[1,0,1]\n"
"v_pk_fma_f32 v[246:247], v[246:247], s[88:89], v[232:233] op_sel_hi:[1,0,1]\n"
"v_pk_fma_f32 v[248:249], v[248:249], s[88:89], v[232:233] op_sel_hi:[1,0,1]\n"
"v_pk_fma_f32 v[250:251], v[250:251], s[88:89], v[232:233] op_sel_hi:[1,0,1]\n"
"v_pk_fma_f32 v[252:253], v[252:253], s[88:89], v[232:233] op_sel_hi:[1,0,1]\n"
"v_pk_fma_f32 v[254:255], v[254:255], s[88:89], v[232:233] op_sel_hi:[1,0,1]\n"
"v_pk_mul_f32 v[240:241], v[240:241], v[144:145]\n"
"v_pk_mul_f32 v[242:243], v[242:243], v[146:147]\n"
"v_pk_mul_f32 v[244:245], v[244:245], v[148:149]\n"
"v_pk_mul_f32 v[246:247], v[246:247], v[150:151]\n"
"v_pk_mul_f32 v[248:249], v[248:249], v[152:153]\n"
"v_pk_mul_f32 v[250:251], v[250:251], v[154:155]\n"
"v_pk_mul_f32 v[252:253], v[252:253], v[156:157]\n"
"v_pk_mul_f32 v[254:255], v[254:255], v[158:159]\n"
"s_waitcnt lgkmcnt(6)\n"
"v_mfma_f32_16x16x32_bf16 v[160:163], a[128:131], a[64:67], v[160:163]\n"
"ds_read_b64_tr_b16 a[144:145], v213 offset:41120\n"
"v_exp_f32 v240, v240\n"
"v_mfma_f32_16x16x32_bf16 v[164:167], a[128:131], a[68:71], v[164:167]\n"
"ds_read_b64_tr_b16 a[146:147], v213 offset:45216\n"
"v_exp_f32 v241, v241\n"
"v_mfma_f32_16x16x32_bf16 v[168:171], a[128:131], a[72:75], v[168:171]\n"
"buffer_store_dwordx2 v[136:137], v227, s[64:67], s98 offen offset:2048 sc1\n"
"v_exp_f32 v242, v242\n"
"v_mfma_f32_16x16x32_bf16 v[172:175], a[128:131], a[76:79], v[172:175]\n"
"s_waitcnt lgkmcnt(6)\n"
"v_exp_f32 v243, v243\n"
"v_mfma_f32_16x16x32_bf16 v[160:163], a[132:135], a[80:83], v[160:163]\n"
"ds_read_b64_tr_b16 a[148:149], v213 offset:49312\n"
"v_exp_f32 v244, v244\n"
"v_mfma_f32_16x16x32_bf16 v[164:167], a[132:135], a[84:87], v[164:167]\n"
"ds_read_b64_tr_b16 a[150:151], v213 offset:53408\n"
"v_exp_f32 v245, v245\n"
"v_mfma_f32_16x16x32_bf16 v[168:171], a[132:135], a[88:91], v[168:171]\n"
"buffer_store_dwordx2 v[140:141], v227, s[64:67], s98 offen offset:3072 sc1\n"
"s_add_u32 s98, s98, s96\n"
"v_exp_f32 v246, v246\n"
"v_mfma_f32_16x16x32_bf16 v[172:175], a[132:135], a[92:95], v[172:175]\n"
"s_waitcnt lgkmcnt(6)\n"
"v_exp_f32 v247, v247\n"
"v_mfma_f32_16x16x32_bf16 v[160:163], a[136:139], a[96:99], v[160:163]\n"
"ds_read_b64_tr_b16 a[152:153], v216 offset:12448\n"
"v_exp_f32 v248, v248\n"
"v_mfma_f32_16x16x32_bf16 v[164:167], a[136:139], a[100:103], v[164:167]\n"
"ds_read_b64_tr_b16 a[154:155], v216 offset:16544\n"
"v_exp_f32 v249, v249\n"
"v_mfma_f32_16x16x32_bf16 v[168:171], a[136:139], a[104:107], v[168:171]\n"
"v_exp_f32 v250, v250\n"
"v_mfma_f32_16x16x32_bf16 v[172:175], a[136:139], a[108:111], v[172:175]\n"
"s_waitcnt lgkmcnt(6)\n"
"v_exp_f32 v251, v251\n"
"v_mfma_f32_16x16x32_bf16 v[160:163], a[140:143], a[112:115], v[160:163]\n"
"ds_read_b64_tr_b16 a[156:157], v216 offset:20640\n"
"v_exp_f32 v252, v252\n"
"v_exp_f32 v253, v253\n"
"v_exp_f32 v254, v254\n"
"v_exp_f32 v255, v255\n"
"v_pk_add_f32 v[240:241], v[240:241], 1.0 op_sel_hi:[1,0]\n"
"v_pk_add_f32 v[242:243], v[242:243], 1.0 op_sel_hi:[1,0]\n"
"v_pk_add_f32 v[244:245], v[244:245], 1.0 op_sel_hi:[1,0]\n"
"v_pk_add_f32 v[246:247], v[246:247], 1.0 op_sel_hi:[1,0]\n"
"v_pk_add_f32 v[248:249], v[248:249], 1.0 op_sel_hi:[1,0]\n"
"v_pk_add_f32 v[250:251], v[250:251], 1.0 op_sel_hi:[1,0]\n"
"v_pk_add_f32 v[252:253], v[252:253], 1.0 op_sel_hi:[1,0]\n"
"v_pk_add_f32 v[254:255], v[254:255], 1.0 op_sel_hi:[1,0]\n"
"v_mfma_f32_16x16x32_bf16 v[164:167], a[140:143], a[116:119], v[164:167]\n"
"ds_read_b64_tr_b16 a[158:159], v216 offset:24736\n"
"v_rcp_f32 v240, v240\n"
"v_mfma_f32_16x16x32_bf16 v[168:171], a[140:143], a[120:123], v[168:171]\n"
"buffer_load_dwordx2 v[234:235], v238, s[64:67], s91 offen sc1\n"
"v_rcp_f32 v241, v241\n"
"v_mfma_f32_16x16x32_bf16 v[172:175], a[140:143], a[124:127], v[172:175]\n"
"v_rcp_f32 v242, v242\n"
"v_mfma_f32_16x16x32_bf16 v[8:11], a[220:223], a[56:59], v[8:11]\n"
"v_rcp_f32 v243, v243\n"
"v_mfma_f32_16x16x32_bf16 v[12:15], a[220:223], a[60:63], v[12:15]\n"
"v_rcp_f32 v244, v244\n"
"v_mfma_f32_16x16x32_bf16 v[28:31], a[224:227], a[12:15], v[28:31]\n"
"v_rcp_f32 v245, v245\n"
"v_mfma_f32_16x16x32_bf16 v[24:27], a[224:227], a[8:11], v[24:27]\n"
"v_rcp_f32 v246, v246\n"
"v_mfma_f32_16x16x32_bf16 v[20:23], a[224:227], a[4:7], v[20:23]\n"
"v_rcp_f32 v247, v247\n"
"v_mfma_f32_16x16x32_bf16 v[16:19], a[224:227], a[0:3], v[16:19]\n"
"v_rcp_f32 v248, v248\n"
"v_mfma_f32_16x16x32_bf16 v[28:31], a[228:231], a[28:31], v[28:31]\n"
"v_rcp_f32 v249, v249\n"
"v_mfma_f32_16x16x32_bf16 v[24:27], a[228:231], a[24:27], v[24:27]\n"
"v_rcp_f32 v250, v250\n"
"v_mfma_f32_16x16x32_bf16 v[20:23], a[228:231], a[20:23], v[20:23]\n"
"v_rcp_f32 v251, v251\n"
"v_mfma_f32_16x16x32_bf16 v[16:19], a[228:231], a[16:19], v[16:19]\n"
"v_rcp_f32 v252, v252\n"
"v_rcp_f32 v253, v253\n"
"v_rcp_f32 v254, v254\n"
"v_rcp_f32 v255, v255\n"
"v_pk_mul_f32 v[240:241], v[240:241], v[144:145]\n"
"v_pk_mul_f32 v[242:243], v[242:243], v[146:147]\n"
"v_pk_mul_f32 v[244:245], v[244:245], v[148:149]\n"
"v_pk_mul_f32 v[246:247], v[246:247], v[150:151]\n"
"v_pk_mul_f32 v[248:249], v[248:249], v[152:153]\n"
"v_pk_mul_f32 v[250:251], v[250:251], v[154:155]\n"
"v_pk_mul_f32 v[252:253], v[252:253], v[156:157]\n"
"v_pk_mul_f32 v[254:255], v[254:255], v[158:159]\n"
"v_cvt_pk_bf16_f32 v144, v240, v241\n"
"v_cvt_pk_bf16_f32 v145, v242, v243\n"
"buffer_store_dwordx2 v[144:145], v227, s[64:67], s98 offen sc1\n"
"v_cvt_pk_bf16_f32 v148, v244, v245\n"
"v_cvt_pk_bf16_f32 v149, v246, v247\n"
"v_cvt_pk_bf16_f32 v152, v248, v249\n"
"v_cvt_pk_bf16_f32 v153, v250, v251\n"
"v_cvt_pk_bf16_f32 v156, v252, v253\n"
"v_cvt_pk_bf16_f32 v157, v254, v255\n"
"v_pk_mul_f32 v[240:241], v[160:161], v[160:161]\n"
"v_pk_mul_f32 v[242:243], v[162:163], v[162:163]\n"
"v_pk_mul_f32 v[244:245], v[164:165], v[164:165]\n"
"v_pk_mul_f32 v[246:247], v[166:167], v[166:167]\n"
"v_pk_mul_f32 v[248:249], v[168:169], v[168:169]\n"
"v_pk_mul_f32 v[250:251], v[170:171], v[170:171]\n"
"v_pk_mul_f32 v[252:253], v[172:173], v[172:173]\n"
"v_pk_mul_f32 v[254:255], v[174:175], v[174:175]\n"
"buffer_store_dwordx2 v[148:149], v227, s[64:67], s98 offen offset:1024 sc1\n"
"v_pk_fma_f32 v[240:241], v[240:241], s[88:89], v[232:233] op_sel_hi:[1,0,1]\n"
"v_pk_fma_f32 v[242:243], v[242:243], s[88:89], v[232:233] op_sel_hi:[1,0,1]\n"
"v_pk_fma_f32 v[244:245], v[244:245], s[88:89], v[232:233] op_sel_hi:[1,0,1]\n"
"v_pk_fma_f32 v[246:247], v[246:247], s[88:89], v[232:233] op_sel_hi:[1,0,1]\n"
"v_pk_fma_f32 v[248:249], v[248:249], s[88:89], v[232:233] op_sel_hi:[1,0,1]\n"
"v_pk_fma_f32 v[250:251], v[250:251], s[88:89], v[232:233] op_sel_hi:[1,0,1]\n"
"v_pk_fma_f32 v[252:253], v[252:253], s[88:89], v[232:233] op_sel_hi:[1,0,1]\n"
"v_pk_fma_f32 v[254:255], v[254:255], s[88:89], v[232:233] op_sel_hi:[1,0,1]\n"
"v_pk_mul_f32 v[240:241], v[240:241], v[160:161]\n"
"v_pk_mul_f32 v[242:243], v[242:243], v[162:163]\n"
"v_pk_mul_f32 v[244:245], v[244:245], v[164:165]\n"
"v_pk_mul_f32 v[246:247], v[246:247], v[166:167]\n"
"v_pk_mul_f32 v[248:249], v[248:249], v[168:169]\n"
"v_pk_mul_f32 v[250:251], v[250:251], v[170:171]\n"
"v_pk_mul_f32 v[252:253], v[252:253], v[172:173]\n"
"v_pk_mul_f32 v[254:255], v[254:255], v[174:175]\n"
"s_waitcnt lgkmcnt(6)\n"
"v_mfma_f32_16x16x32_bf16 v[176:179], a[144:147], a[64:67], v[176:179]\n"
"ds_read_b64_tr_b16 a[128:129], v211 offset:28672\n"
"v_exp_f32 v240, v240\n"
"v_mfma_f32_16x16x32_bf16 v[180:183], a[144:147], a[68:71], v[180:183]\n"
"ds_read_b64_tr_b16 a[130:131], v211 offset:31744\n"
"v_exp_f32 v241, v241\n"
"v_mfma_f32_16x16x32_bf16 v[184:187], a[144:147], a[72:75], v[184:187]\n"
"buffer_store_dwordx2 v[152:153], v227, s[64:67], s98 offen offset:2048 sc1\n"
"v_exp_f32 v242, v242\n"
"v_mfma_f32_16x16x32_bf16 v[188:191], a[144:147], a[76:79], v[188:191]\n"
"s_waitcnt lgkmcnt(6)\n"
"v_exp_f32 v243, v243\n"
"v_mfma_f32_16x16x32_bf16 v[176:179], a[148:151], a[80:83], v[176:179]\n"
"ds_read_b64_tr_b16 a[132:133], v211 offset:34816\n"
"v_exp_f32 v244, v244\n"
"v_mfma_f32_16x16x32_bf16 v[180:183], a[148:151], a[84:87], v[180:183]\n"
"ds_read_b64_tr_b16 a[134:135], v211 offset:37888\n"
"v_exp_f32 v245, v245\n"
"v_mfma_f32_16x16x32_bf16 v[184:187], a[148:151], a[88:91], v[184:187]\n"
"buffer_store_dwordx2 v[156:157], v227, s[64:67], s98 offen offset:3072 sc1\n"
"s_add_u32 s98, s98, s97\n"
"v_exp_f32 v246, v246\n"
"v_mfma_f32_16x16x32_bf16 v[188:191], a[148:151], a[92:95], v[188:191]\n"
"s_waitcnt lgkmcnt(6)\n"
"v_exp_f32 v247, v247\n"
"v_mfma_f32_16x16x32_bf16 v[176:179], a[152:155], a[96:99], v[176:179]\n"
"ds_read_b64_tr_b16 a[136:137], v213 offset:41152\n"
"v_exp_f32 v248, v248\n"
"v_mfma_f32_16x16x32_bf16 v[180:183], a[152:155], a[100:103], v[180:183]\n"
"ds_read_b64_tr_b16 a[138:139], v213 offset:45248\n"
"v_exp_f32 v249, v249\n"
"v_mfma_f32_16x16x32_bf16 v[184:187], a[152:155], a[104:107], v[184:187]\n"
"v_exp_f32 v250, v250\n"
"v_mfma_f32_16x16x32_bf16 v[188:191], a[152:155], a[108:111], v[188:191]\n"
"s_waitcnt lgkmcnt(6)\n"
"s_waitcnt vmcnt(4)\n"
"s_cmp_eq_u32 s76, 0\n"
"s_cbranch_scc1 .Lok2_%=\n"
".Lchk2_%=:\n"
"v_cmp_ne_u64 vcc, s[84:85], v[234:235]\n"
"s_nop 1\n"
"s_cmp_eq_u64 vcc, 0\n"
"s_cbranch_scc1 .Lok2_%=\n"
"s_cmp_eq_u32 s94, 0\n"
"s_cbranch_scc1 .Lok2_%=\n"
"s_sub_u32 s94, s94, 1\n"
"s_sleep 1\n"
"buffer_load_dwordx2 v[234:235], v238, s[64:67], s91 offen sc1\n"
"s_waitcnt vmcnt(0)\n"
"s_branch .Lchk2_%=\n"
".Lok2_%=:\n"
"v_exp_f32 v251, v251\n"
"v_mfma_f32_16x16x32_bf16 v[176:179], a[156:159], a[112:115], v[176:179]\n"
"ds_read_b64_tr_b16 a[140:141], v213 offset:49344\n"
"v_exp_f32 v252, v252\n"
"buffer_load_dwordx4 a[160:163], v224, s[64:67], s92 offen sc1\n"
"v_exp_f32 v253, v253\n"
"v_exp_f32 v254, v254\n"
"v_exp_f32 v255, v255\n"
"v_pk_add_f32 v[240:241], v[240:241], 1.0 op_sel_hi:[1,0]\n"
"v_pk_add_f32 v[242:243], v[242:243], 1.0 op_sel_hi:[1,0]\n"
"v_pk_add_f32 v[244:245], v[244:245], 1.0 op_sel_hi:[1,0]\n"
"v_pk_add_f32 v[246:247], v[246:247], 1.0 op_sel_hi:[1,0]\n"
"v_pk_add_f32 v[248:249], v[248:249], 1.0 op_sel_hi:[1,0]\n"
"v_pk_add_f32 v[250:251], v[250:251], 1.0 op_sel_hi:[1,0]\n"
"v_pk_add_f32 v[252:253], v[252:253], 1.0 op_sel_hi:[1,0]\n"
"v_pk_add_f32 v[254:255], v[254:255], 1.0 op_sel_hi:[1,0]\n"
"v_mfma_f32_16x16x32_bf16 v[180:183], a[156:159], a[116:119], v[180:183]\n"
"ds_read_b64_tr_b16 a[142:143], v213 offset:53440\n"
"v_rcp_f32 v240, v240\n"
"v_mfma_f32_16x16x32_bf16 v[184:187], a[156:159], a[120:123], v[184:187]\n"
"buffer_load_dwordx4 a[164:167], v224, s[64:67], s92 offen offset:1024 sc1\n"
"v_rcp_f32 v241, v241\n"
"v_mfma_f32_16x16x32_bf16 v[188:191], a[156:159], a[124:127], v[188:191]\n"
"s_waitcnt lgkmcnt(6)\n"
"v_rcp_f32 v242, v242\n"
"v_mfma_f32_16x16x32_bf16 v[0:3], a[128:131], a[64:67], v[0:3]\n"
"ds_read_b64_tr_b16 a[144:145], v216 offset:12480\n"
"v_rcp_f32 v243, v243\n"
"v_mfma_f32_16x16x32_bf16 v[4:7], a[128:131], a[68:71], v[4:7]\n"
"ds_read_b64_tr_b16 a[146:147], v216 offset:16576\n"
"v_rcp_f32 v244, v244\n"
"v_mfma_f32_16x16x32_bf16 v[8:11], a[128:131], a[72:75], v[8:11]\n"
"buffer_load_dwordx4 a[168:171], v224, s[64:67], s92 offen offset:2048 sc1\n"
"v_rcp_f32 v245, v245\n"
"v_mfma_f32_16x16x32_bf16 v[12:15], a[128:131], a[76:79], v[12:15]\n"
"s_waitcnt lgkmcnt(6)\n"
"v_rcp_f32 v246, v246\n"
"v_mfma_f32_16x16x32_bf16 v[0:3], a[132:135], a[80:83], v[0:3]\n"
"ds_read_b64_tr_b16 a[148:149], v216 offset:20672\n"
"v_rcp_f32 v247, v247\n"
"v_mfma_f32_16x16x32_bf16 v[4:7], a[132:135], a[84:87], v[4:7]\n"
"ds_read_b64_tr_b16 a[150:151], v216 offset:24768\n"
"v_rcp_f32 v248, v248\n"
"v_mfma_f32_16x16x32_bf16 v[8:11], a[132:135], a[88:91], v[8:11]\n"
"buffer_load_dwordx4 a[172:175], v224, s[64:67], s92 offen offset:3072 sc1\n"
"v_rcp_f32 v249, v249\n"
"v_mfma_f32_16x16x32_bf16 v[12:15], a[132:135], a[92:95], v[12:15]\n"
"v_rcp_f32 v250, v250\n"
"v_mfma_f32_16x16x32_bf16 v[28:31], a[232:235], a[44:47], v[28:31]\n"
"v_rcp_f32 v251, v251\n"
"v_mfma_f32_16x16x32_bf16 v[24:27], a[232:235], a[40:43], v[24:27]\n"
"v_rcp_f32 v252, v252\n"
"v_rcp_f32 v253, v253\n"
"buffer_load_dwordx4 a[176:179], v224, s[64:67], s93 offen sc1\n"
"v_rcp_f32 v254, v254\n"
"v_rcp_f32 v255, v255\n"
"v_pk_mul_f32 v[240:241], v[240:241], v[160:161]\n"
"v_pk_mul_f32 v[242:243], v[242:243], v[162:163]\n"
"v_pk_mul_f32 v[244:245], v[244:245], v[164:165]\n"
"v_pk_mul_f32 v[246:247], v[246:247], v[166:167]\n"
"v_pk_mul_f32 v[248:249], v[248:249], v[168:169]\n"
"v_pk_mul_f32 v[250:251], v[250:251], v[170:171]\n"
"v_pk_mul_f32 v[252:253], v[252:253], v[172:173]\n"
"v_pk_mul_f32 v[254:255], v[254:255], v[174:175]\n"
"v_cvt_pk_bf16_f32 v160, v240, v241\n"
"v_cvt_pk_bf16_f32 v161, v242, v243\n"
"buffer_load_dwordx4 a[180:183], v224, s[64:67], s93 offen offset:1024 sc1\n"
"v_cvt_pk_bf16_f32 v164, v244, v245\n"
".Lpad7_%=:\n"
"s_cbranch_execz .Lpad11_%=\n"
"v_cvt_pk_bf16_f32 v165, v246, v247\n"
"v_cvt_pk_bf16_f32 v168, v248, v249\n"
"v_cvt_pk_bf16_f32 v169, v250, v251\n"
"v_cvt_pk_bf16_f32 v172, v252, v253\n"
"v_cvt_pk_bf16_f32 v173, v254, v255\n"
"v_pk_mul_f32 v[240:241], v[176:177], v[176:177]\n"
"v_pk_mul_f32 v[242:243], v[178:179], v[178:179]\n"
"v_pk_mul_f32 v[244:245], v[180:181], v[180:181]\n"
"v_pk_mul_f32 v[246:247], v[182:183], v[182:183]\n"
"v_pk_mul_f32 v[248:249], v[184:185], v[184:185]\n"
"v_pk_mul_f32 v[250:251], v[186:187], v[186:187]\n"
"v_pk_mul_f32 v[252:253], v[188:189], v[188:189]\n"
"v_pk_mul_f32 v[254:255], v[190:191], v[190:191]\n"
"buffer_load_dwordx4 a[184:187], v224, s[64:67], s93 offen offset:2048 sc1\n"
"v_pk_fma_f32 v[240:241], v[240:241], s[88:89], v[232:233] op_sel_hi:[1,0,1]\n"
"v_pk_fma_f32 v[242:243], v[242:243], s[88:89], v[232:233] op_sel_hi:[1,0,1]\n"
"v_pk_fma_f32 v[244:245], v[244:245], s[88:89], v[232:233] op_sel_hi:[1,0,1]\n"
"v_pk_fma_f32 v[246:247], v[246:247], s[88:89], v[232:233] op_sel_hi:[1,0,1]\n"
"v_pk_fma_f32 v[248:249], v[248:249], s[88:89], v[232:233] op_sel_hi:[1,0,1]\n"
"v_pk_fma_f32 v[250:251], v[250:251], s[88:89], v[232:233] op_sel_hi:[1,0,1]\n"
"v_pk_fma_f32 v[252:253], v[252:253], s[88:89], v[232:233] op_sel_hi:[1,0,1]\n"
"v_pk_fma_f32 v[254:255], v[254:255], s[88:89], v[232:233] op_sel_hi:[1,0,1]\n"
"v_pk_mul_f32 v[240:241], v[240:241], v[176:177]\n"
"v_pk_mul_f32 v[242:243], v[242:243], v[178:179]\n"
"v_pk_mul_f32 v[244:245], v[244:245], v[180:181]\n"
"v_pk_mul_f32 v[246:247], v[246:247], v[182:183]\n"
"v_pk_mul_f32 v[248:249], v[248:249], v[184:185]\n"
"v_pk_mul_f32 v[250:251], v[250:251], v[186:187]\n"
"v_pk_mul_f32 v[252:253], v[252:253], v[188:189]\n"
"v_pk_mul_f32 v[254:255], v[254:255], v[190:191]\n"
"s_waitcnt lgkmcnt(6)\n"
"v_mfma_f32_16x16x32_bf16 v[192:195], a[136:139], a[64:67], v[192:195]\n"
"ds_read_b64_tr_b16 a[152:153], v214\n"
"v_exp_f32 v240, v240\n"
"v_mfma_f32_16x16x32_bf16 v[196:199], a[136:139], a[68:71], v[196:199]\n"
"ds_read_b64_tr_b16 a[154:155], v214 offset:3072\n"
"v_exp_f32 v241, v241\n"
"v_mfma_f32_16x16x32_bf16 v[200:203], a[136:139], a[72:75], v[200:203]\n"
"buffer_load_dwordx4 a[188:191], v224, s[64:67], s93 offen offset:3072 sc1\n"
"s_nop 0\n"
"v_exp_f32 v242, v242\n"
"v_mfma_f32_16x16x32_bf16 v[204:207], a[136:139], a[76:79], v[204:207]\n"
"s_waitcnt lgkmcnt(6)\n"
"v_exp_f32 v243, v243\n"
"v_mfma_f32_16x16x32_bf16 v[192:195], a[140:143], a[80:83], v[192:195]\n"
"ds_read_b64_tr_b16 a[156:157], v214 offset:6144\n"
"v_exp_f32 v244, v244\n"
"v_mfma_f32_16x16x32_bf16 v[196:199], a[140:143], a[84:87], v[196:199]\n"
"ds_read_b64_tr_b16 a[158:159], v214 offset:9216\n"
"v_exp_f32 v245, v245\n"
"v_mfma_f32_16x16x32_bf16 v[200:203], a[140:143], a[88:91], v[200:203]\n"
"buffer_store_dwordx2 v[160:161], v227, s[64:67], s98 offen sc1\n"
"v_exp_f32 v246, v246\n"
"v_mfma_f32_16x16x32_bf16 v[204:207], a[140:143], a[92:95], v[204:207]\n"
"s_waitcnt lgkmcnt(6)\n"
"v_exp_f32 v247, v247\n"
"v_mfma_f32_16x16x32_bf16 v[192:195], a[144:147], a[96:99], v[192:195]\n"
"ds_read_b64_tr_b16 a[128:129], v212 offset:28672\n"
"v_exp_f32 v248, v248\n"
"v_mfma_f32_16x16x32_bf16 v[196:199], a[144:147], a[100:103], v[196:199]\n"
"ds_read_b64_tr_b16 a[130:131], v212 offset:31744\n"
"v_exp_f32 v249, v249\n"
"v_mfma_f32_16x16x32_bf16 v[200:203], a[144:147], a[104:107], v[200:203]\n"
"buffer_store_dwordx2 v[164:165], v227, s[64:67], s98 offen offset:1024 sc1\n"
"v_exp_f32 v250, v250\n"
"v_mfma_f32_16x16x32_bf16 v[204:207], a[144:147], a[108:111], v[204:207]\n"
"s_waitcnt lgkmcnt(6)\n"
"v_exp_f32 v251, v251\n"
"v_mfma_f32_16x16x32_bf16 v[192:195], a[148:151], a[112:115], v[192:195]\n"
"ds_read_b64_tr_b16 a[132:133], v212 offset:34816\n"
"v_exp_f32 v252, v252\n"
"v_exp_f32 v253, v253\n"
"v_exp_f32 v254, v254\n"
"buffer_store_dwordx2 v[168:169], v227, s[64:67], s98 offen offset:2048 sc1\n"
"v_exp_f32 v255, v255\n"
"v_pk_add_f32 v[240:241], v[240:241], 1.0 op_sel_hi:[1,0]\n"
"v_pk_add_f32 v[242:243], v[242:243], 1.0 op_sel_hi:[1,0]\n"
"v_pk_add_f32 v[244:245], v[244:245], 1.0 op_sel_hi:[1,0]\n"
"v_pk_add_f32 v[246:247], v[246:247], 1.0 op_sel_hi:[1,0]\n"
"v_pk_add_f32 v[248:249], v[248:249], 1.0 op_sel_hi:[1,0]\n"
"v_pk_add_f32 v[250:251], v[250:251], 1.0 op_sel_hi:[1,0]\n"
"v_pk_add_f32 v[252:253], v[252:253], 1.0 op_sel_hi:[1,0]\n"
"v_pk_add_f32 v[254:255], v[254:255], 1.0 op_sel_hi:[1,0]\n"
"v_mfma_f32_16x16x32_bf16 v[196:199], a[148:151], a[116:119], v[196:199]\n"
"ds_read_b64_tr_b16 a[134:135], v212 offset:37888\n"
"v_rcp_f32 v240, v240\n"
"v_mfma_f32_16x16x32_bf16 v[200:203], a[148:151], a[120:123], v[200:203]\n"
"buffer_store_dwordx2 v[172:173], v227, s[64:67], s98 offen offset:3072 sc1\n"
"s_add_u32 s98, s98, s96\n"
"v_rcp_f32 v241, v241\n"
"v_mfma_f32_16x16x32_bf16 v[204:207], a[148:151], a[124:127], v[204:207]\n"
"s_waitcnt lgkmcnt(6)\n"
"v_rcp_f32 v242, v242\n"
"v_mfma_f32_16x16x32_bf16 v[0:3], a[152:155], a[96:99], v[0:3]\n"
"ds_read_b64_tr_b16 a[136:137], v215\n"
"v_rcp_f32 v243, v243\n"
"v_mfma_f32_16x16x32_bf16 v[4:7], a[152:155], a[100:103], v[4:7]\n"
"ds_read_b64_tr_b16 a[138:139], v215 offset:3072\n"
"v_rcp_f32 v244, v244\n"
"v_mfma_f32_16x16x32_bf16 v[8:11], a[152:155], a[104:107], v[8:11]\n"
"v_rcp_f32 v245, v245\n"
"v_mfma_f32_16x16x32_bf16 v[12:15], a[152:155], a[108:111], v[12:15]\n"
"s_waitcnt lgkmcnt(6)\n"
"v_rcp_f32 v246, v246\n"
"v_mfma_f32_16x16x32_bf16 v[0:3], a[156:159], a[112:115], v[0:3]\n"
"ds_read_b64_tr_b16 a[140:141], v215 offset:6144\n"
"v_rcp_f32 v247, v247\n"
"v_mfma_f32_16x16x32_bf16 v[4:7], a[156:159], a[116:119], v[4:7]\n"
"ds_read_b64_tr_b16 a[142:143], v215 offset:9216\n"
"v_rcp_f32 v248, v248\n"
"v_mfma_f32_16x16x32_bf16 v[8:11], a[156:159], a[120:123], v[8:11]\n"
"v_rcp_f32 v249, v249\n"
"v_mfma_f32_16x16x32_bf16 v[12:15], a[156:159], a[124:127], v[12:15]\n"
"v_rcp_f32 v250, v250\n"
"v_mfma_f32_16x16x32_bf16 v[20:23], a[232:235], a[36:39], v[20:23]\n"
"v_rcp_f32 v251, v251\n"
"v_mfma_f32_16x16x32_bf16 v[16:19], a[232:235], a[32:35], v[16:19]\n"
"v_rcp_f32 v252, v252\n"
"v_rcp_f32 v253, v253\n"
"v_rcp_f32 v254, v254\n"
"v_rcp_f32 v255, v255\n"
"v_pk_mul_f32 v[240:241], v[240:241], v[176:177]\n"
"v_pk_mul_f32 v[242:243], v[242:243], v[178:179]\n"
"v_pk_mul_f32 v[244:245], v[244:245], v[180:181]\n"
"v_pk_mul_f32 v[246:247], v[246:247], v[182:183]\n"
"v_pk_mul_f32 v[248:249], v[248:249], v[184:185]\n"
"v_pk_mul_f32 v[250:251], v[250:251], v[186:187]\n"
"v_pk_mul_f32 v[252:253], v[252:253], v[188:189]\n"
"v_pk_mul_f32 v[254:255], v[254:255], v[190:191]\n"
"v_cvt_pk_bf16_f32 v176, v240, v241\n"
"v_cvt_pk_bf16_f32 v177, v242, v243\n"
"buffer_store_dwordx2 v[176:177], v227, s[64:67], s98 offen sc1\n"
"v_cvt_pk_bf16_f32 v180, v244, v245\n"
"v_cvt_pk_bf16_f32 v181, v246, v247\n"
"v_cvt_pk_bf16_f32 v184, v248, v249\n"
"v_cvt_pk_bf16_f32 v185, v250, v251\n"
"v_cvt_pk_bf16_f32 v188, v252, v253\n"
"v_cvt_pk_bf16_f32 v189, v254, v255\n"
"v_pk_mul_f32 v[240:241], v[192:193], v[192:193]\n"
"s_waitcnt vmcnt(12)\n"
"ds_write_b128 v237, a[160:163]\n"
"v_pk_mul_f32 v[242:243], v[194:195], v[194:195]\n"
"v_pk_mul_f32 v[244:245], v[196:197], v[196:197]\n"
"v_pk_mul_f32 v[246:247], v[198:199], v[198:199]\n"
"v_pk_mul_f32 v[248:249], v[200:201], v[200:201]\n"
"v_pk_mul_f32 v[250:251], v[202:203], v[202:203]\n"
"buffer_store_dwordx2 v[180:181], v227, s[64:67], s98 offen offset:1024 sc1\n"
"v_pk_mul_f32 v[252:253], v[204:205], v[204:205]\n"
"v_pk_mul_f32 v[254:255], v[206:207], v[206:207]\n"
"v_pk_fma_f32 v[240:241], v[240:241], s[88:89], v[232:233] op_sel_hi:[1,0,1]\n"
"v_pk_fma_f32 v[242:243], v[242:243], s[88:89], v[232:233] op_sel_hi:[1,0,1]\n"
"v_pk_fma_f32 v[244:245], v[244:245], s[88:89], v[232:233] op_sel_hi:[1,0,1]\n"
"s_waitcnt vmcnt(12)\n"
"ds_write_b128 v237, a[164:167] offset:1024\n"
"v_pk_fma_f32 v[246:247], v[246:247], s[88:89], v[232:233] op_sel_hi:[1,0,1]\n"
"v_pk_fma_f32 v[248:249], v[248:249], s[88:89], v[232:233] op_sel_hi:[1,0,1]\n"
"v_pk_fma_f32 v[250:251], v[250:251], s[88:89], v[232:233] op_sel_hi:[1,0,1]\n"
"v_pk_fma_f32 v[252:253], v[252:253], s[88:89], v[232:233] op_sel_hi:[1,0,1]\n"
"v_pk_fma_f32 v[254:255], v[254:255], s[88:89], v[232:233] op_sel_hi:[1,0,1]\n"
"v_pk_mul_f32 v[240:241], v[240:241], v[192:193]\n"
"v_pk_mul_f32 v[242:243], v[242:243], v[194:195]\n"
"v_pk_mul_f32 v[244:245], v[244:245], v[196:197]\n"
"v_pk_mul_f32 v[246:247], v[246:247], v[198:199]\n"
"v_pk_mul_f32 v[248:249], v[248:249], v[200:201]\n"
"v_pk_mul_f32 v[250:251], v[250:251], v[202:203]\n"
"v_pk_mul_f32 v[252:253], v[252:253], v[204:205]\n"
"v_pk_mul_f32 v[254:255], v[254:255], v[206:207]\n"
"s_waitcnt lgkmcnt(8)\n"
"v_mfma_f32_16x16x32_bf16 v[16:19], a[128:131], a[64:67], v[16:19]\n"
"buffer_store_dwordx2 v[184:185], v227, s[64:67], s98 offen offset:2048 sc1\n"
"v_exp_f32 v240, v240\n"
"v_mfma_f32_16x16x32_bf16 v[20:23], a[128:131], a[68:71], v[20:23]\n"
"s_waitcnt vmcnt(12)\n"
"ds_write_b128 v237, a[168:171] offset:2048\n"
"v_exp_f32 v241, v241\n"
"v_mfma_f32_16x16x32_bf16 v[24:27], a[128:131], a[72:75], v[24:27]\n"
"v_exp_f32 v242, v242\n"
"v_mfma_f32_16x16x32_bf16 v[28:31], a[128:131], a[76:79], v[28:31]\n"
"s_waitcnt lgkmcnt(7)\n"
"v_exp_f32 v243, v243\n"
"v_exp_f32 v244, v244\n"
"v_mfma_f32_16x16x32_bf16 v[16:19], a[132:135], a[80:83], v[16:19]\n"
"buffer_store_dwordx2 v[188:189], v227, s[64:67], s98 offen offset:3072 sc1\n"
"s_add_u32 s98, s98, s97\n"
"v_exp_f32 v245, v245\n"
"v_mfma_f32_16x16x32_bf16 v[20:23], a[132:135], a[84:87], v[20:23]\n"
"s_waitcnt vmcnt(12)\n"
"ds_write_b128 v237, a[172:175] offset:3072\n"
"v_exp_f32 v246, v246\n"
"v_mfma_f32_16x16x32_bf16 v[24:27], a[132:135], a[88:91], v[24:27]\n"
"v_exp_f32 v247, v247\n"
"v_exp_f32 v248, v248\n"
"v_mfma_f32_16x16x32_bf16 v[28:31], a[132:135], a[92:95], v[28:31]\n"
"s_waitcnt lgkmcnt(6)\n"
"v_exp_f32 v249, v249\n"
"v_mfma_f32_16x16x32_bf16 v[16:19], a[136:139], a[96:99], v[16:19]\n"
"s_waitcnt vmcnt(11)\n"
"ds_write_b128 v237, a[176:179] offset:32768\n"
"v_exp_f32 v250, v250\n"
"v_mfma_f32_16x16x32_bf16 v[20:23], a[136:139], a[100:103], v[20:23]\n"
"v_exp_f32 v251, v251\n"
"v_exp_f32 v252, v252\n"
"v_exp_f32 v253, v253\n"
"v_exp_f32 v254, v254\n"
"v_exp_f32 v255, v255\n"
"v_pk_add_f32 v[240:241], v[240:241], 1.0 op_sel_hi:[1,0]\n"
"v_pk_add_f32 v[242:243], v[242:243], 1.0 op_sel_hi:[1,0]\n"
"v_pk_add_f32 v[244:245], v[244:245], 1.0 op_sel_hi:[1,0]\n"
"v_pk_add_f32 v[246:247], v[246:247], 1.0 op_sel_hi:[1,0]\n"
"v_pk_add_f32 v[248:249], v[248:249], 1.0 op_sel_hi:[1,0]\n"
"v_pk_add_f32 v[250:251], v[250:251], 1.0 op_sel_hi:[1,0]\n"
"v_pk_add_f32 v[252:253], v[252:253], 1.0 op_sel_hi:[1,0]\n"
"v_pk_add_f32 v[254:255], v[254:255], 1.0 op_sel_hi:[1,0]\n"
"v_mfma_f32_16x16x32_bf16 v[24:27], a[136:139], a[104:107], v[24:27]\n"
"s_waitcnt vmcnt(10)\n"
"ds_write_b128 v237, a[180:183] offset:33792\n"
"v_rcp_f32 v240, v240\n"
"v_mfma_f32_16x16x32_bf16 v[28:31], a[136:139], a[108:111], v[28:31]\n"
"s_waitcnt lgkmcnt(6)\n"
"v_rcp_f32 v241, v241\n"
"v_mfma_f32_16x16x32_bf16 v[16:19], a[140:143], a[112:115], v[16:19]\n"
"v_rcp_f32 v242, v242\n"
"v_mfma_f32_16x16x32_bf16 v[20:23], a[140:143], a[116:119], v[20:23]\n"
"v_rcp_f32 v243, v243\n"
"v_rcp_f32 v244, v244\n"
"v_mfma_f32_16x16x32_bf16 v[24:27], a[140:143], a[120:123], v[24:27]\n"
"s_waitcnt vmcnt(9)\n"
"ds_write_b128 v237, a[184:187] offset:34816\n"
"v_rcp_f32 v245, v245\n"
"v_mfma_f32_16x16x32_bf16 v[28:31], a[140:143], a[124:127], v[28:31]\n"
"v_rcp_f32 v246, v246\n"
"v_mfma_f32_16x16x32_bf16 v[28:31], a[236:239], a[60:63], v[28:31]\n"
"v_rcp_f32 v247, v247\n"
"v_rcp_f32 v248, v248\n"
"v_mfma_f32_16x16x32_bf16 v[24:27], a[236:239], a[56:59], v[24:27]\n"
"s_waitcnt vmcnt(8)\n"
"ds_write_b128 v237, a[188:191] offset:35840\n"
"v_rcp_f32 v249, v249\n"
"v_mfma_f32_16x16x32_bf16 v[20:23], a[236:239], a[52:55], v[20:23]\n"
"v_rcp_f32 v250, v250\n"
"v_mfma_f32_16x16x32_bf16 v[16:19], a[236:239], a[48:51], v[16:19]\n"
"v_rcp_f32 v251, v251\n"
"v_rcp_f32 v252, v252\n"
"v_rcp_f32 v253, v253\n"
"v_rcp_f32 v254, v254\n"
"v_rcp_f32 v255, v255\n"
"v_pk_mul_f32 v[240:241], v[240:241], v[192:193]\n"
"v_pk_mul_f32 v[242:243], v[242:243], v[194:195]\n"
"v_pk_mul_f32 v[244:245], v[244:245], v[196:197]\n"
"v_pk_mul_f32 v[246:247], v[246:247], v[198:199]\n"
"v_pk_mul_f32 v[248:249], v[248:249], v[200:201]\n"
"v_pk_mul_f32 v[250:251], v[250:251], v[202:203]\n"
"v_pk_mul_f32 v[252:253], v[252:253], v[204:205]\n"
"v_pk_mul_f32 v[254:255], v[254:255], v[206:207]\n"
"v_cvt_pk_bf16_f32 v48, v240, v241\n"
"v_cvt_pk_bf16_f32 v49, v242, v243\n"
"buffer_store_dwordx2 v[48:49], v227, s[64:67], s98 offen sc1\n"
"v_cvt_pk_bf16_f32 v52, v244, v245\n"
"v_cvt_pk_bf16_f32 v53, v246, v247\n"
"v_cvt_pk_bf16_f32 v56, v248, v249\n"
"v_cvt_pk_bf16_f32 v57, v250, v251\n"
"v_cvt_pk_bf16_f32 v60, v252, v253\n"
"v_cvt_pk_bf16_f32 v61, v254, v255\n"
"s_sub_u32 s90, 4, s78\n"
"s_cmp_eq_u32 s76, 0\n"
"s_cselect_b32 s90, s90, 0\n"
"s_cmp_gt_u32 s90, 0\n"
"s_cselect_b32 s92, 0xf149f2ca, 0\n"
"v_mov_b32 v176, s92\n"
"v_mov_b32 v177, s92\n"
"v_mov_b32 v178, s92\n"
"buffer_store_dwordx2 v[52:53], v227, s[64:67], s98 offen offset:1024 sc1\n"
"v_mov_b32 v179, s92\n"
"s_cmp_gt_u32 s90, 1\n"
"s_cselect_b32 s92, 0xf149f2ca, 0\n"
"v_mov_b32 v180, s92\n"
"v_mov_b32 v181, s92\n"
"v_mov_b32 v182, s92\n"
"v_mov_b32 v183, s92\n"
"s_cmp_gt_u32 s90, 2\n"
"s_cselect_b32 s92, 0xf149f2ca, 0\n"
"v_mov_b32 v184, s92\n"
"v_mov_b32 v185, s92\n"
"v_mov_b32 v186, s92\n"
"v_mov_b32 v187, s92\n"
"s_cmp_gt_u32 s90, 3\n"
"s_cselect_b32 s92, 0xf149f2ca, 0\n"
"v_mov_b32 v188, s92\n"
"buffer_store_dwordx2 v[56:57], v227, s[64:67], s98 offen offset:2048 sc1\n"
"v_mov_b32 v189, s92\n"
"v_mov_b32 v190, s92\n"
"v_mov_b32 v191, s92\n"
"v_lshrrev_b32 v200, 2, v236\n"
"v_and_b32 v200, 12, v200\n"
"v_and_b32 v201, 15, v236\n"
"v_sub_u32 v200, v200, v201\n"
"v_mov_b32 v201, 0xf149f2ca\n"
"v_mov_b32 v202, 0\n"
"v_cmp_lt_i32 vcc, 0, v200\n"
"v_mov_b32 v240, 0x3f803f80\n"
"s_nop 0\n"
"v_cndmask_b32 v192, v201, v176, vcc\n"
"v_cndmask_b32 v196, v202, v201, vcc\n"
"buffer_store_dwordx2 v[60:61], v227, s[64:67], s98 offen offset:3072 sc1\n"
"v_cmp_lt_i32 vcc, -1, v200\n"
"v_mov_b32 v241, 0x3f803f80\n"
"s_nop 0\n"
"v_cndmask_b32 v193, v201, v176, vcc\n"
"v_cndmask_b32 v197, v202, v201, vcc\n"
"v_cmp_lt_i32 vcc, -2, v200\n"
"v_mov_b32 v242, 0x3f803f80\n"
"s_nop 0\n"
"v_cndmask_b32 v194, v201, v176, vcc\n"
"v_cndmask_b32 v198, v202, v201, vcc\n"
"v_cmp_lt_i32 vcc, -3, v200\n"
"v_mov_b32 v243, 0x3f803f80\n"
"s_nop 0\n"
"v_cndmask_b32 v195, v201, v176, vcc\n"
"v_cndmask_b32 v199, v202, v201, vcc\n"
"s_mov_b32 s88, 0x3e8293ee\n"
"v_cvt_pk_bf16_f32 v0, v0, v1\n"
"v_cvt_pk_bf16_f32 v1, v2, v3\n"
"v_cvt_pk_bf16_f32 v2, v16, v17\n"
"v_cvt_pk_bf16_f32 v3, v18, v19\n"
"v_cvt_pk_bf16_f32 v4, v4, v5\n"
"v_cvt_pk_bf16_f32 v5, v6, v7\n"
"v_cvt_pk_bf16_f32 v6, v20, v21\n"
"v_cvt_pk_bf16_f32 v7, v22, v23\n"
"v_cvt_pk_bf16_f32 v8, v8, v9\n"
"v_cvt_pk_bf16_f32 v9, v10, v11\n"
"v_cvt_pk_bf16_f32 v10, v24, v25\n"
"v_cvt_pk_bf16_f32 v11, v26, v27\n"
"v_cvt_pk_bf16_f32 v12, v12, v13\n"
"v_cvt_pk_bf16_f32 v13, v14, v15\n"
"v_cvt_pk_bf16_f32 v14, v28, v29\n"
"v_cvt_pk_bf16_f32 v15, v30, v31\n"
"s_waitcnt lgkmcnt(0)\n"
"s_barrier\n"
"ds_read_b128 a[96:99], v237\n"
"ds_read_b128 a[100:103], v237 offset:1024\n"
"ds_read_b128 a[104:107], v237 offset:2048\n"
"ds_read_b128 a[108:111], v237 offset:3072\n"
"ds_read_b128 a[112:115], v237 offset:4096\n"
"ds_read_b128 a[116:119], v237 offset:5120\n"
"ds_read_b128 a[120:123], v237 offset:6144\n"
"ds_read_b128 a[124:127], v237 offset:7168\n"
"s_waitcnt lgkmcnt(7)\n"
"v_mfma_f32_16x16x32_bf16 v[16:19], a[96:99], v[0:3], v[192:195]\n"
"ds_read_b128 a[128:131], v237 offset:8192\n"
"ds_read_b128 a[132:135], v237 offset:9216\n"
"s_waitcnt lgkmcnt(8)\n"
"v_mfma_f32_16x16x32_bf16 v[20:23], a[100:103], v[0:3], v[176:179]\n"
"ds_read_b128 a[136:139], v237 offset:10240\n"
"s_waitcnt lgkmcnt(8)\n"
"v_mfma_f32_16x16x32_bf16 v[24:27], a[104:107], v[0:3], v[176:179]\n"
"ds_read_b128 a[140:143], v237 offset:11264\n"
"s_waitcnt lgkmcnt(8)\n"
"v_mfma_f32_16x16x32_bf16 v[28:31], a[108:111], v[0:3], v[176:179]\n"
"ds_read_b128 a[144:147], v237 offset:12288\n"
"s_waitcnt lgkmcnt(8)\n"
"v_mfma_f32_16x16x32_bf16 v[32:35], a[112:115], v[0:3], v[180:183]\n"
"ds_read_b128 a[148:151], v237 offset:13312\n"
"s_waitcnt lgkmcnt(8)\n"
"v_mfma_f32_16x16x32_bf16 v[36:39], a[116:119], v[0:3], v[180:183]\n"
"ds_read_b128 a[152:155], v237 offset:14336\n"
"s_waitcnt lgkmcnt(8)\n"
"v_mfma_f32_16x16x32_bf16 v[40:43], a[120:123], v[0:3], v[180:183]\n"
"ds_read_b128 a[156:159], v237 offset:15360\n"
"s_waitcnt lgkmcnt(8)\n"
"v_mfma_f32_16x16x32_bf16 v[44:47], a[124:127], v[0:3], v[180:183]\n"
"ds_read_b128 a[160:163], v237 offset:16384\n"
"s_waitcnt lgkmcnt(8)\n"
"v_mfma_f32_16x16x32_bf16 v[48:51], a[128:131], v[0:3], v[184:187]\n"
"ds_read_b128 a[164:167], v237 offset:17408\n"
"s_waitcnt lgkmcnt(8)\n"
"v_mfma_f32_16x16x32_bf16 v[52:55], a[132:135], v[0:3], v[184:187]\n"
"ds_read_b128 a[168:171], v237 offset:18432\n"
"s_waitcnt lgkmcnt(8)\n"
"v_mfma_f32_16x16x32_bf16 v[56:59], a[136:139], v[0:3], v[184:187]\n"
"ds_read_b128 a[172:175], v237 offset:19456\n"
"s_waitcnt lgkmcnt(8)\n"
"v_mfma_f32_16x16x32_bf16 v[60:63], a[140:143], v[0:3], v[184:187]\n"
"s_waitcnt lgkmcnt(7)\n"
"v_mfma_f32_16x16x32_bf16 v[64:67], a[144:147], v[0:3], v[188:191]\n"
"s_waitcnt lgkmcnt(6)\n"
"v_mfma_f32_16x16x32_bf16 v[68:71], a[148:151], v[0:3], v[188:191]\n"
"ds_read_b128 a[176:179], v237 offset:32768\n"
"s_waitcnt lgkmcnt(6)\n"
"v_mfma_f32_16x16x32_bf16 v[72:75], a[152:155], v[0:3], v[188:191]\n"
"s_waitcnt lgkmcnt(5)\n"
"v_mfma_f32_16x16x32_bf16 v[76:79], a[156:159], v[0:3], v[188:191]\n"
"ds_read_b128 a[180:183], v237 offset:33792\n"
"s_waitcnt lgkmcnt(5)\n"
"v_mfma_f32_16x16x32_bf16 v[80:83], a[160:163], v[0:3], v[196:199]\n"
"v_max3_f32 v204, v16, v17, v18\n"
"v_max3_f32 v206, v19, v20, v21\n"
"v_mfma_f32_16x16x32_bf16 v[84:87], a[100:103], v[4:7], v[192:195]\n"
"ds_read_b128 a[184:187], v237 offset:34816\n"
"v_max3_f32 v204, v204, v22, v23\n"
"v_max3_f32 v206, v206, v24, v25\n"
"v_mfma_f32_16x16x32_bf16 v[88:91], a[104:107], v[4:7], v[176:179]\n"
"ds_read_b128 a[188:191], v237 offset:35840\n"
"v_max3_f32 v204, v204, v26, v27\n"
"v_max3_f32 v206, v206, v28, v29\n"
"v_mfma_f32_16x16x32_bf16 v[92:95], a[108:111], v[4:7], v[176:179]\n"
"ds_read_b128 a[192:195], v237 offset:36864\n"
"v_max3_f32 v204, v204, v30, v31\n"
"v_max3_f32 v206, v206, v32, v33\n"
"v_mfma_f32_16x16x32_bf16 v[96:99], a[112:115], v[4:7], v[180:183]\n"
"ds_read_b128 a[196:199], v237 offset:37888\n"
"v_max3_f32 v204, v204, v34, v35\n"
"v_max3_f32 v206, v206, v36, v37\n"
"v_mfma_f32_16x16x32_bf16 v[100:103], a[116:119], v[4:7], v[180:183]\n"
"ds_read_b128 a[200:203], v237 offset:38912\n"
"v_max3_f32 v204, v204, v38, v39\n"
"v_max3_f32 v206, v206, v40, v41\n"
"v_mfma_f32_16x16x32_bf16 v[104:107], a[120:123], v[4:7], v[180:183]\n"
"ds_read_b128 a[204:207], v237 offset:39936\n"
"v_max3_f32 v204, v204, v42, v43\n"
"v_max3_f32 v206, v206, v44, v45\n"
"v_mfma_f32_16x16x32_bf16 v[108:111], a[124:127], v[4:7], v[180:183]\n"
"ds_read_b128 a[208:211], v237 offset:40960\n"
"v_max3_f32 v204, v204, v46, v47\n"
"v_max3_f32 v206, v206, v48, v49\n"
"v_mfma_f32_16x16x32_bf16 v[112:115], a[128:131], v[4:7], v[184:187]\n"
"ds_read_b128 a[212:215], v237 offset:41984\n"
"v_max3_f32 v204, v204, v50, v51\n"
"v_max3_f32 v206, v206, v52, v53\n"
"v_mfma_f32_16x16x32_bf16 v[116:119], a[132:135], v[4:7], v[184:187]\n"
"ds_read_b128 a[216:219], v237 offset:43008\n"
"s_add_u32 m0, s82, 0x10000\n"
"v_max3_f32 v204, v204, v54, v55\n"
"v_max3_f32 v206, v206, v56, v57\n"
"v_mfma_f32_16x16x32_bf16 v[120:123], a[136:139], v[4:7], v[184:187]\n"
"ds_read_b128 a[220:223], v237 offset:44032\n"
"v_max3_f32 v204, v204, v58, v59\n"
"v_max3_f32 v206, v206, v60, v61\n"
"v_mfma_f32_16x16x32_bf16 v[124:127], a[140:143], v[4:7], v[184:187]\n"
"s_waitcnt lgkmcnt(9)\n"
"ds_read_b128 a[224:227], v237 offset:45056\n"
"v_max3_f32 v204, v204, v62, v63\n"
"v_max3_f32 v206, v206, v64, v65\n"
"v_mfma_f32_16x16x32_bf16 v[128:131], a[144:147], v[4:7], v[188:191]\n"
"ds_read_b128 a[228:231], v237 offset:46080\n"
"v_max3_f32 v204, v204, v66, v67\n"
"v_max3_f32 v206, v206, v68, v69\n"
"v_mfma_f32_16x16x32_bf16 v[132:135], a[148:151], v[4:7], v[188:191]\n"
"ds_read_b128 a[232:235], v237 offset:47104\n"
"v_max3_f32 v204, v204, v70, v71\n"
"v_max3_f32 v206, v206, v72, v73\n"
"v_mfma_f32_16x16x32_bf16 v[136:139], a[152:155], v[4:7], v[188:191]\n"
"ds_read_b128 a[236:239], v237 offset:48128\n"
"v_max3_f32 v204, v204, v74, v75\n"
"v_max3_f32 v206, v206, v76, v77\n"
"v_mfma_f32_16x16x32_bf16 v[140:143], a[156:159], v[4:7], v[188:191]\n"
"buffer_load_dwordx4 v217, s[60:63], s80 offen lds\n"
"ds_read_b128 a[240:243], v237 offset:49152\n"
"v_max3_f32 v204, v204, v78, v79\n"
"v_max3_f32 v206, v206, v80, v81\n"
"v_mfma_f32_16x16x32_bf16 v[144:147], a[160:163], v[4:7], 0\n"
"ds_read_b128 a[244:247], v237 offset:50176\n"
"v_max3_f32 v204, v204, v82, v83\n"
"v_max_f32 v204, v204, v206\n"
"v_mfma_f32_16x16x32_bf16 v[148:151], a[164:167], v[4:7], v[196:199]\n"
"s_waitcnt lgkmcnt(9)\n"
"ds_read_b128 a[248:251], v237 offset:51200\n"
"v_mov_b32 v205, v204\n"
"s_nop 1\n"
"v_permlane16_swap_b32 v205, v204\n"
"v_max_f32 v204, v204, v205\n"
"v_mov_b32 v205, v204\n"
"ds_read_b128 a[252:255], v237 offset:52224\n"
"s_nop 0\n"
"v_permlane32_swap_b32 v205, v204\n"
"buffer_load_dwordx4 v218, s[60:63], s80 offen offset:1024 lds\n"
"v_max_f32 v204, v204, v205\n"
"v_mul_f32 v202, 0xbe8293ee, v204\n"
"v_pk_fma_f32 v[16:17], v[16:17], s[88:89], v[202:203] op_sel_hi:[1,0,0]\n"
"v_pk_fma_f32 v[18:19], v[18:19], s[88:89], v[202:203] op_sel_hi:[1,0,0]\n"
"v_pk_fma_f32 v[20:21], v[20:21], s[88:89], v[202:203] op_sel_hi:[1,0,0]\n"
"v_pk_fma_f32 v[22:23], v[22:23], s[88:89], v[202:203] op_sel_hi:[1,0,0]\n"
"v_pk_fma_f32 v[24:25], v[24:25], s[88:89], v[202:203] op_sel_hi:[1,0,0]\n"
"v_pk_fma_f32 v[26:27], v[26:27], s[88:89], v[202:203] op_sel_hi:[1,0,0]\n"
"v_pk_fma_f32 v[28:29], v[28:29], s[88:89], v[202:203] op_sel_hi:[1,0,0]\n"
"v_pk_fma_f32 v[30:31], v[30:31], s[88:89], v[202:203] op_sel_hi:[1,0,0]\n"
"v_pk_fma_f32 v[32:33], v[32:33], s[88:89], v[202:203] op_sel_hi:[1,0,0]\n"
"v_pk_fma_f32 v[34:35], v[34:35], s[88:89], v[202:203] op_sel_hi:[1,0,0]\n"
"v_pk_fma_f32 v[36:37], v[36:37], s[88:89], v[202:203] op_sel_hi:[1,0,0]\n"
"v_pk_fma_f32 v[38:39], v[38:39], s[88:89], v[202:203] op_sel_hi:[1,0,0]\n"
"buffer_load_dwordx4 v219, s[60:63], s80 offen offset:2048 lds\n"
"v_pk_fma_f32 v[40:41], v[40:41], s[88:89], v[202:203] op_sel_hi:[1,0,0]\n"
"v_pk_fma_f32 v[42:43], v[42:43], s[88:89], v[202:203] op_sel_hi:[1,0,0]\n"
"v_pk_fma_f32 v[44:45], v[44:45], s[88:89], v[202:203] op_sel_hi:[1,0,0]\n"
"v_pk_fma_f32 v[46:47], v[46:47], s[88:89], v[202:203] op_sel_hi:[1,0,0]\n"
"v_pk_fma_f32 v[48:49], v[48:49], s[88:89], v[202:203] op_sel_hi:[1,0,0]\n"
"v_pk_fma_f32 v[50:51], v[50:51], s[88:89], v[202:203] op_sel_hi:[1,0,0]\n"
"v_pk_fma_f32 v[52:53], v[52:53], s[88:89], v[202:203] op_sel_hi:[1,0,0]\n"
"v_pk_fma_f32 v[54:55], v[54:55], s[88:89], v[202:203] op_sel_hi:[1,0,0]\n"
"v_pk_fma_f32 v[56:57], v[56:57], s[88:89], v[202:203] op_sel_hi:[1,0,0]\n"
"v_pk_fma_f32 v[58:59], v[58:59], s[88:89], v[202:203] op_sel_hi:[1,0,0]\n"
"v_pk_fma_f32 v[60:61], v[60:61], s[88:89], v[202:203] op_sel_hi:[1,0,0]\n"
"v_pk_fma_f32 v[62:63], v[62:63], s[88:89], v[202:203] op_sel_hi:[1,0,0]\n"
"v_pk_fma_f32 v[64:65], v[64:65], s[88:89], v[202:203] op_sel_hi:[1,0,0]\n"
"v_pk_fma_f32 v[66:67], v[66:67], s[88:89], v[202:203] op_sel_hi:[1,0,0]\n"
"buffer_load_dwordx4 v220, s[60:63], s80 offen offset:3072 lds\n"
"v_pk_fma_f32 v[68:69], v[68:69], s[88:89], v[202:203] op_sel_hi:[1,0,0]\n"
"s_add_u32 m0, s82, 0x14000\n"
"v_pk_fma_f32 v[70:71], v[70:71], s[88:89], v[202:203] op_sel_hi:[1,0,0]\n"
"s_add_u32 s80, s80, 0x40000\n"
"v_pk_fma_f32 v[72:73], v[72:73], s[88:89], v[202:203] op_sel_hi:[1,0,0]\n"
"v_pk_fma_f32 v[74:75], v[74:75], s[88:89], v[202:203] op_sel_hi:[1,0,0]\n"
"v_pk_fma_f32 v[76:77], v[76:77], s[88:89], v[202:203] op_sel_hi:[1,0,0]\n"
"v_pk_fma_f32 v[78:79], v[78:79], s[88:89], v[202:203] op_sel_hi:[1,0,0]\n"
"v_pk_fma_f32 v[80:81], v[80:81], s[88:89], v[202:203] op_sel_hi:[1,0,0]\n"
"v_pk_fma_f32 v[82:83], v[82:83], s[88:89], v[202:203] op_sel_hi:[1,0,0]\n"
"v_exp_f32 v16, v16\n"
"v_exp_f32 v17, v17\n"
"v_exp_f32 v18, v18\n"
"buffer_load_dwordx4 v217, s[60:63], s36 offen lds\n"
"v_exp_f32 v19, v19\n"
"v_exp_f32 v20, v20\n"
"v_exp_f32 v21, v21\n"
"v_exp_f32 v22, v22\n"
"v_exp_f32 v23, v23\n"
"v_cvt_pk_bf16_f32 v16, v16, v17\n"
"v_cvt_pk_bf16_f32 v17, v18, v19\n"
"v_exp_f32 v24, v24\n"
"v_exp_f32 v25, v25\n"
"v_exp_f32 v26, v26\n"
"v_exp_f32 v27, v27\n"
"v_cvt_pk_bf16_f32 v18, v20, v21\n"
"v_cvt_pk_bf16_f32 v19, v22, v23\n"
"v_exp_f32 v28, v28\n"
"v_exp_f32 v29, v29\n"
"v_mfma_f32_16x16x32_bf16 v[152:155], a[176:179], v[16:19], 0\n"
"buffer_load_dwordx4 v218, s[60:63], s36 offen offset:1024 lds\n"
"v_exp_f32 v30, v30\n"
"v_mfma_f32_16x16x32_bf16 v[156:159], a[180:183], v[16:19], 0\n"
"v_exp_f32 v31, v31\n"
"v_mfma_f32_16x16x32_bf16 v[160:163], v[240:243], v[16:19], 0\n"
"v_cvt_pk_bf16_f32 v20, v24, v25\n"
"v_cvt_pk_bf16_f32 v21, v26, v27\n"
"v_exp_f32 v32, v32\n"
"v_exp_f32 v33, v33\n"
"v_exp_f32 v34, v34\n"
"buffer_load_dwordx4 v219, s[60:63], s36 offen offset:2048 lds\n"
"v_exp_f32 v35, v35\n"
"v_cvt_pk_bf16_f32 v22, v28, v29\n"
"v_cvt_pk_bf16_f32 v23, v30, v31\n"
"v_exp_f32 v36, v36\n"
"v_exp_f32 v37, v37\n"
"v_mfma_f32_16x16x32_bf16 v[152:155], a[184:187], v[20:23], v[152:155]\n"
"v_exp_f32 v38, v38\n"
"v_mfma_f32_16x16x32_bf16 v[156:159], a[188:191], v[20:23], v[156:159]\n"
"v_exp_f32 v39, v39\n"
"v_mfma_f32_16x16x32_bf16 v[160:163], v[240:243], v[20:23], v[160:163]\n"
"buffer_load_dwordx4 v220, s[60:63], s36 offen offset:3072 lds\n"
"s_add_u32 m0, s82, 0x18000\n"
"v_cvt_pk_bf16_f32 v24, v32, v33\n"
"s_add_u32 s36, s36, 0x40000\n"
"v_cvt_pk_bf16_f32 v25, v34, v35\n"
"v_exp_f32 v40, v40\n"
"v_exp_f32 v41, v41\n"
"v_exp_f32 v42, v42\n"
"v_exp_f32 v43, v43\n"
"v_cvt_pk_bf16_f32 v26, v36, v37\n"
"v_cvt_pk_bf16_f32 v27, v38, v39\n"
"v_exp_f32 v44, v44\n"
"v_exp_f32 v45, v45\n"
"v_mfma_f32_16x16x32_bf16 v[152:155], a[192:195], v[24:27], v[152:155]\n"
"v_exp_f32 v46, v46\n"
"v_mfma_f32_16x16x32_bf16 v[156:159], a[196:199], v[24:27], v[156:159]\n"
"buffer_load_dwordx4 v217, s[60:63], s80 offen lds\n"
"v_exp_f32 v47, v47\n"
"v_mfma_f32_16x16x32_bf16 v[160:163], v[240:243], v[24:27], v[160:163]\n"
"v_cvt_pk_bf16_f32 v28, v40, v41\n"
"v_cvt_pk_bf16_f32 v29, v42, v43\n"
"v_exp_f32 v48, v48\n"
"v_exp_f32 v49, v49\n"
"v_exp_f32 v50, v50\n"
"v_exp_f32 v51, v51\n"
"v_cvt_pk_bf16_f32 v30, v44, v45\n"
"v_cvt_pk_bf16_f32 v31, v46, v47\n"
"v_exp_f32 v52, v52\n"
"v_exp_f32 v53, v53\n"
"v_mfma_f32_16x16x32_bf16 v[152:155], a[200:203], v[28:31], v[152:155]\n"
"buffer_load_dwordx4 v218, s[60:63], s80 offen offset:1024 lds\n"
"v_exp_f32 v54, v54\n"
"v_mfma_f32_16x16x32_bf16 v[156:159], a[204:207], v[28:31], v[156:159]\n"
"v_exp_f32 v55, v55\n"
"v_mfma_f32_16x16x32_bf16 v[160:163], v[240:243], v[28:31], v[160:163]\n"
"v_cvt_pk_bf16_f32 v32, v48, v49\n"
"v_cvt_pk_bf16_f32 v33, v50, v51\n"
"v_exp_f32 v56, v56\n"
"v_exp_f32 v57, v57\n"
"v_exp_f32 v58, v58\n"
"v_exp_f32 v59, v59\n"
"buffer_load_dwordx4 v219, s[60:63], s80 offen offset:2048 lds\n"
"v_cvt_pk_bf16_f32 v34, v52, v53\n"
"v_cvt_pk_bf16_f32 v35, v54, v55\n"
"v_mov_b32 v50, 0\n"
"v_mov_b32 v51, 0\n"
"v_exp_f32 v60, v60\n"
"v_exp_f32 v61, v61\n"
"v_mfma_f32_16x16x32_bf16 v[152:155], a[208:211], v[32:35], v[152:155]\n"
"v_exp_f32 v62, v62\n"
"s_waitcnt lgkmcnt(10)\n"
"v_mfma_f32_16x16x32_bf16 v[156:159], a[212:215], v[32:35], v[156:159]\n"
"s_waitcnt vmcnt(11)\n"
"buffer_store_dwordx2 v[228:229], v225, s[64:67], s42 offen sc1\n"
"v_exp_f32 v63, v63\n"
"v_mfma_f32_16x16x32_bf16 v[160:163], v[240:243], v[32:35], v[160:163]\n"
"v_cvt_pk_bf16_f32 v36, v56, v57\n"
"v_cvt_pk_bf16_f32 v37, v58, v59\n"
"v_exp_f32 v64, v64\n"
"v_exp_f32 v65, v65\n"
"v_exp_f32 v66, v66\n"
"v_exp_f32 v67, v67\n"
"v_cvt_pk_bf16_f32 v38, v60, v61\n"
"v_cvt_pk_bf16_f32 v39, v62, v63\n"
"v_exp_f32 v68, v68\n"
"v_exp_f32 v69, v69\n"
"s_waitcnt lgkmcnt(9)\n"
"v_mfma_f32_16x16x32_bf16 v[152:155], a[216:219], v[36:39], v[152:155]\n"
".Lpad8_%=:\n"
"s_cbranch_execz .Lpad12_%=\n"
"buffer_load_dwordx4 v220, s[60:63], s80 offen offset:3072 lds\n"
"s_add_u32 m0, s82, 0x1c000\n"
"v_exp_f32 v70, v70\n"
"s_waitcnt lgkmcnt(8)\n"
"v_mfma_f32_16x16x32_bf16 v[156:159], a[220:223], v[36:39], v[156:159]\n"
"s_add_u32 s80, s80, 0x40000\n"
"v_exp_f32 v71, v71\n"
"v_mfma_f32_16x16x32_bf16 v[160:163], v[240:243], v[36:39], v[160:163]\n"
"v_cvt_pk_bf16_f32 v40, v64, v65\n"
"v_cvt_pk_bf16_f32 v41, v66, v67\n"
"v_exp_f32 v72, v72\n"
"v_exp_f32 v73, v73\n"
"buffer_load_dwordx4 v217, s[60:63], s36 offen lds\n"
"v_exp_f32 v74, v74\n"
"s_waitcnt lgkmcnt(0)\n"
"s_barrier\n"
"v_exp_f32 v75, v75\n"
"v_cvt_pk_bf16_f32 v42, v68, v69\n"
"v_cvt_pk_bf16_f32 v43, v70, v71\n"
"v_exp_f32 v76, v76\n"
"v_exp_f32 v77, v77\n"
"v_mfma_f32_16x16x32_bf16 v[152:155], a[224:227], v[40:43], v[152:155]\n"
"buffer_load_dwordx4 v218, s[60:63], s36 offen offset:1024 lds\n"
"v_exp_f32 v78, v78\n"
"v_mfma_f32_16x16x32_bf16 v[156:159], a[228:231], v[40:43], v[156:159]\n"
"v_exp_f32 v79, v79\n"
"v_mfma_f32_16x16x32_bf16 v[160:163], v[240:243], v[40:43], v[160:163]\n"
"v_cvt_pk_bf16_f32 v44, v72, v73\n"
"v_cvt_pk_bf16_f32 v45, v74, v75\n"
"v_exp_f32 v80, v80\n"
"v_exp_f32 v81, v81\n"
"v_exp_f32 v82, v82\n"
"v_exp_f32 v83, v83\n"
"v_cvt_pk_bf16_f32 v46, v76, v77\n"
"v_cvt_pk_bf16_f32 v47, v78, v79\n"
"v_cvt_pk_bf16_f32 v48, v80, v81\n"
"v_cvt_pk_bf16_f32 v49, v82, v83\n"
"v_max3_f32 v204, v84, v85, v86\n"
"v_max3_f32 v206, v87, v88, v89\n"
"v_mfma_f32_16x16x32_bf16 v[152:155], a[232:235], v[44:47], v[152:155]\n"
"buffer_load_dwordx4 v219, s[60:63], s36 offen offset:2048 lds\n"
"v_max3_f32 v204, v204, v90, v91\n"
"v_max3_f32 v206, v206, v92, v93\n"
"v_mfma_f32_16x16x32_bf16 v[156:159], a[236:239], v[44:47], v[156:159]\n"
"v_max3_f32 v204, v204, v94, v95\n"
"v_max3_f32 v206, v206, v96, v97\n"
"v_mfma_f32_16x16x32_bf16 v[160:163], v[240:243], v[44:47], v[160:163]\n"
"v_max3_f32 v204, v204, v98, v99\n"
"v_max3_f32 v206, v206, v100, v101\n"
"v_mfma_f32_16x16x32_bf16 v[152:155], a[240:243], v[48:51], v[152:155]\n"
"v_max3_f32 v204, v204, v102, v103\n"
"v_max3_f32 v206, v206, v104, v105\n"
"v_mfma_f32_16x16x32_bf16 v[156:159], a[244:247], v[48:51], v[156:159]\n"
"buffer_load_dwordx4 v220, s[60:63], s36 offen offset:3072 lds\n"
"s_add_u32 m0, s82, 0x20000\n"
"v_max3_f32 v204, v204, v106, v107\n"
"v_max3_f32 v206, v206, v108, v109\n"
"v_mfma_f32_16x16x32_bf16 v[160:163], v[240:243], v[48:51], v[160:163]\n"
"s_add_u32 s36, s36, 0x40000\n"
"v_max3_f32 v204, v204, v110, v111\n"
"v_max3_f32 v206, v206, v112, v113\n"
"v_mfma_f32_16x16x32_bf16 v[16:19], a[104:107], v[8:11], v[192:195]\n"
"v_max3_f32 v204, v204, v114, v115\n"
"v_max3_f32 v206, v206, v116, v117\n"
"v_mfma_f32_16x16x32_bf16 v[20:23], a[108:111], v[8:11], v[176:179]\n"
"v_max3_f32 v204, v204, v118, v119\n"
"v_max3_f32 v206, v206, v120, v121\n"
"v_mfma_f32_16x16x32_bf16 v[24:27], a[112:115], v[8:11], v[180:183]\n"
"v_max3_f32 v204, v204, v122, v123\n"
"v_max3_f32 v206, v206, v124, v125\n"
"v_mfma_f32_16x16x32_bf16 v[28:31], a[116:119], v[8:11], v[180:183]\n"
"buffer_load_dwordx4 v217, s[60:63], s80 offen lds\n"
"v_rcp_f32 v160, v160\n"
"v_mfma_f32_16x16x32_bf16 v[32:35], a[120:123], v[8:11], v[180:183]\n"
"v_mul_f32 v152, v152, v160\n"
"v_mul_f32 v153, v153, v160\n"
"v_mfma_f32_16x16x32_bf16 v[36:39], a[124:127], v[8:11], v[180:183]\n"
"v_mul_f32 v154, v154, v160\n"
"v_mul_f32 v155, v155, v160\n"
"v_mfma_f32_16x16x32_bf16 v[40:43], a[128:131], v[8:11], v[184:187]\n"
"v_mul_f32 v156, v156, v160\n"
"v_mul_f32 v157, v157, v160\n"
"v_mfma_f32_16x16x32_bf16 v[44:47], a[132:135], v[8:11], v[184:187]\n"
"v_mul_f32 v158, v158, v160\n"
"v_mul_f32 v159, v159, v160\n"
"v_mfma_f32_16x16x32_bf16 v[48:51], a[136:139], v[8:11], v[184:187]\n"
"buffer_load_dwordx4 v218, s[60:63], s80 offen offset:1024 lds\n"
"v_cvt_pk_bf16_f32 v152, v152, v153\n"
"v_cvt_pk_bf16_f32 v153, v154, v155\n"
"v_mfma_f32_16x16x32_bf16 v[52:55], a[140:143], v[8:11], v[184:187]\n"
"v_cvt_pk_bf16_f32 v154, v156, v157\n"
"v_cvt_pk_bf16_f32 v155, v158, v159\n"
"v_mfma_f32_16x16x32_bf16 v[56:59], a[144:147], v[8:11], v[188:191]\n"
"v_max3_f32 v204, v204, v126, v127\n"
"v_max3_f32 v206, v206, v128, v129\n"
"v_mfma_f32_16x16x32_bf16 v[60:63], a[148:151], v[8:11], v[188:191]\n"
"v_max3_f32 v204, v204, v130, v131\n"
"v_max3_f32 v206, v206, v132, v133\n"
"v_mfma_f32_16x16x32_bf16 v[64:67], a[152:155], v[8:11], v[188:191]\n"
"buffer_store_dwordx2 v[152:153], v227, s[64:67], s38 offen sc1\n"
"v_max3_f32 v204, v204, v134, v135\n"
"v_max3_f32 v206, v206, v136, v137\n"
"v_mfma_f32_16x16x32_bf16 v[68:71], a[156:159], v[8:11], v[188:191]\n"
"v_max3_f32 v204, v204, v138, v139\n"
"v_max3_f32 v206, v206, v140, v141\n"
"v_mfma_f32_16x16x32_bf16 v[72:75], a[160:163], v[8:11], 0\n"
"v_max3_f32 v204, v204, v142, v143\n"
"v_max3_f32 v206, v206, v144, v145\n"
"v_mfma_f32_16x16x32_bf16 v[76:79], a[164:167], v[8:11], 0\n"
"v_max3_f32 v204, v204, v146, v147\n"
"v_max3_f32 v206, v206, v148, v149\n"
"v_mfma_f32_16x16x32_bf16 v[80:83], a[168:171], v[8:11], v[196:199]\n"
"buffer_store_dwordx2 v[154:155], v227, s[64:67], s38 offen offset:512 sc1\n"
"v_max3_f32 v204, v204, v150, v151\n"
"v_max_f32 v204, v204, v206\n"
"v_mov_b32 v205, v204\n"
"s_nop 1\n"
"v_permlane16_swap_b32 v205, v204\n"
"v_max_f32 v204, v204, v205\n"
"v_mov_b32 v205, v204\n"
"s_nop 1\n"
"v_permlane32_swap_b32 v205, v204\n"
"v_max_f32 v204, v204, v205\n"
"v_mul_f32 v202, 0xbe8293ee, v204\n"
"v_pk_fma_f32 v[84:85], v[84:85], s[88:89], v[202:203] op_sel_hi:[1,0,0]\n"
"v_pk_fma_f32 v[86:87], v[86:87], s[88:89], v[202:203] op_sel_hi:[1,0,0]\n"
"v_pk_fma_f32 v[88:89], v[88:89], s[88:89], v[202:203] op_sel_hi:[1,0,0]\n"
"v_pk_fma_f32 v[90:91], v[90:91], s[88:89], v[202:203] op_sel_hi:[1,0,0]\n"
"v_pk_fma_f32 v[92:93], v[92:93], s[88:89], v[202:203] op_sel_hi:[1,0,0]\n"
"v_pk_fma_f32 v[94:95], v[94:95], s[88:89], v[202:203] op_sel_hi:[1,0,0]\n"
"v_pk_fma_f32 v[96:97], v[96:97], s[88:89], v[202:203] op_sel_hi:[1,0,0]\n"
"buffer_load_dwordx4 v219, s[60:63], s80 offen offset:2048 lds\n"
"v_pk_fma_f32 v[98:99], v[98:99], s[88:89], v[202:203] op_sel_hi:[1,0,0]\n"
"v_pk_fma_f32 v[100:101], v[100:101], s[88:89], v[202:203] op_sel_hi:[1,0,0]\n"
"v_pk_fma_f32 v[102:103], v[102:103], s[88:89], v[202:203] op_sel_hi:[1,0,0]\n"
"v_pk_fma_f32 v[104:105], v[104:105], s[88:89], v[202:203] op_sel_hi:[1,0,0]\n"
"v_pk_fma_f32 v[106:107], v[106:107], s[88:89], v[202:203] op_sel_hi:[1,0,0]\n"
"v_pk_fma_f32 v[108:109], v[108:109], s[88:89], v[202:203] op_sel_hi:[1,0,0]\n"
"v_pk_fma_f32 v[110:111], v[110:111], s[88:89], v[202:203] op_sel_hi:[1,0,0]\n"
"v_pk_fma_f32 v[112:113], v[112:113], s[88:89], v[202:203] op_sel_hi:[1,0,0]\n"
"v_pk_fma_f32 v[114:115], v[114:115], s[88:89], v[202:203] op_sel_hi:[1,0,0]\n"
"v_pk_fma_f32 v[116:117], v[116:117], s[88:89], v[202:203] op_sel_hi:[1,0,0]\n"
"v_pk_fma_f32 v[118:119], v[118:119], s[88:89], v[202:203] op_sel_hi:[1,0,0]\n"
"v_pk_fma_f32 v[120:121], v[120:121], s[88:89], v[202:203] op_sel_hi:[1,0,0]\n"
"v_pk_fma_f32 v[122:123], v[122:123], s[88:89], v[202:203] op_sel_hi:[1,0,0]\n"
"v_pk_fma_f32 v[124:125], v[124:125], s[88:89], v[202:203] op_sel_hi:[1,0,0]\n"
"buffer_load_dwordx4 v220, s[60:63], s80 offen offset:3072 lds\n"
"v_pk_fma_f32 v[126:127], v[126:127], s[88:89], v[202:203] op_sel_hi:[1,0,0]\n"
"s_add_u32 m0, s82, 0x24000\n"
"v_pk_fma_f32 v[128:129], v[128:129], s[88:89], v[202:203] op_sel_hi:[1,0,0]\n"
"s_add_u32 s80, s80, 0x40000\n"
"v_pk_fma_f32 v[130:131], v[130:131], s[88:89], v[202:203] op_sel_hi:[1,0,0]\n"
"v_pk_fma_f32 v[132:133], v[132:133], s[88:89], v[202:203] op_sel_hi:[1,0,0]\n"
"v_pk_fma_f32 v[134:135], v[134:135], s[88:89], v[202:203] op_sel_hi:[1,0,0]\n"
"v_pk_fma_f32 v[136:137], v[136:137], s[88:89], v[202:203] op_sel_hi:[1,0,0]\n"
"v_pk_fma_f32 v[138:139], v[138:139], s[88:89], v[202:203] op_sel_hi:[1,0,0]\n"
"v_pk_fma_f32 v[140:141], v[140:141], s[88:89], v[202:203] op_sel_hi:[1,0,0]\n"
"v_pk_fma_f32 v[142:143], v[142:143], s[88:89], v[202:203] op_sel_hi:[1,0,0]\n"
"v_pk_fma_f32 v[144:145], v[144:145], s[88:89], v[202:203] op_sel_hi:[1,0,0]\n"
"v_pk_fma_f32 v[146:147], v[146:147], s[88:89], v[202:203] op_sel_hi:[1,0,0]\n"
"v_pk_fma_f32 v[148:149], v[148:149], s[88:89], v[202:203] op_sel_hi:[1,0,0]\n"
"buffer_load_dwordx4 v217, s[60:63], s36 offen lds\n"
"v_pk_fma_f32 v[150:151], v[150:151], s[88:89], v[202:203] op_sel_hi:[1,0,0]\n"
"v_exp_f32 v84, v84\n"
"v_exp_f32 v85, v85\n"
"v_exp_f32 v86, v86\n"
"v_exp_f32 v87, v87\n"
"v_exp_f32 v88, v88\n"
"v_exp_f32 v89, v89\n"
"v_exp_f32 v90, v90\n"
"buffer_load_dwordx4 v218, s[60:63], s36 offen offset:1024 lds\n"
"v_exp_f32 v91, v91\n"
"v_cvt_pk_bf16_f32 v87, v86, v87\n"
"v_cvt_pk_bf16_f32 v86, v84, v85\n"
"v_mov_b32 v84, 0\n"
"v_mov_b32 v85, 0\n"
"v_exp_f32 v92, v92\n"
"v_exp_f32 v93, v93\n"
"v_mfma_f32_16x16x32_bf16 v[164:167], a[176:179], v[84:87], 0\n"
"v_exp_f32 v94, v94\n"
"v_mfma_f32_16x16x32_bf16 v[168:171], a[180:183], v[84:87], 0\n"
"buffer_load_dwordx4 v219, s[60:63], s36 offen offset:2048 lds\n"
"v_exp_f32 v95, v95\n"
"v_mfma_f32_16x16x32_bf16 v[172:175], v[240:243], v[84:87], 0\n"
"v_cvt_pk_bf16_f32 v88, v88, v89\n"
"v_cvt_pk_bf16_f32 v89, v90, v91\n"
"v_exp_f32 v96, v96\n"
"v_exp_f32 v97, v97\n"
"v_exp_f32 v98, v98\n"
"v_exp_f32 v99, v99\n"
"v_cvt_pk_bf16_f32 v90, v92, v93\n"
"v_cvt_pk_bf16_f32 v91, v94, v95\n"
"v_exp_f32 v100, v100\n"
"v_exp_f32 v101, v101\n"
"v_mfma_f32_16x16x32_bf16 v[164:167], a[184:187], v[88:91], v[164:167]\n"
"buffer_load_dwordx2 v[234:235], v226, s[64:67], s43 offen sc1\n"
"v_exp_f32 v102, v102\n"
"v_mfma_f32_16x16x32_bf16 v[168:171], a[188:191], v[88:91], v[168:171]\n"
"v_exp_f32 v103, v103\n"
"v_mfma_f32_16x16x32_bf16 v[172:175], v[240:243], v[88:91], v[172:175]\n"
"v_cvt_pk_bf16_f32 v92, v96, v97\n"
"v_cvt_pk_bf16_f32 v93, v98, v99\n"
"v_exp_f32 v104, v104\n"
"v_exp_f32 v105, v105\n"
"v_exp_f32 v106, v106\n"
"buffer_load_dwordx4 v220, s[60:63], s36 offen offset:3072 lds\n"
"v_exp_f32 v107, v107\n"
"v_cvt_pk_bf16_f32 v94, v100, v101\n"
"v_cvt_pk_bf16_f32 v95, v102, v103\n"
"v_exp_f32 v108, v108\n"
"v_exp_f32 v109, v109\n"
"v_mfma_f32_16x16x32_bf16 v[164:167], a[192:195], v[92:95], v[164:167]\n"
"s_add_u32 m0, s82, 0\n"
"s_add_u32 s36, s36, 0x40000\n"
"v_exp_f32 v110, v110\n"
"v_mfma_f32_16x16x32_bf16 v[168:171], a[196:199], v[92:95], v[168:171]\n"
"v_exp_f32 v111, v111\n"
"v_mfma_f32_16x16x32_bf16 v[172:175], v[240:243], v[92:95], v[172:175]\n"
"v_cvt_pk_bf16_f32 v96, v104, v105\n"
"v_cvt_pk_bf16_f32 v97, v106, v107\n"
"v_exp_f32 v112, v112\n"
"v_exp_f32 v113, v113\n"
"v_exp_f32 v114, v114\n"
"v_exp_f32 v115, v115\n"
"v_cvt_pk_bf16_f32 v98, v108, v109\n"
"v_cvt_pk_bf16_f32 v99, v110, v111\n"
"v_exp_f32 v116, v116\n"
"v_exp_f32 v117, v117\n"
"v_mfma_f32_16x16x32_bf16 v[164:167], a[200:203], v[96:99], v[164:167]\n"
"v_exp_f32 v118, v118\n"
"v_mfma_f32_16x16x32_bf16 v[168:171], a[204:207], v[96:99], v[168:171]\n"
"v_exp_f32 v119, v119\n"
"v_mfma_f32_16x16x32_bf16 v[172:175], v[240:243], v[96:99], v[172:175]\n"
"v_cvt_pk_bf16_f32 v100, v112, v113\n"
"v_cvt_pk_bf16_f32 v101, v114, v115\n"
"v_exp_f32 v120, v120\n"
"v_exp_f32 v121, v121\n"
"v_exp_f32 v122, v122\n"
"v_exp_f32 v123, v123\n"
"v_cvt_pk_bf16_f32 v102, v116, v117\n"
"v_cvt_pk_bf16_f32 v103, v118, v119\n"
"v_exp_f32 v124, v124\n"
"v_exp_f32 v125, v125\n"
"v_mfma_f32_16x16x32_bf16 v[164:167], a[208:211], v[100:103], v[164:167]\n"
"v_exp_f32 v126, v126\n"
"v_mfma_f32_16x16x32_bf16 v[168:171], a[212:215], v[100:103], v[168:171]\n"
"v_exp_f32 v127, v127\n"
"v_mfma_f32_16x16x32_bf16 v[172:175], v[240:243], v[100:103], v[172:175]\n"
"v_cvt_pk_bf16_f32 v104, v120, v121\n"
"v_cvt_pk_bf16_f32 v105, v122, v123\n"
"v_exp_f32 v128, v128\n"
"v_exp_f32 v129, v129\n"
"v_exp_f32 v130, v130\n"
"v_exp_f32 v131, v131\n"
"v_cvt_pk_bf16_f32 v106, v124, v125\n"
"v_cvt_pk_bf16_f32 v107, v126, v127\n"
"v_exp_f32 v132, v132\n"
"v_exp_f32 v133, v133\n"
"v_mfma_f32_16x16x32_bf16 v[164:167], a[216:219], v[104:107], v[164:167]\n"
"v_exp_f32 v134, v134\n"
"v_mfma_f32_16x16x32_bf16 v[168:171], a[220:223], v[104:107], v[168:171]\n"
"v_exp_f32 v135, v135\n"
"v_mfma_f32_16x16x32_bf16 v[172:175], v[240:243], v[104:107], v[172:175]\n"
"v_cvt_pk_bf16_f32 v108, v128, v129\n"
"v_cvt_pk_bf16_f32 v109, v130, v131\n"
"v_exp_f32 v136, v136\n"
"v_exp_f32 v137, v137\n"
"v_exp_f32 v138, v138\n"
"v_exp_f32 v139, v139\n"
"v_cvt_pk_bf16_f32 v110, v132, v133\n"
"v_cvt_pk_bf16_f32 v111, v134, v135\n"
"v_exp_f32 v140, v140\n"
"v_exp_f32 v141, v141\n"
"v_mfma_f32_16x16x32_bf16 v[164:167], a[224:227], v[108:111], v[164:167]\n"
"v_exp_f32 v142, v142\n"
"v_mfma_f32_16x16x32_bf16 v[168:171], a[228:231], v[108:111], v[168:171]\n"
"v_exp_f32 v143, v143\n"
"v_mfma_f32_16x16x32_bf16 v[172:175], v[240:243], v[108:111], v[172:175]\n"
"v_cvt_pk_bf16_f32 v112, v136, v137\n"
"v_cvt_pk_bf16_f32 v113, v138, v139\n"
"v_exp_f32 v144, v144\n"
"v_exp_f32 v145, v145\n"
"v_exp_f32 v146, v146\n"
"s_waitcnt vmcnt(1)\n"
".Lchk3_%=:\n"
"v_cmp_ne_u64 vcc, s[84:85], v[234:235]\n"
"s_nop 1\n"
"s_cmp_eq_u64 vcc, 0\n"
"s_cbranch_scc1 .Lok3_%=\n"
"s_cmp_eq_u32 s94, 0\n"
"s_cbranch_scc1 .Lok3_%=\n"
"s_sub_u32 s94, s94, 1\n"
"s_sleep 1\n"
"buffer_load_dwordx2 v[234:235], v226, s[64:67], s43 offen sc1\n"
"s_waitcnt vmcnt(0)\n"
"s_branch .Lchk3_%=\n"
".Lok3_%=:\n"
"v_exp_f32 v147, v147\n"
"v_cvt_pk_bf16_f32 v114, v140, v141\n"
"v_cvt_pk_bf16_f32 v115, v142, v143\n"
"v_exp_f32 v148, v148\n"
"v_exp_f32 v149, v149\n"
"v_mfma_f32_16x16x32_bf16 v[164:167], a[232:235], v[112:115], v[164:167]\n"
"buffer_load_dwordx4 a[0:3], v208, s[64:67], s81 offen sc1\n"
"v_exp_f32 v150, v150\n"
"v_mfma_f32_16x16x32_bf16 v[168:171], a[236:239], v[112:115], v[168:171]\n"
"v_exp_f32 v151, v151\n"
"v_mfma_f32_16x16x32_bf16 v[172:175], v[240:243], v[112:115], v[172:175]\n"
"v_cvt_pk_bf16_f32 v116, v144, v145\n"
"v_cvt_pk_bf16_f32 v117, v146, v147\n"
"v_cvt_pk_bf16_f32 v118, v148, v149\n"
"v_cvt_pk_bf16_f32 v119, v150, v151\n"
"v_max3_f32 v204, v16, v17, v18\n"
"v_max3_f32 v206, v19, v20, v21\n"
"v_mfma_f32_16x16x32_bf16 v[164:167], a[240:243], v[116:119], v[164:167]\n"
"buffer_load_dwordx4 a[4:7], v208, s[64:67], s81 offen offset:1024 sc1\n"
"v_max3_f32 v204, v204, v22, v23\n"
"v_max3_f32 v206, v206, v24, v25\n"
"v_mfma_f32_16x16x32_bf16 v[168:171], a[244:247], v[116:119], v[168:171]\n"
"v_max3_f32 v204, v204, v26, v27\n"
"v_max3_f32 v206, v206, v28, v29\n"
"v_mfma_f32_16x16x32_bf16 v[172:175], v[240:243], v[116:119], v[172:175]\n"
"v_max3_f32 v204, v204, v30, v31\n"
"v_max3_f32 v206, v206, v32, v33\n"
"v_mfma_f32_16x16x32_bf16 v[84:87], a[108:111], v[12:15], v[192:195]\n"
"v_max3_f32 v204, v204, v34, v35\n"
"v_max3_f32 v206, v206, v36, v37\n"
"v_mfma_f32_16x16x32_bf16 v[88:91], a[112:115], v[12:15], v[180:183]\n"
"buffer_load_dwordx4 v217, s[60:63], s80 offen lds\n"
"v_max3_f32 v204, v204, v38, v39\n"
"v_max3_f32 v206, v206, v40, v41\n"
"v_mfma_f32_16x16x32_bf16 v[92:95], a[116:119], v[12:15], v[180:183]\n"
"v_max3_f32 v204, v204, v42, v43\n"
"v_max3_f32 v206, v206, v44, v45\n"
"v_mfma_f32_16x16x32_bf16 v[96:99], a[120:123], v[12:15], v[180:183]\n"
"v_max3_f32 v204, v204, v46, v47\n"
"v_max3_f32 v206, v206, v48, v49\n"
"v_mfma_f32_16x16x32_bf16 v[100:103], a[124:127], v[12:15], v[180:183]\n"
"v_max3_f32 v204, v204, v50, v51\n"
"v_max3_f32 v206, v206, v52, v53\n"
"v_mfma_f32_16x16x32_bf16 v[104:107], a[128:131], v[12:15], v[184:187]\n"
"buffer_load_dwordx4 a[8:11], v209, s[64:67], s81 offen sc1\n"
"v_max3_f32 v204, v204, v54, v55\n"
"v_max3_f32 v206, v206, v56, v57\n"
"v_mfma_f32_16x16x32_bf16 v[108:111], a[132:135], v[12:15], v[184:187]\n"
"v_rcp_f32 v172, v172\n"
"v_mfma_f32_16x16x32_bf16 v[112:115], a[136:139], v[12:15], v[184:187]\n"
"v_mul_f32 v164, v164, v172\n"
"v_mul_f32 v165, v165, v172\n"
"v_mfma_f32_16x16x32_bf16 v[116:119], a[140:143], v[12:15], v[184:187]\n"
"v_mul_f32 v166, v166, v172\n"
"v_mul_f32 v167, v167, v172\n"
"v_mfma_f32_16x16x32_bf16 v[120:123], a[144:147], v[12:15], v[188:191]\n"
"buffer_load_dwordx4 a[12:15], v209, s[64:67], s81 offen offset:1024 sc1\n"
"s_add_u32 s81, s81, 0x1000\n"
"v_mul_f32 v168, v168, v172\n"
"v_mul_f32 v169, v169, v172\n"
"v_mfma_f32_16x16x32_bf16 v[124:127], a[148:151], v[12:15], v[188:191]\n"
"v_mul_f32 v170, v170, v172\n"
"v_mul_f32 v171, v171, v172\n"
"v_mfma_f32_16x16x32_bf16 v[128:131], a[152:155], v[12:15], v[188:191]\n"
"v_cvt_pk_bf16_f32 v164, v164, v165\n"
"v_cvt_pk_bf16_f32 v165, v166, v167\n"
"v_mfma_f32_16x16x32_bf16 v[132:135], a[156:159], v[12:15], v[188:191]\n"
"v_cvt_pk_bf16_f32 v166, v168, v169\n"
"v_cvt_pk_bf16_f32 v167, v170, v171\n"
"v_mfma_f32_16x16x32_bf16 v[136:139], a[160:163], v[12:15], 0\n"
"buffer_store_dwordx2 v[164:165], v227, s[64:67], s38 offen offset:1024 sc1\n"
"v_max3_f32 v204, v204, v58, v59\n"
"v_max3_f32 v206, v206, v60, v61\n"
"v_mfma_f32_16x16x32_bf16 v[140:143], a[164:167], v[12:15], 0\n"
"v_max3_f32 v204, v204, v62, v63\n"
"v_max3_f32 v206, v206, v64, v65\n"
"v_mfma_f32_16x16x32_bf16 v[144:147], a[168:171], v[12:15], 0\n"
"v_max3_f32 v204, v204, v66, v67\n"
"v_max3_f32 v206, v206, v68, v69\n"
"v_mfma_f32_16x16x32_bf16 v[148:151], a[172:175], v[12:15], v[196:199]\n"
"v_max3_f32 v204, v204, v70, v71\n"
"v_max3_f32 v206, v206, v72, v73\n"
"v_max3_f32 v204, v204, v74, v75\n"
"v_max3_f32 v206, v206, v76, v77\n"
"buffer_store_dwordx2 v[166:167], v227, s[64:67], s38 offen offset:1536 sc1\n"
"v_max3_f32 v204, v204, v78, v79\n"
"v_max3_f32 v206, v206, v80, v81\n"
"v_max3_f32 v204, v204, v82, v83\n"
"v_max_f32 v204, v204, v206\n"
"v_mov_b32 v205, v204\n"
"s_nop 1\n"
"v_permlane16_swap_b32 v205, v204\n"
"v_max_f32 v204, v204, v205\n"
"v_mov_b32 v205, v204\n"
"s_nop 1\n"
"v_permlane32_swap_b32 v205, v204\n"
"v_max_f32 v204, v204, v205\n"
"v_mul_f32 v202, 0xbe8293ee, v204\n"
"v_pk_fma_f32 v[16:17], v[16:17], s[88:89], v[202:203] op_sel_hi:[1,0,0]\n"
"v_pk_fma_f32 v[18:19], v[18:19], s[88:89], v[202:203] op_sel_hi:[1,0,0]\n"
"v_pk_fma_f32 v[20:21], v[20:21], s[88:89], v[202:203] op_sel_hi:[1,0,0]\n"
"buffer_load_dwordx4 v218, s[60:63], s80 offen offset:1024 lds\n"
"v_pk_fma_f32 v[22:23], v[22:23], s[88:89], v[202:203] op_sel_hi:[1,0,0]\n"
"v_pk_fma_f32 v[24:25], v[24:25], s[88:89], v[202:203] op_sel_hi:[1,0,0]\n"
"v_pk_fma_f32 v[26:27], v[26:27], s[88:89], v[202:203] op_sel_hi:[1,0,0]\n"
"v_pk_fma_f32 v[28:29], v[28:29], s[88:89], v[202:203] op_sel_hi:[1,0,0]\n"
"v_pk_fma_f32 v[30:31], v[30:31], s[88:89], v[202:203] op_sel_hi:[1,0,0]\n"
"v_pk_fma_f32 v[32:33], v[32:33], s[88:89], v[202:203] op_sel_hi:[1,0,0]\n"
"v_pk_fma_f32 v[34:35], v[34:35], s[88:89], v[202:203] op_sel_hi:[1,0,0]\n"
"v_pk_fma_f32 v[36:37], v[36:37], s[88:89], v[202:203] op_sel_hi:[1,0,0]\n"
"v_pk_fma_f32 v[38:39], v[38:39], s[88:89], v[202:203] op_sel_hi:[1,0,0]\n"
"v_pk_fma_f32 v[40:41], v[40:41], s[88:89], v[202:203] op_sel_hi:[1,0,0]\n"
"v_pk_fma_f32 v[42:43], v[42:43], s[88:89], v[202:203] op_sel_hi:[1,0,0]\n"
"v_pk_fma_f32 v[44:45], v[44:45], s[88:89], v[202:203] op_sel_hi:[1,0,0]\n"
"v_pk_fma_f32 v[46:47], v[46:47], s[88:89], v[202:203] op_sel_hi:[1,0,0]\n"
"v_pk_fma_f32 v[48:49], v[48:49], s[88:89], v[202:203] op_sel_hi:[1,0,0]\n"
"buffer_load_dwordx4 a[16:19], v208, s[64:67], s81 offen sc1\n"
"v_pk_fma_f32 v[50:51], v[50:51], s[88:89], v[202:203] op_sel_hi:[1,0,0]\n"
"v_pk_fma_f32 v[52:53], v[52:53], s[88:89], v[202:203] op_sel_hi:[1,0,0]\n"
"v_pk_fma_f32 v[54:55], v[54:55], s[88:89], v[202:203] op_sel_hi:[1,0,0]\n"
"v_pk_fma_f32 v[56:57], v[56:57], s[88:89], v[202:203] op_sel_hi:[1,0,0]\n"
"v_pk_fma_f32 v[58:59], v[58:59], s[88:89], v[202:203] op_sel_hi:[1,0,0]\n"
"v_pk_fma_f32 v[60:61], v[60:61], s[88:89], v[202:203] op_sel_hi:[1,0,0]\n"
"v_pk_fma_f32 v[62:63], v[62:63], s[88:89], v[202:203] op_sel_hi:[1,0,0]\n"
"v_pk_fma_f32 v[64:65], v[64:65], s[88:89], v[202:203] op_sel_hi:[1,0,0]\n"
"v_pk_fma_f32 v[66:67], v[66:67], s[88:89], v[202:203] op_sel_hi:[1,0,0]\n"
"v_pk_fma_f32 v[68:69], v[68:69], s[88:89], v[202:203] op_sel_hi:[1,0,0]\n"
"v_pk_fma_f32 v[70:71], v[70:71], s[88:89], v[202:203] op_sel_hi:[1,0,0]\n"
"v_pk_fma_f32 v[72:73], v[72:73], s[88:89], v[202:203] op_sel_hi:[1,0,0]\n"
"v_pk_fma_f32 v[74:75], v[74:75], s[88:89], v[202:203] op_sel_hi:[1,0,0]\n"
"v_pk_fma_f32 v[76:77], v[76:77], s[88:89], v[202:203] op_sel_hi:[1,0,0]\n"
"buffer_load_dwordx4 a[20:23], v208, s[64:67], s81 offen offset:1024 sc1\n"
"v_pk_fma_f32 v[78:79], v[78:79], s[88:89], v[202:203] op_sel_hi:[1,0,0]\n"
"v_pk_fma_f32 v[80:81], v[80:81], s[88:89], v[202:203] op_sel_hi:[1,0,0]\n"
"v_pk_fma_f32 v[82:83], v[82:83], s[88:89], v[202:203] op_sel_hi:[1,0,0]\n"
"v_exp_f32 v16, v16\n"
"v_exp_f32 v17, v17\n"
"v_exp_f32 v18, v18\n"
"v_exp_f32 v19, v19\n"
"v_exp_f32 v20, v20\n"
"v_exp_f32 v21, v21\n"
"buffer_load_dwordx4 v219, s[60:63], s80 offen offset:2048 lds\n"
"v_exp_f32 v22, v22\n"
"v_exp_f32 v23, v23\n"
"v_cvt_pk_bf16_f32 v16, v16, v17\n"
"v_cvt_pk_bf16_f32 v17, v18, v19\n"
"v_exp_f32 v24, v24\n"
"v_exp_f32 v25, v25\n"
"v_exp_f32 v26, v26\n"
"v_exp_f32 v27, v27\n"
"v_cvt_pk_bf16_f32 v18, v20, v21\n"
"v_cvt_pk_bf16_f32 v19, v22, v23\n"
"v_exp_f32 v28, v28\n"
"v_exp_f32 v29, v29\n"
"v_mfma_f32_16x16x32_bf16 v[152:155], a[184:187], v[16:19], 0\n"
"buffer_load_dwordx4 a[24:27], v209, s[64:67], s81 offen sc1\n"
"v_exp_f32 v30, v30\n"
"v_mfma_f32_16x16x32_bf16 v[156:159], a[188:191], v[16:19], 0\n"
"v_exp_f32 v31, v31\n"
"v_mfma_f32_16x16x32_bf16 v[160:163], v[240:243], v[16:19], 0\n"
"v_cvt_pk_bf16_f32 v20, v24, v25\n"
"v_cvt_pk_bf16_f32 v21, v26, v27\n"
"v_exp_f32 v32, v32\n"
"v_exp_f32 v33, v33\n"
"v_exp_f32 v34, v34\n"
"buffer_load_dwordx4 a[28:31], v209, s[64:67], s81 offen offset:1024 sc1\n"
"v_exp_f32 v35, v35\n"
"v_cvt_pk_bf16_f32 v22, v28, v29\n"
"v_cvt_pk_bf16_f32 v23, v30, v31\n"
"v_exp_f32 v36, v36\n"
"v_exp_f32 v37, v37\n"
"v_mfma_f32_16x16x32_bf16 v[152:155], a[192:195], v[20:23], v[152:155]\n"
"s_add_u32 s81, s81, 0x1000\n"
"v_exp_f32 v38, v38\n"
"v_mfma_f32_16x16x32_bf16 v[156:159], a[196:199], v[20:23], v[156:159]\n"
"buffer_load_dwordx4 v220, s[60:63], s80 offen offset:3072 lds\n"
"s_add_u32 m0, s82, 0x4000\n"
"v_exp_f32 v39, v39\n"
"v_mfma_f32_16x16x32_bf16 v[160:163], v[240:243], v[20:23], v[160:163]\n"
"s_add_u32 s80, s80, 0x40000\n"
"v_cvt_pk_bf16_f32 v24, v32, v33\n"
"v_cvt_pk_bf16_f32 v25, v34, v35\n"
"v_exp_f32 v40, v40\n"
"v_exp_f32 v41, v41\n"
"v_exp_f32 v42, v42\n"
"v_exp_f32 v43, v43\n"
"v_cvt_pk_bf16_f32 v26, v36, v37\n"
"v_cvt_pk_bf16_f32 v27, v38, v39\n"
"v_exp_f32 v44, v44\n"
"v_exp_f32 v45, v45\n"
"v_mfma_f32_16x16x32_bf16 v[152:155], a[200:203], v[24:27], v[152:155]\n"
"buffer_load_dwordx4 a[32:35], v208, s[64:67], s81 offen sc1\n"
"v_exp_f32 v46, v46\n"
"v_mfma_f32_16x16x32_bf16 v[156:159], a[204:207], v[24:27], v[156:159]\n"
"v_exp_f32 v47, v47\n"
"v_mfma_f32_16x16x32_bf16 v[160:163], v[240:243], v[24:27], v[160:163]\n"
"v_cvt_pk_bf16_f32 v28, v40, v41\n"
"v_cvt_pk_bf16_f32 v29, v42, v43\n"
"v_exp_f32 v48, v48\n"
"v_exp_f32 v49, v49\n"
"v_exp_f32 v50, v50\n"
"buffer_load_dwordx4 a[36:39], v208, s[64:67], s81 offen offset:1024 sc1\n"
"v_exp_f32 v51, v51\n"
"v_cvt_pk_bf16_f32 v30, v44, v45\n"
"v_cvt_pk_bf16_f32 v31, v46, v47\n"
"v_exp_f32 v52, v52\n"
"v_exp_f32 v53, v53\n"
"v_mfma_f32_16x16x32_bf16 v[152:155], a[208:211], v[28:31], v[152:155]\n"
"v_exp_f32 v54, v54\n"
"v_mfma_f32_16x16x32_bf16 v[156:159], a[212:215], v[28:31], v[156:159]\n"
"buffer_load_dwordx4 v217, s[60:63], s36 offen lds\n"
"v_exp_f32 v55, v55\n"
"v_mfma_f32_16x16x32_bf16 v[160:163], v[240:243], v[28:31], v[160:163]\n"
"v_cvt_pk_bf16_f32 v32, v48, v49\n"
"v_cvt_pk_bf16_f32 v33, v50, v51\n"
"v_exp_f32 v56, v56\n"
"v_exp_f32 v57, v57\n"
"v_exp_f32 v58, v58\n"
"v_exp_f32 v59, v59\n"
"v_cvt_pk_bf16_f32 v34, v52, v53\n"
"v_cvt_pk_bf16_f32 v35, v54, v55\n"
"v_mov_b32 v50, 0\n"
"v_mov_b32 v51, 0\n"
"v_exp_f32 v60, v60\n"
"v_exp_f32 v61, v61\n"
"v_mfma_f32_16x16x32_bf16 v[152:155], a[216:219], v[32:35], v[152:155]\n"
"buffer_load_dwordx4 a[40:43], v209, s[64:67], s81 offen sc1\n"
"v_exp_f32 v62, v62\n"
"v_mfma_f32_16x16x32_bf16 v[156:159], a[220:223], v[32:35], v[156:159]\n"
"v_exp_f32 v63, v63\n"
"v_mfma_f32_16x16x32_bf16 v[160:163], v[240:243], v[32:35], v[160:163]\n"
"v_cvt_pk_bf16_f32 v36, v56, v57\n"
"v_cvt_pk_bf16_f32 v37, v58, v59\n"
"v_exp_f32 v64, v64\n"
"v_exp_f32 v65, v65\n"
"v_exp_f32 v66, v66\n"
"buffer_load_dwordx4 a[44:47], v209, s[64:67], s81 offen offset:1024 sc1\n"
"v_exp_f32 v67, v67\n"
"v_cvt_pk_bf16_f32 v38, v60, v61\n"
"v_cvt_pk_bf16_f32 v39, v62, v63\n"
"v_exp_f32 v68, v68\n"
"v_exp_f32 v69, v69\n"
"v_mfma_f32_16x16x32_bf16 v[152:155], a[224:227], v[36:39], v[152:155]\n"
"s_add_u32 s81, s81, 0x1000\n"
"v_exp_f32 v70, v70\n"
"v_mfma_f32_16x16x32_bf16 v[156:159], a[228:231], v[36:39], v[156:159]\n"
"buffer_load_dwordx4 v218, s[60:63], s36 offen offset:1024 lds\n"
"v_exp_f32 v71, v71\n"
"v_mfma_f32_16x16x32_bf16 v[160:163], v[240:243], v[36:39], v[160:163]\n"
"v_cvt_pk_bf16_f32 v40, v64, v65\n"
"v_cvt_pk_bf16_f32 v41, v66, v67\n"
"v_exp_f32 v72, v72\n"
"v_exp_f32 v73, v73\n"
"v_exp_f32 v74, v74\n"
"v_exp_f32 v75, v75\n"
"v_cvt_pk_bf16_f32 v42, v68, v69\n"
"v_cvt_pk_bf16_f32 v43, v70, v71\n"
"v_exp_f32 v76, v76\n"
"v_exp_f32 v77, v77\n"
"v_mfma_f32_16x16x32_bf16 v[152:155], a[232:235], v[40:43], v[152:155]\n"
"buffer_load_dwordx4 a[48:51], v208, s[64:67], s81 offen sc1\n"
"v_exp_f32 v78, v78\n"
"v_mfma_f32_16x16x32_bf16 v[156:159], a[236:239], v[40:43], v[156:159]\n"
"v_exp_f32 v79, v79\n"
"v_mfma_f32_16x16x32_bf16 v[160:163], v[240:243], v[40:43], v[160:163]\n"
"v_cvt_pk_bf16_f32 v44, v72, v73\n"
"v_cvt_pk_bf16_f32 v45, v74, v75\n"
"v_exp_f32 v80, v80\n"
"v_exp_f32 v81, v81\n"
"v_exp_f32 v82, v82\n"
"buffer_load_dwordx4 a[52:55], v208, s[64:67], s81 offen offset:1024 sc1\n"
"v_exp_f32 v83, v83\n"
"v_cvt_pk_bf16_f32 v46, v76, v77\n"
"v_cvt_pk_bf16_f32 v47, v78, v79\n"
"v_cvt_pk_bf16_f32 v48, v80, v81\n"
"v_cvt_pk_bf16_f32 v49, v82, v83\n"
"v_max3_f32 v204, v84, v85, v86\n"
"v_max3_f32 v206, v87, v88, v89\n"
"v_mfma_f32_16x16x32_bf16 v[152:155], a[240:243], v[44:47], v[152:155]\n"
"v_max3_f32 v204, v204, v90, v91\n"
"v_max3_f32 v206, v206, v92, v93\n"
"v_mfma_f32_16x16x32_bf16 v[156:159], a[244:247], v[44:47], v[156:159]\n"
"buffer_load_dwordx4 v219, s[60:63], s36 offen offset:2048 lds\n"
"v_max3_f32 v204, v204, v94, v95\n"
"v_max3_f32 v206, v206, v96, v97\n"
"v_mfma_f32_16x16x32_bf16 v[160:163], v[240:243], v[44:47], v[160:163]\n"
"v_max3_f32 v204, v204, v98, v99\n"
"v_max3_f32 v206, v206, v100, v101\n"
"v_mfma_f32_16x16x32_bf16 v[152:155], a[248:251], v[48:51], v[152:155]\n"
"v_max3_f32 v204, v204, v102, v103\n"
"v_max3_f32 v206, v206, v104, v105\n"
"v_mfma_f32_16x16x32_bf16 v[156:159], a[252:255], v[48:51], v[156:159]\n"
"v_max3_f32 v204, v204, v106, v107\n"
"v_max3_f32 v206, v206, v108, v109\n"
"v_mfma_f32_16x16x32_bf16 v[160:163], v[240:243], v[48:51], v[160:163]\n"
"buffer_load_dwordx4 a[56:59], v209, s[64:67], s81 offen sc1\n"
"v_max3_f32 v204, v204, v110, v111\n"
"v_max3_f32 v206, v206, v112, v113\n"
"v_max3_f32 v204, v204, v114, v115\n"
"v_max3_f32 v206, v206, v116, v117\n"
"v_max3_f32 v204, v204, v118, v119\n"
"v_max3_f32 v206, v206, v120, v121\n"
"v_max3_f32 v204, v204, v122, v123\n"
"v_max3_f32 v206, v206, v124, v125\n"
"v_rcp_f32 v160, v160\n"
"s_nop 0\n"
"v_mul_f32 v152, v152, v160\n"
"v_mul_f32 v153, v153, v160\n"
"v_mul_f32 v154, v154, v160\n"
"v_mul_f32 v155, v155, v160\n"
"v_mul_f32 v156, v156, v160\n"
"v_mul_f32 v157, v157, v160\n"
"buffer_load_dwordx4 a[60:63], v209, s[64:67], s81 offen offset:1024 sc1\n"
"v_mul_f32 v158, v158, v160\n"
"s_add_u32 s81, s81, 0x1000\n"
"v_mul_f32 v159, v159, v160\n"
"v_cvt_pk_bf16_f32 v152, v152, v153\n"
"v_cvt_pk_bf16_f32 v153, v154, v155\n"
"v_cvt_pk_bf16_f32 v154, v156, v157\n"
"v_cvt_pk_bf16_f32 v155, v158, v159\n"
"v_max3_f32 v204, v204, v126, v127\n"
".Lpad9_%=:\n"
"s_cbranch_execz .Lpad13_%=\n"
"v_max3_f32 v206, v206, v128, v129\n"
"v_max3_f32 v204, v204, v130, v131\n"
"v_max3_f32 v206, v206, v132, v133\n"
"v_max3_f32 v204, v204, v134, v135\n"
"v_max3_f32 v206, v206, v136, v137\n"
"v_max3_f32 v204, v204, v138, v139\n"
"buffer_store_dwordx2 v[152:153], v227, s[64:67], s38 offen offset:2048 sc1\n"
"v_max3_f32 v206, v206, v140, v141\n"
"v_max3_f32 v204, v204, v142, v143\n"
"v_max3_f32 v206, v206, v144, v145\n"
"v_max3_f32 v204, v204, v146, v147\n"
"v_max3_f32 v206, v206, v148, v149\n"
"v_max3_f32 v204, v204, v150, v151\n"
"v_max_f32 v204, v204, v206\n"
"v_mov_b32 v205, v204\n"
"s_nop 1\n"
"v_permlane16_swap_b32 v205, v204\n"
"v_max_f32 v204, v204, v205\n"
"v_mov_b32 v205, v204\n"
"s_nop 1\n"
"v_permlane32_swap_b32 v205, v204\n"
"v_max_f32 v204, v204, v205\n"
"v_mul_f32 v202, 0xbe8293ee, v204\n"
"buffer_store_dwordx2 v[154:155], v227, s[64:67], s38 offen offset:2560 sc1\n"
"v_pk_fma_f32 v[84:85], v[84:85], s[88:89], v[202:203] op_sel_hi:[1,0,0]\n"
"v_pk_fma_f32 v[86:87], v[86:87], s[88:89], v[202:203] op_sel_hi:[1,0,0]\n"
"v_pk_fma_f32 v[88:89], v[88:89], s[88:89], v[202:203] op_sel_hi:[1,0,0]\n"
"v_pk_fma_f32 v[90:91], v[90:91], s[88:89], v[202:203] op_sel_hi:[1,0,0]\n"
"v_pk_fma_f32 v[92:93], v[92:93], s[88:89], v[202:203] op_sel_hi:[1,0,0]\n"
"v_pk_fma_f32 v[94:95], v[94:95], s[88:89], v[202:203] op_sel_hi:[1,0,0]\n"
"v_pk_fma_f32 v[96:97], v[96:97], s[88:89], v[202:203] op_sel_hi:[1,0,0]\n"
"v_pk_fma_f32 v[98:99], v[98:99], s[88:89], v[202:203] op_sel_hi:[1,0,0]\n"
"v_pk_fma_f32 v[100:101], v[100:101], s[88:89], v[202:203] op_sel_hi:[1,0,0]\n"
"v_pk_fma_f32 v[102:103], v[102:103], s[88:89], v[202:203] op_sel_hi:[1,0,0]\n"
"v_pk_fma_f32 v[104:105], v[104:105], s[88:89], v[202:203] op_sel_hi:[1,0,0]\n"
"v_pk_fma_f32 v[106:107], v[106:107], s[88:89], v[202:203] op_sel_hi:[1,0,0]\n"
"v_pk_fma_f32 v[108:109], v[108:109], s[88:89], v[202:203] op_sel_hi:[1,0,0]\n"
"v_pk_fma_f32 v[110:111], v[110:111], s[88:89], v[202:203] op_sel_hi:[1,0,0]\n"
"buffer_load_dwordx4 v220, s[60:63], s36 offen offset:3072 lds\n"
"v_pk_fma_f32 v[112:113], v[112:113], s[88:89], v[202:203] op_sel_hi:[1,0,0]\n"
"s_add_u32 m0, s82, 0x8000\n"
"v_pk_fma_f32 v[114:115], v[114:115], s[88:89], v[202:203] op_sel_hi:[1,0,0]\n"
"s_add_u32 s36, s36, 0x40000\n"
"v_pk_fma_f32 v[116:117], v[116:117], s[88:89], v[202:203] op_sel_hi:[1,0,0]\n"
"v_pk_fma_f32 v[118:119], v[118:119], s[88:89], v[202:203] op_sel_hi:[1,0,0]\n"
"v_pk_fma_f32 v[120:121], v[120:121], s[88:89], v[202:203] op_sel_hi:[1,0,0]\n"
"v_pk_fma_f32 v[122:123], v[122:123], s[88:89], v[202:203] op_sel_hi:[1,0,0]\n"
"v_pk_fma_f32 v[124:125], v[124:125], s[88:89], v[202:203] op_sel_hi:[1,0,0]\n"
"v_pk_fma_f32 v[126:127], v[126:127], s[88:89], v[202:203] op_sel_hi:[1,0,0]\n"
"v_pk_fma_f32 v[128:129], v[128:129], s[88:89], v[202:203] op_sel_hi:[1,0,0]\n"
"v_pk_fma_f32 v[130:131], v[130:131], s[88:89], v[202:203] op_sel_hi:[1,0,0]\n"
"v_pk_fma_f32 v[132:133], v[132:133], s[88:89], v[202:203] op_sel_hi:[1,0,0]\n"
"v_pk_fma_f32 v[134:135], v[134:135], s[88:89], v[202:203] op_sel_hi:[1,0,0]\n"
"buffer_load_dwordx4 a[64:67], v208, s[64:67], s81 offen sc1\n"
"v_pk_fma_f32 v[136:137], v[136:137], s[88:89], v[202:203] op_sel_hi:[1,0,0]\n"
"v_pk_fma_f32 v[138:139], v[138:139], s[88:89], v[202:203] op_sel_hi:[1,0,0]\n"
"v_pk_fma_f32 v[140:141], v[140:141], s[88:89], v[202:203] op_sel_hi:[1,0,0]\n"
"v_pk_fma_f32 v[142:143], v[142:143], s[88:89], v[202:203] op_sel_hi:[1,0,0]\n"
"v_pk_fma_f32 v[144:145], v[144:145], s[88:89], v[202:203] op_sel_hi:[1,0,0]\n"
"v_pk_fma_f32 v[146:147], v[146:147], s[88:89], v[202:203] op_sel_hi:[1,0,0]\n"
"v_pk_fma_f32 v[148:149], v[148:149], s[88:89], v[202:203] op_sel_hi:[1,0,0]\n"
"v_pk_fma_f32 v[150:151], v[150:151], s[88:89], v[202:203] op_sel_hi:[1,0,0]\n"
"v_exp_f32 v84, v84\n"
"v_exp_f32 v85, v85\n"
"v_exp_f32 v86, v86\n"
"buffer_load_dwordx4 a[68:71], v208, s[64:67], s81 offen offset:1024 sc1\n"
"v_exp_f32 v87, v87\n"
"v_exp_f32 v88, v88\n"
"v_exp_f32 v89, v89\n"
"v_exp_f32 v90, v90\n"
"v_exp_f32 v91, v91\n"
"v_cvt_pk_bf16_f32 v87, v86, v87\n"
"v_cvt_pk_bf16_f32 v86, v84, v85\n"
"v_mov_b32 v84, 0\n"
"v_mov_b32 v85, 0\n"
"v_exp_f32 v92, v92\n"
"v_exp_f32 v93, v93\n"
"v_mfma_f32_16x16x32_bf16 v[164:167], a[184:187], v[84:87], 0\n"
"buffer_load_dwordx4 v217, s[60:63], s36 offen lds\n"
"v_exp_f32 v94, v94\n"
"v_mfma_f32_16x16x32_bf16 v[168:171], a[188:191], v[84:87], 0\n"
"v_exp_f32 v95, v95\n"
"v_mfma_f32_16x16x32_bf16 v[172:175], v[240:243], v[84:87], 0\n"
"v_cvt_pk_bf16_f32 v88, v88, v89\n"
"v_cvt_pk_bf16_f32 v89, v90, v91\n"
"v_exp_f32 v96, v96\n"
"v_exp_f32 v97, v97\n"
"v_exp_f32 v98, v98\n"
"buffer_load_dwordx4 a[72:75], v209, s[64:67], s81 offen sc1\n"
"v_exp_f32 v99, v99\n"
"v_cvt_pk_bf16_f32 v90, v92, v93\n"
"v_cvt_pk_bf16_f32 v91, v94, v95\n"
"v_exp_f32 v100, v100\n"
"v_exp_f32 v101, v101\n"
"v_mfma_f32_16x16x32_bf16 v[164:167], a[192:195], v[88:91], v[164:167]\n"
"v_exp_f32 v102, v102\n"
"v_mfma_f32_16x16x32_bf16 v[168:171], a[196:199], v[88:91], v[168:171]\n"
"buffer_load_dwordx4 a[76:79], v209, s[64:67], s81 offen offset:1024 sc1\n"
"s_add_u32 s81, s81, 0x1000\n"
"v_exp_f32 v103, v103\n"
"v_mfma_f32_16x16x32_bf16 v[172:175], v[240:243], v[88:91], v[172:175]\n"
"v_cvt_pk_bf16_f32 v92, v96, v97\n"
"v_cvt_pk_bf16_f32 v93, v98, v99\n"
"v_exp_f32 v104, v104\n"
"v_exp_f32 v105, v105\n"
"v_exp_f32 v106, v106\n"
"v_exp_f32 v107, v107\n"
"v_cvt_pk_bf16_f32 v94, v100, v101\n"
"v_cvt_pk_bf16_f32 v95, v102, v103\n"
"v_exp_f32 v108, v108\n"
"v_exp_f32 v109, v109\n"
"v_mfma_f32_16x16x32_bf16 v[164:167], a[200:203], v[92:95], v[164:167]\n"
"buffer_load_dwordx4 v218, s[60:63], s36 offen offset:1024 lds\n"
"v_exp_f32 v110, v110\n"
"v_mfma_f32_16x16x32_bf16 v[168:171], a[204:207], v[92:95], v[168:171]\n"
"v_exp_f32 v111, v111\n"
"v_mfma_f32_16x16x32_bf16 v[172:175], v[240:243], v[92:95], v[172:175]\n"
"v_cvt_pk_bf16_f32 v96, v104, v105\n"
"v_cvt_pk_bf16_f32 v97, v106, v107\n"
"v_exp_f32 v112, v112\n"
"v_exp_f32 v113, v113\n"
"v_exp_f32 v114, v114\n"
"buffer_load_dwordx4 a[80:83], v208, s[64:67], s81 offen sc1\n"
"v_exp_f32 v115, v115\n"
"v_cvt_pk_bf16_f32 v98, v108, v109\n"
"v_cvt_pk_bf16_f32 v99, v110, v111\n"
"v_exp_f32 v116, v116\n"
"v_exp_f32 v117, v117\n"
"v_mfma_f32_16x16x32_bf16 v[164:167], a[208:211], v[96:99], v[164:167]\n"
"v_exp_f32 v118, v118\n"
"v_mfma_f32_16x16x32_bf16 v[168:171], a[212:215], v[96:99], v[168:171]\n"
"buffer_load_dwordx4 a[84:87], v208, s[64:67], s81 offen offset:1024 sc1\n"
"v_exp_f32 v119, v119\n"
"v_mfma_f32_16x16x32_bf16 v[172:175], v[240:243], v[96:99], v[172:175]\n"
"v_cvt_pk_bf16_f32 v100, v112, v113\n"
"v_cvt_pk_bf16_f32 v101, v114, v115\n"
"v_exp_f32 v120, v120\n"
"v_exp_f32 v121, v121\n"
"v_exp_f32 v122, v122\n"
"v_exp_f32 v123, v123\n"
"v_cvt_pk_bf16_f32 v102, v116, v117\n"
"v_cvt_pk_bf16_f32 v103, v118, v119\n"
"v_exp_f32 v124, v124\n"
"v_exp_f32 v125, v125\n"
"v_mfma_f32_16x16x32_bf16 v[164:167], a[216:219], v[100:103], v[164:167]\n"
"buffer_load_dwordx4 v219, s[60:63], s36 offen offset:2048 lds\n"
"v_exp_f32 v126, v126\n"
"v_mfma_f32_16x16x32_bf16 v[168:171], a[220:223], v[100:103], v[168:171]\n"
"v_exp_f32 v127, v127\n"
"v_mfma_f32_16x16x32_bf16 v[172:175], v[240:243], v[100:103], v[172:175]\n"
"v_cvt_pk_bf16_f32 v104, v120, v121\n"
"v_cvt_pk_bf16_f32 v105, v122, v123\n"
"v_exp_f32 v128, v128\n"
"v_exp_f32 v129, v129\n"
"v_exp_f32 v130, v130\n"
"buffer_load_dwordx4 a[88:91], v209, s[64:67], s81 offen sc1\n"
"v_exp_f32 v131, v131\n"
"v_cvt_pk_bf16_f32 v106, v124, v125\n"
"v_cvt_pk_bf16_f32 v107, v126, v127\n"
"v_exp_f32 v132, v132\n"
"v_exp_f32 v133, v133\n"
"v_mfma_f32_16x16x32_bf16 v[164:167], a[224:227], v[104:107], v[164:167]\n"
"v_exp_f32 v134, v134\n"
"v_mfma_f32_16x16x32_bf16 v[168:171], a[228:231], v[104:107], v[168:171]\n"
"buffer_load_dwordx4 a[92:95], v209, s[64:67], s81 offen offset:1024 sc1\n"
"s_add_u32 s81, s81, 0x1000\n"
"v_exp_f32 v135, v135\n"
"v_mfma_f32_16x16x32_bf16 v[172:175], v[240:243], v[104:107], v[172:175]\n"
"v_cvt_pk_bf16_f32 v108, v128, v129\n"
"v_cvt_pk_bf16_f32 v109, v130, v131\n"
"v_exp_f32 v136, v136\n"
"v_exp_f32 v137, v137\n"
"v_exp_f32 v138, v138\n"
"v_exp_f32 v139, v139\n"
"v_cvt_pk_bf16_f32 v110, v132, v133\n"
"v_cvt_pk_bf16_f32 v111, v134, v135\n"
"v_exp_f32 v140, v140\n"
"v_exp_f32 v141, v141\n"
"v_mfma_f32_16x16x32_bf16 v[164:167], a[232:235], v[108:111], v[164:167]\n"
"buffer_load_dwordx4 v220, s[60:63], s36 offen offset:3072 lds\n"
"s_add_u32 m0, s82, 0xc000\n"
"v_exp_f32 v142, v142\n"
"v_mfma_f32_16x16x32_bf16 v[168:171], a[236:239], v[108:111], v[168:171]\n"
"s_add_u32 s36, s36, 0x40000\n"
"v_exp_f32 v143, v143\n"
"v_mfma_f32_16x16x32_bf16 v[172:175], v[240:243], v[108:111], v[172:175]\n"
"v_cvt_pk_bf16_f32 v112, v136, v137\n"
"v_cvt_pk_bf16_f32 v113, v138, v139\n"
"v_exp_f32 v144, v144\n"
"v_exp_f32 v145, v145\n"
"buffer_load_dwordx4 v217, s[60:63], s36 offen lds\n"
"v_exp_f32 v146, v146\n"
"v_exp_f32 v147, v147\n"
"v_cvt_pk_bf16_f32 v114, v140, v141\n"
"v_cvt_pk_bf16_f32 v115, v142, v143\n"
"v_exp_f32 v148, v148\n"
"v_exp_f32 v149, v149\n"
"v_mfma_f32_16x16x32_bf16 v[164:167], a[240:243], v[112:115], v[164:167]\n"
"v_exp_f32 v150, v150\n"
"v_mfma_f32_16x16x32_bf16 v[168:171], a[244:247], v[112:115], v[168:171]\n"
"buffer_load_dwordx4 v218, s[60:63], s36 offen offset:1024 lds\n"
"v_exp_f32 v151, v151\n"
"v_mfma_f32_16x16x32_bf16 v[172:175], v[240:243], v[112:115], v[172:175]\n"
"v_cvt_pk_bf16_f32 v116, v144, v145\n"
"v_cvt_pk_bf16_f32 v117, v146, v147\n"
"v_cvt_pk_bf16_f32 v118, v148, v149\n"
"v_cvt_pk_bf16_f32 v119, v150, v151\n"
"s_nop 1\n"
"v_mfma_f32_16x16x32_bf16 v[164:167], a[248:251], v[116:119], v[164:167]\n"
"v_mfma_f32_16x16x32_bf16 v[168:171], a[252:255], v[116:119], v[168:171]\n"
"v_mfma_f32_16x16x32_bf16 v[172:175], v[240:243], v[116:119], v[172:175]\n"
"s_nop 7\n"
"v_rcp_f32 v172, v172\n"
"buffer_load_dwordx4 v219, s[60:63], s36 offen offset:2048 lds\n"
"v_mul_f32 v164, v164, v172\n"
"v_mul_f32 v165, v165, v172\n"
"v_mul_f32 v166, v166, v172\n"
"v_mul_f32 v167, v167, v172\n"
"v_mul_f32 v168, v168, v172\n"
"v_mul_f32 v169, v169, v172\n"
"v_mul_f32 v170, v170, v172\n"
"v_mul_f32 v171, v171, v172\n"
"v_cvt_pk_bf16_f32 v164, v164, v165\n"
"v_cvt_pk_bf16_f32 v165, v166, v167\n"
"v_cvt_pk_bf16_f32 v166, v168, v169\n"
"v_cvt_pk_bf16_f32 v167, v170, v171\n"
"buffer_store_dwordx2 v[164:165], v227, s[64:67], s38 offen offset:3072 sc1\n"
"buffer_store_dwordx2 v[166:167], v227, s[64:67], s38 offen offset:3584 sc1\n"
"buffer_load_dwordx4 v220, s[60:63], s36 offen offset:3072 lds\n"
"s_add_u32 s36, s36, 0x40000\n"
"s_barrier\n"
"ds_read_b64_tr_b16 a[128:129], v221\n"
"ds_read_b64_tr_b16 a[130:131], v221 offset:2048\n"
"ds_read_b64_tr_b16 a[132:133], v222\n"
"ds_read_b64_tr_b16 a[134:135], v222 offset:2048\n"
"ds_read_b64_tr_b16 a[136:137], v223\n"
"ds_read_b64_tr_b16 a[138:139], v223 offset:2048\n"
"ds_read_b64_tr_b16 a[140:141], v239\n"
"ds_read_b64_tr_b16 a[142:143], v239 offset:2048\n"
"ds_read_b64_tr_b16 a[144:145], v221 offset:16384\n"
"ds_read_b64_tr_b16 a[146:147], v221 offset:18432\n"
"s_waitcnt vmcnt(41) lgkmcnt(8)\n"
"v_mfma_f32_16x16x32_bf16 v[64:67], a[128:131], a[0:3], 0\n"
"ds_read_b64_tr_b16 a[148:149], v222 offset:16384\n"
"v_mfma_f32_16x16x32_bf16 v[96:99], a[128:131], a[4:7], 0\n"
"ds_read_b64_tr_b16 a[150:151], v222 offset:18432\n"
"v_mfma_f32_16x16x32_bf16 v[128:131], a[128:131], a[8:11], 0\n"
"buffer_load_dwordx4 a[96:99], v208, s[64:67], s81 offen sc1\n"
"v_mfma_f32_16x16x32_bf16 v[160:163], a[128:131], a[12:15], 0\n"
"s_waitcnt lgkmcnt(8)\n"
"v_add_u32 v44, 0x10000, v221\n"
"v_mfma_f32_16x16x32_bf16 v[164:167], a[132:135], a[12:15], 0\n"
"ds_read_b64_tr_b16 a[152:153], v223 offset:16384\n"
"v_add_u32 v45, 0x10000, v222\n"
"v_mfma_f32_16x16x32_bf16 v[132:135], a[132:135], a[8:11], 0\n"
"ds_read_b64_tr_b16 a[154:155], v223 offset:18432\n"
"v_add_u32 v46, 0x10000, v223\n"
"v_mfma_f32_16x16x32_bf16 v[100:103], a[132:135], a[4:7], 0\n"
"buffer_load_dwordx4 a[100:103], v208, s[64:67], s81 offen offset:1024 sc1\n"
"v_add_u32 v47, 0x10000, v239\n"
"v_mfma_f32_16x16x32_bf16 v[68:71], a[132:135], a[0:3], 0\n"
"s_waitcnt lgkmcnt(8)\n"
"v_subrev_u32 v40, 0x10000, v221\n"
"v_mfma_f32_16x16x32_bf16 v[72:75], a[136:139], a[0:3], 0\n"
"ds_read_b64_tr_b16 a[156:157], v239 offset:16384\n"
"v_subrev_u32 v41, 0x10000, v222\n"
"v_mfma_f32_16x16x32_bf16 v[104:107], a[136:139], a[4:7], 0\n"
"ds_read_b64_tr_b16 a[158:159], v239 offset:18432\n"
"v_subrev_u32 v42, 0x10000, v223\n"
"v_mfma_f32_16x16x32_bf16 v[136:139], a[136:139], a[8:11], 0\n"
"buffer_load_dwordx4 a[104:107], v209, s[64:67], s81 offen sc1\n"
"v_subrev_u32 v43, 0x10000, v239\n"
"v_mfma_f32_16x16x32_bf16 v[168:171], a[136:139], a[12:15], 0\n"
"s_waitcnt lgkmcnt(8)\n"
"s_lshr_b32 s44, s54, 3\n"
"v_mfma_f32_16x16x32_bf16 v[172:175], a[140:143], a[12:15], 0\n"
"ds_read_b64_tr_b16 a[128:129], v221 offset:4096\n"
"s_lshl_b32 s44, s44, 2\n"
"v_mfma_f32_16x16x32_bf16 v[140:143], a[140:143], a[8:11], 0\n"
"ds_read_b64_tr_b16 a[130:131], v221 offset:6144\n"
"s_add_u32 s44, s44, s78\n"
"v_mfma_f32_16x16x32_bf16 v[108:111], a[140:143], a[4:7], 0\n"
"buffer_load_dwordx4 a[108:111], v209, s[64:67], s81 offen offset:1024 sc1\n"
"s_mul_i32 s44, s44, 128\n"
"v_mfma_f32_16x16x32_bf16 v[76:79], a[140:143], a[0:3], 0\n"
"s_add_u32 s81, s81, 0x1000\n"
"s_waitcnt lgkmcnt(8)\n"
"v_mfma_f32_16x16x32_bf16 v[80:83], a[144:147], a[0:3], 0\n"
"ds_read_b64_tr_b16 a[132:133], v222 offset:4096\n"
"s_add_u32 s87, s44, 2048\n"
"v_mfma_f32_16x16x32_bf16 v[112:115], a[144:147], a[4:7], 0\n"
"ds_read_b64_tr_b16 a[134:135], v222 offset:6144\n"
"s_getpc_b64 s[44:45]\n"
"v_mfma_f32_16x16x32_bf16 v[144:147], a[144:147], a[8:11], 0\n"
"buffer_load_dwordx4 a[112:115], v208, s[64:67], s81 offen sc1\n"
"s_add_u32 s44, s44, s87\n"
"s_addc_u32 s45, s45, 0\n"
"v_mfma_f32_16x16x32_bf16 v[176:179], a[144:147], a[12:15], 0\n"
"s_waitcnt lgkmcnt(8)\n"
"s_load_dword s33, s[44:45], 0x0\n"
"v_mfma_f32_16x16x32_bf16 v[180:183], a[148:151], a[12:15], 0\n"
"ds_read_b64_tr_b16 a[136:137], v223 offset:4096\n"
"s_load_dword s34, s[44:45], 0x40\n"
"v_mfma_f32_16x16x32_bf16 v[148:151], a[148:151], a[8:11], 0\n"
"ds_read_b64_tr_b16 a[138:139], v223 offset:6144\n"
"v_lshrrev_b32 v56, 2, v236\n"
"v_mfma_f32_16x16x32_bf16 v[116:119], a[148:151], a[4:7], 0\n"
"v_and_b32 v56, 12, v56\n"
"v_mfma_f32_16x16x32_bf16 v[84:87], a[148:151], a[0:3], 0\n"
"s_waitcnt lgkmcnt(8)\n"
"v_and_b32 v57, 15, v236\n"
"v_mfma_f32_16x16x32_bf16 v[88:91], a[152:155], a[0:3], 0\n"
"ds_read_b64_tr_b16 a[140:141], v239 offset:4096\n"
"v_sub_u32 v56, v57, v56\n"
"v_mfma_f32_16x16x32_bf16 v[120:123], a[152:155], a[4:7], 0\n"
"ds_read_b64_tr_b16 a[142:143], v239 offset:6144\n"
"v_mov_b32 v57, 0\n"
"v_mfma_f32_16x16x32_bf16 v[152:155], a[152:155], a[8:11], 0\n"
"s_waitcnt vmcnt(32)\n"
"buffer_load_dwordx4 a[116:119], v208, s[64:67], s81 offen offset:1024 sc1\n"
"v_mov_b32 v58, 0x3f80\n"
"v_mfma_f32_16x16x32_bf16 v[184:187], a[152:155], a[12:15], 0\n"
"s_waitcnt lgkmcnt(8)\n"
"v_mov_b32 v59, 0x3f800000\n"
"v_mfma_f32_16x16x32_bf16 v[188:191], a[156:159], a[12:15], 0\n"
"ds_read_b64_tr_b16 a[144:145], v221 offset:20480\n"
"v_mov_b32 v210, 0\n"
"v_mfma_f32_16x16x32_bf16 v[156:159], a[156:159], a[8:11], 0\n"
"ds_read_b64_tr_b16 a[146:147], v221 offset:22528\n"
"v_mov_b32 v211, 0\n"
"v_mfma_f32_16x16x32_bf16 v[124:127], a[156:159], a[4:7], 0\n"
"buffer_load_dwordx4 a[120:123], v209, s[64:67], s81 offen sc1\n"
"v_mov_b32 v214, 0\n"
"v_mfma_f32_16x16x32_bf16 v[92:95], a[156:159], a[0:3], 0\n"
"s_waitcnt lgkmcnt(8)\n"
"v_mov_b32 v215, 0\n"
"v_mfma_f32_16x16x32_bf16 v[64:67], a[128:131], a[16:19], v[64:67]\n"
"ds_read_b64_tr_b16 a[148:149], v222 offset:20480\n"
"v_cmp_eq_u32 vcc, 0, v56\n"
"v_mfma_f32_16x16x32_bf16 v[96:99], a[128:131], a[20:23], v[96:99]\n"
"ds_read_b64_tr_b16 a[150:151], v222 offset:22528\n"
"v_cndmask_b32 v212, v57, v58, vcc\n"
"v_mfma_f32_16x16x32_bf16 v[128:131], a[128:131], a[24:27], v[128:131]\n"
"buffer_load_dwordx4 a[124:127], v209, s[64:67], s81 offen offset:1024 sc1\n"
"v_mfma_f32_16x16x32_bf16 v[160:163], a[128:131], a[28:31], v[160:163]\n"
"s_add_u32 s81, s81, 0x1000\n"
"s_waitcnt lgkmcnt(8)\n"
"v_mfma_f32_16x16x32_bf16 v[164:167], a[132:135], a[28:31], v[164:167]\n"
"ds_read_b64_tr_b16 a[152:153], v223 offset:20480\n"
"v_cmp_eq_u32 vcc, 1, v56\n"
"v_mfma_f32_16x16x32_bf16 v[132:135], a[132:135], a[24:27], v[132:135]\n"
"ds_read_b64_tr_b16 a[154:155], v223 offset:22528\n"
"v_cndmask_b32 v212, v212, v59, vcc\n"
"v_mfma_f32_16x16x32_bf16 v[100:103], a[132:135], a[20:23], v[100:103]\n"
"buffer_load_dwordx4 a[160:163], v208, s[64:67], s81 offen sc1\n"
"v_mfma_f32_16x16x32_bf16 v[68:71], a[132:135], a[16:19], v[68:71]\n"
"s_waitcnt lgkmcnt(8)\n"
"v_cmp_eq_u32 vcc, 2, v56\n"
"v_mfma_f32_16x16x32_bf16 v[72:75], a[136:139], a[16:19], v[72:75]\n"
"ds_read_b64_tr_b16 a[156:157], v239 offset:20480\n"
"v_mfma_f32_16x16x32_bf16 v[104:107], a[136:139], a[20:23], v[104:107]\n"
"ds_read_b64_tr_b16 a[158:159], v239 offset:22528\n"
"v_cndmask_b32 v213, v57, v58, vcc\n"
"v_mfma_f32_16x16x32_bf16 v[136:139], a[136:139], a[24:27], v[136:139]\n"
"buffer_load_dwordx4 a[164:167], v208, s[64:67], s81 offen offset:1024 sc1\n"
"v_mfma_f32_16x16x32_bf16 v[168:171], a[136:139], a[28:31], v[168:171]\n"
"s_waitcnt lgkmcnt(8)\n"
"v_cmp_eq_u32 vcc, 3, v56\n"
"v_mfma_f32_16x16x32_bf16 v[172:175], a[140:143], a[28:31], v[172:175]\n"
"ds_read_b64_tr_b16 a[128:129], v221 offset:8192\n"
"v_mfma_f32_16x16x32_bf16 v[140:143], a[140:143], a[24:27], v[140:143]\n"
"ds_read_b64_tr_b16 a[130:131], v221 offset:10240\n"
"v_cndmask_b32 v213, v213, v59, vcc\n"
"v_mfma_f32_16x16x32_bf16 v[108:111], a[140:143], a[20:23], v[108:111]\n"
"v_mfma_f32_16x16x32_bf16 v[76:79], a[140:143], a[16:19], v[76:79]\n"
"s_waitcnt lgkmcnt(8)\n"
"v_mfma_f32_16x16x32_bf16 v[80:83], a[144:147], a[16:19], v[80:83]\n"
"ds_read_b64_tr_b16 a[132:133], v222 offset:8192\n"
"v_mfma_f32_16x16x32_bf16 v[112:115], a[144:147], a[20:23], v[112:115]\n"
"ds_read_b64_tr_b16 a[134:135], v222 offset:10240\n"
"v_mfma_f32_16x16x32_bf16 v[144:147], a[144:147], a[24:27], v[144:147]\n"
"buffer_load_dwordx4 a[168:171], v209, s[64:67], s81 offen sc1\n"
"v_mfma_f32_16x16x32_bf16 v[176:179], a[144:147], a[28:31], v[176:179]\n"
"s_waitcnt lgkmcnt(8)\n"
"v_mfma_f32_16x16x32_bf16 v[180:183], a[148:151], a[28:31], v[180:183]\n"
"ds_read_b64_tr_b16 a[136:137], v223 offset:8192\n"
"v_mfma_f32_16x16x32_bf16 v[148:151], a[148:151], a[24:27], v[148:151]\n"
"ds_read_b64_tr_b16 a[138:139], v223 offset:10240\n"
"v_mfma_f32_16x16x32_bf16 v[116:119], a[148:151], a[20:23], v[116:119]\n"
"buffer_load_dwordx4 a[172:175], v209, s[64:67], s81 offen offset:1024 sc1\n"
"v_mfma_f32_16x16x32_bf16 v[84:87], a[148:151], a[16:19], v[84:87]\n"
"s_add_u32 s81, s81, 0x1000\n"
"s_waitcnt lgkmcnt(8)\n"
"v_mfma_f32_16x16x32_bf16 v[88:91], a[152:155], a[16:19], v[88:91]\n"
"ds_read_b64_tr_b16 a[140:141], v239 offset:8192\n"
"v_mfma_f32_16x16x32_bf16 v[120:123], a[152:155], a[20:23], v[120:123]\n"
"ds_read_b64_tr_b16 a[142:143], v239 offset:10240\n"
"v_mfma_f32_16x16x32_bf16 v[152:155], a[152:155], a[24:27], v[152:155]\n"
"buffer_load_dwordx4 a[176:179], v208, s[64:67], s81 offen sc1\n"
"v_mfma_f32_16x16x32_bf16 v[184:187], a[152:155], a[28:31], v[184:187]\n"
"s_waitcnt lgkmcnt(8)\n"
"v_mfma_f32_16x16x32_bf16 v[188:191], a[156:159], a[28:31], v[188:191]\n"
"ds_read_b64_tr_b16 a[144:145], v221 offset:24576\n"
"v_mfma_f32_16x16x32_bf16 v[156:159], a[156:159], a[24:27], v[156:159]\n"
"ds_read_b64_tr_b16 a[146:147], v221 offset:26624\n"
"v_mfma_f32_16x16x32_bf16 v[124:127], a[156:159], a[20:23], v[124:127]\n"
"buffer_load_dwordx4 a[180:183], v208, s[64:67], s81 offen offset:1024 sc1\n"
"v_mfma_f32_16x16x32_bf16 v[92:95], a[156:159], a[16:19], v[92:95]\n"
"s_waitcnt lgkmcnt(8)\n"
"v_mfma_f32_16x16x32_bf16 v[64:67], a[128:131], a[32:35], v[64:67]\n"
"ds_read_b64_tr_b16 a[148:149], v222 offset:24576\n"
"v_mfma_f32_16x16x32_bf16 v[96:99], a[128:131], a[36:39], v[96:99]\n"
"ds_read_b64_tr_b16 a[150:151], v222 offset:26624\n"
"v_mfma_f32_16x16x32_bf16 v[128:131], a[128:131], a[40:43], v[128:131]\n"
"buffer_load_dwordx4 a[184:187], v209, s[64:67], s81 offen sc1\n"
"v_mfma_f32_16x16x32_bf16 v[160:163], a[128:131], a[44:47], v[160:163]\n"
"s_waitcnt lgkmcnt(8)\n"
"v_mfma_f32_16x16x32_bf16 v[164:167], a[132:135], a[44:47], v[164:167]\n"
"ds_read_b64_tr_b16 a[152:153], v223 offset:24576\n"
"v_mfma_f32_16x16x32_bf16 v[132:135], a[132:135], a[40:43], v[132:135]\n"
"ds_read_b64_tr_b16 a[154:155], v223 offset:26624\n"
"v_mfma_f32_16x16x32_bf16 v[100:103], a[132:135], a[36:39], v[100:103]\n"
"v_mfma_f32_16x16x32_bf16 v[68:71], a[132:135], a[32:35], v[68:71]\n"
"s_waitcnt lgkmcnt(8)\n"
"v_mfma_f32_16x16x32_bf16 v[72:75], a[136:139], a[32:35], v[72:75]\n"
"ds_read_b64_tr_b16 a[156:157], v239 offset:24576\n"
"v_mfma_f32_16x16x32_bf16 v[104:107], a[136:139], a[36:39], v[104:107]\n"
"ds_read_b64_tr_b16 a[158:159], v239 offset:26624\n"
"v_mfma_f32_16x16x32_bf16 v[136:139], a[136:139], a[40:43], v[136:139]\n"
"buffer_load_dwordx4 a[188:191], v209, s[64:67], s81 offen offset:1024 sc1\n"
"v_mfma_f32_16x16x32_bf16 v[168:171], a[136:139], a[44:47], v[168:171]\n"
"s_add_u32 s81, s81, 0x1000\n"
"s_waitcnt lgkmcnt(8)\n"
"v_mfma_f32_16x16x32_bf16 v[172:175], a[140:143], a[44:47], v[172:175]\n"
"ds_read_b64_tr_b16 a[128:129], v221 offset:12288\n"
"v_mfma_f32_16x16x32_bf16 v[140:143], a[140:143], a[40:43], v[140:143]\n"
"ds_read_b64_tr_b16 a[130:131], v221 offset:14336\n"
"v_mfma_f32_16x16x32_bf16 v[108:111], a[140:143], a[36:39], v[108:111]\n"
"buffer_load_dwordx4 a[192:195], v208, s[64:67], s81 offen sc1\n"
"v_mfma_f32_16x16x32_bf16 v[76:79], a[140:143], a[32:35], v[76:79]\n"
"s_waitcnt lgkmcnt(8)\n"
"v_mfma_f32_16x16x32_bf16 v[80:83], a[144:147], a[32:35], v[80:83]\n"
"ds_read_b64_tr_b16 a[132:133], v222 offset:12288\n"
"s_add_u32 s42, s42, 0x20000\n"
"v_mfma_f32_16x16x32_bf16 v[112:115], a[144:147], a[36:39], v[112:115]\n"
"ds_read_b64_tr_b16 a[134:135], v222 offset:14336\n"
"v_mfma_f32_16x16x32_bf16 v[144:147], a[144:147], a[40:43], v[144:147]\n"
"s_waitcnt vmcnt(18)\n"
"buffer_store_dwordx2 v[228:229], v225, s[64:67], s42 offen sc1\n"
"v_mfma_f32_16x16x32_bf16 v[176:179], a[144:147], a[44:47], v[176:179]\n"
"s_waitcnt lgkmcnt(8)\n"
"v_and_b32 v62, 15, v236\n"
"v_mfma_f32_16x16x32_bf16 v[180:183], a[148:151], a[44:47], v[180:183]\n"
"ds_read_b64_tr_b16 a[136:137], v223 offset:12288\n"
"v_lshrrev_b32 v63, 4, v236\n"
"v_mfma_f32_16x16x32_bf16 v[148:151], a[148:151], a[40:43], v[148:151]\n"
"ds_read_b64_tr_b16 a[138:139], v223 offset:14336\n"
"v_lshrrev_b32 v56, 3, v236\n"
"v_mfma_f32_16x16x32_bf16 v[116:119], a[148:151], a[36:39], v[116:119]\n"
"buffer_load_dwordx4 a[196:199], v208, s[64:67], s81 offen offset:1024 sc1\n"
"v_and_b32 v57, 7, v236\n"
"v_mfma_f32_16x16x32_bf16 v[84:87], a[148:151], a[32:35], v[84:87]\n"
"s_waitcnt lgkmcnt(8)\n"
"v_lshlrev_b32 v216, 4, v57\n"
"v_mfma_f32_16x16x32_bf16 v[88:91], a[152:155], a[32:35], v[88:91]\n"
"ds_read_b64_tr_b16 a[140:141], v239 offset:12288\n"
"v_add_u32 v216, s39, v216\n"
"v_mfma_f32_16x16x32_bf16 v[120:123], a[152:155], a[36:39], v[120:123]\n"
"ds_read_b64_tr_b16 a[142:143], v239 offset:14336\n"
"v_xor_b32 v58, v56, v57\n"
"v_mfma_f32_16x16x32_bf16 v[152:155], a[152:155], a[40:43], v[152:155]\n"
"buffer_load_dwordx4 a[200:203], v209, s[64:67], s81 offen sc1\n"
"v_lshlrev_b32 v58, 4, v58\n"
"v_mfma_f32_16x16x32_bf16 v[184:187], a[152:155], a[44:47], v[184:187]\n"
"s_waitcnt lgkmcnt(8)\n"
"v_lshl_add_u32 v59, v56, 11, v58\n"
"v_mfma_f32_16x16x32_bf16 v[188:191], a[156:159], a[44:47], v[188:191]\n"
"ds_read_b64_tr_b16 a[144:145], v221 offset:28672\n"
"s_lshl_b32 s93, s35, 1\n"
"v_mfma_f32_16x16x32_bf16 v[156:159], a[156:159], a[40:43], v[156:159]\n"
"ds_read_b64_tr_b16 a[146:147], v221 offset:30720\n"
"s_and_b32 s96, s30, 1\n"
"v_mfma_f32_16x16x32_bf16 v[124:127], a[156:159], a[36:39], v[124:127]\n"
"buffer_load_dwordx4 a[204:207], v209, s[64:67], s81 offen offset:1024 sc1\n"
"s_add_u32 s93, s93, s96\n"
"v_mfma_f32_16x16x32_bf16 v[92:95], a[156:159], a[32:35], v[92:95]\n"
"s_add_u32 s81, s81, 0x1000\n"
"s_waitcnt lgkmcnt(8)\n"
"v_mfma_f32_16x16x32_bf16 v[64:67], a[128:131], a[48:51], v[64:67]\n"
"ds_read_b64_tr_b16 a[148:149], v222 offset:28672\n"
"s_lshl_b32 s93, s93, 16\n"
"v_mfma_f32_16x16x32_bf16 v[96:99], a[128:131], a[52:55], v[96:99]\n"
"ds_read_b64_tr_b16 a[150:151], v222 offset:30720\n"
"s_add_u32 s97, s93, s39\n"
"v_mfma_f32_16x16x32_bf16 v[128:131], a[128:131], a[56:59], v[128:131]\n"
"buffer_load_dwordx4 a[208:211], v208, s[64:67], s81 offen sc1\n"
"s_add_u32 s96, s97, 0x8000\n"
"v_mfma_f32_16x16x32_bf16 v[160:163], a[128:131], a[60:63], v[160:163]\n"
"s_waitcnt lgkmcnt(8)\n"
"s_add_u32 s98, s82, 0x10000\n"
"v_mfma_f32_16x16x32_bf16 v[164:167], a[132:135], a[60:63], v[164:167]\n"
"ds_read_b64_tr_b16 a[152:153], v223 offset:28672\n"
"v_lshlrev_b32 v8, 3, v63\n"
"v_mfma_f32_16x16x32_bf16 v[132:135], a[132:135], a[56:59], v[132:135]\n"
"ds_read_b64_tr_b16 a[154:155], v223 offset:30720\n"
"v_add_u32 v8, s98, v8\n"
"v_mfma_f32_16x16x32_bf16 v[100:103], a[132:135], a[52:55], v[100:103]\n"
"buffer_load_dwordx4 a[212:215], v208, s[64:67], s81 offen offset:1024 sc1\n"
"v_lshrrev_b32 v56, 1, v63\n"
"v_mfma_f32_16x16x32_bf16 v[68:71], a[132:135], a[48:51], v[68:71]\n"
"s_waitcnt lgkmcnt(8)\n"
"v_xor_b32 v56, v56, v57\n"
"v_mfma_f32_16x16x32_bf16 v[72:75], a[136:139], a[48:51], v[72:75]\n"
"ds_read_b64_tr_b16 a[156:157], v239 offset:28672\n"
"v_and_b32 v57, 1, v63\n"
"v_mfma_f32_16x16x32_bf16 v[104:107], a[136:139], a[52:55], v[104:107]\n"
"ds_read_b64_tr_b16 a[158:159], v239 offset:30720\n"
"v_lshlrev_b32 v57, 3, v57\n"
"v_mfma_f32_16x16x32_bf16 v[136:139], a[136:139], a[56:59], v[136:139]\n"
"v_lshl_add_u32 v57, v62, 7, v57\n"
"v_mfma_f32_16x16x32_bf16 v[168:171], a[136:139], a[60:63], v[168:171]\n"
"s_waitcnt lgkmcnt(8)\n"
"v_add_u32 v57, s98, v57\n"
"v_mfma_f32_16x16x32_bf16 v[172:175], a[140:143], a[60:63], v[172:175]\n"
"ds_read_b64_tr_b16 a[128:129], v221 offset:32768\n"
"v_xor_b32 v58, 0, v56\n"
"v_mfma_f32_16x16x32_bf16 v[140:143], a[140:143], a[56:59], v[140:143]\n"
"ds_read_b64_tr_b16 a[130:131], v221 offset:34816\n"
"v_lshl_add_u32 v0, v58, 4, v57\n"
"v_mfma_f32_16x16x32_bf16 v[108:111], a[140:143], a[52:55], v[108:111]\n"
"buffer_load_dwordx4 a[216:219], v209, s[64:67], s81 offen sc1\n"
"v_xor_b32 v58, 2, v56\n"
"v_mfma_f32_16x16x32_bf16 v[76:79], a[140:143], a[48:51], v[76:79]\n"
"s_waitcnt lgkmcnt(8)\n"
"v_lshl_add_u32 v1, v58, 4, v57\n"
"v_mfma_f32_16x16x32_bf16 v[80:83], a[144:147], a[48:51], v[80:83]\n"
"ds_read_b64_tr_b16 a[132:133], v222 offset:32768\n"
"v_xor_b32 v58, 4, v56\n"
"v_mfma_f32_16x16x32_bf16 v[112:115], a[144:147], a[52:55], v[112:115]\n"
"ds_read_b64_tr_b16 a[134:135], v222 offset:34816\n"
"v_lshl_add_u32 v2, v58, 4, v57\n"
"v_mfma_f32_16x16x32_bf16 v[144:147], a[144:147], a[56:59], v[144:147]\n"
"buffer_load_dwordx4 a[220:223], v209, s[64:67], s81 offen offset:1024 sc1\n"
"v_xor_b32 v58, 6, v56\n"
"v_mfma_f32_16x16x32_bf16 v[176:179], a[144:147], a[60:63], v[176:179]\n"
"s_add_u32 s81, s81, 0x1000\n"
"s_waitcnt lgkmcnt(8)\n"
"v_mfma_f32_16x16x32_bf16 v[180:183], a[148:151], a[60:63], v[180:183]\n"
"ds_read_b64_tr_b16 a[136:137], v223 offset:32768\n"
"v_lshl_add_u32 v3, v58, 4, v57\n"
"v_mfma_f32_16x16x32_bf16 v[148:151], a[148:151], a[56:59], v[148:151]\n"
"ds_read_b64_tr_b16 a[138:139], v223 offset:34816\n"
"s_mov_b32 s72, s50\n"
"v_mfma_f32_16x16x32_bf16 v[116:119], a[148:151], a[52:55], v[116:119]\n"
"buffer_load_dwordx4 a[224:227], v208, s[64:67], s81 offen sc1\n"
"s_and_b32 s73, s51, 0xffff\n"
"v_mfma_f32_16x16x32_bf16 v[84:87], a[148:151], a[48:51], v[84:87]\n"
"s_waitcnt lgkmcnt(8)\n"
"s_mov_b32 s74, 0x400000\n"
"v_mfma_f32_16x16x32_bf16 v[88:91], a[152:155], a[48:51], v[88:91]\n"
"ds_read_b64_tr_b16 a[140:141], v239 offset:32768\n"
"s_mov_b32 s75, 0x20000\n"
"v_mfma_f32_16x16x32_bf16 v[120:123], a[152:155], a[52:55], v[120:123]\n"
"ds_read_b64_tr_b16 a[142:143], v239 offset:34816\n"
"v_lshlrev_b32 v62, 11, v62\n"
"v_mfma_f32_16x16x32_bf16 v[152:155], a[152:155], a[56:59], v[152:155]\n"
"buffer_load_dwordx4 a[228:231], v208, s[64:67], s81 offen offset:1024 sc1\n"
"v_and_b32 v58, 1, v63\n"
"v_mfma_f32_16x16x32_bf16 v[184:187], a[152:155], a[60:63], v[184:187]\n"
"s_waitcnt lgkmcnt(8)\n"
"v_lshl_add_u32 v62, v58, 5, v62\n"
"v_mfma_f32_16x16x32_bf16 v[188:191], a[156:159], a[60:63], v[188:191]\n"
"ds_read_b64_tr_b16 a[144:145], v221 offset:49152\n"
"v_lshrrev_b32 v58, 1, v63\n"
"v_mfma_f32_16x16x32_bf16 v[156:159], a[156:159], a[56:59], v[156:159]\n"
"ds_read_b64_tr_b16 a[146:147], v221 offset:51200\n"
"v_lshl_add_u32 v62, v58, 4, v62\n"
"v_mfma_f32_16x16x32_bf16 v[124:127], a[156:159], a[52:55], v[124:127]\n"
"buffer_load_dwordx4 a[232:235], v209, s[64:67], s81 offen sc1\n"
"v_mov_b32 v4, v212\n"
"v_mfma_f32_16x16x32_bf16 v[92:95], a[156:159], a[48:51], v[92:95]\n"
"v_mov_b32 v5, v213\n"
"s_barrier\n"
"s_waitcnt lgkmcnt(8)\n"
"v_mfma_f32_16x16x32_bf16 v[64:67], a[128:131], a[64:67], v[64:67]\n"
"ds_read_b64_tr_b16 a[148:149], v222 offset:49152\n"
"v_mov_b32 v6, v212\n"
"v_mfma_f32_16x16x32_bf16 v[96:99], a[128:131], a[68:71], v[96:99]\n"
"ds_read_b64_tr_b16 a[150:151], v222 offset:51200\n"
"v_mov_b32 v7, v213\n"
"v_mfma_f32_16x16x32_bf16 v[128:131], a[128:131], a[72:75], v[128:131]\n"
"s_add_u32 m0, s98, 0\n"
"s_add_u32 s93, s97, 0\n"
"buffer_load_dwordx4 v59, s[68:71], s93 offen lds\n"
"v_mfma_f32_16x16x32_bf16 v[160:163], a[128:131], a[76:79], v[160:163]\n"
"s_waitcnt lgkmcnt(8)\n"
"v_mfma_f32_16x16x32_bf16 v[164:167], a[132:135], a[76:79], v[164:167]\n"
"ds_read_b64_tr_b16 a[152:153], v223 offset:49152\n"
"v_mfma_f32_16x16x32_bf16 v[132:135], a[132:135], a[72:75], v[132:135]\n"
"ds_read_b64_tr_b16 a[154:155], v223 offset:51200\n"
"v_mfma_f32_16x16x32_bf16 v[100:103], a[132:135], a[68:71], v[100:103]\n"
"buffer_load_dwordx4 a[236:239], v209, s[64:67], s81 offen offset:1024 sc1\n"
"v_mfma_f32_16x16x32_bf16 v[68:71], a[132:135], a[64:67], v[68:71]\n"
"s_add_u32 s81, s81, 0x1000\n"
"s_waitcnt lgkmcnt(8)\n"
"v_mfma_f32_16x16x32_bf16 v[72:75], a[136:139], a[64:67], v[72:75]\n"
".Lpad10_%=:\n"
"s_cbranch_execz .Lpad14_%=\n"
"ds_read_b64_tr_b16 a[156:157], v239 offset:49152\n"
"v_mfma_f32_16x16x32_bf16 v[104:107], a[136:139], a[68:71], v[104:107]\n"
"ds_read_b64_tr_b16 a[158:159], v239 offset:51200\n"
"v_mfma_f32_16x16x32_bf16 v[136:139], a[136:139], a[72:75], v[136:139]\n"
"s_add_u32 m0, s98, 0x400\n"
"s_add_u32 s93, s97, 0x4000\n"
"buffer_load_dwordx4 v59, s[68:71], s93 offen lds\n"
"v_mfma_f32_16x16x32_bf16 v[168:171], a[136:139], a[76:79], v[168:171]\n"
"s_waitcnt lgkmcnt(8)\n"
"v_mfma_f32_16x16x32_bf16 v[172:175], a[140:143], a[76:79], v[172:175]\n"
"ds_read_b64_tr_b16 a[128:129], v221 offset:36864\n"
"v_mfma_f32_16x16x32_bf16 v[140:143], a[140:143], a[72:75], v[140:143]\n"
"ds_read_b64_tr_b16 a[130:131], v221 offset:38912\n"
"v_mfma_f32_16x16x32_bf16 v[108:111], a[140:143], a[68:71], v[108:111]\n"
"buffer_load_dwordx4 a[240:243], v208, s[64:67], s81 offen sc1\n"
"v_mfma_f32_16x16x32_bf16 v[76:79], a[140:143], a[64:67], v[76:79]\n"
"s_waitcnt lgkmcnt(8)\n"
"v_mfma_f32_16x16x32_bf16 v[80:83], a[144:147], a[64:67], v[80:83]\n"
"ds_read_b64_tr_b16 a[132:133], v222 offset:36864\n"
"v_mfma_f32_16x16x32_bf16 v[112:115], a[144:147], a[68:71], v[112:115]\n"
"ds_read_b64_tr_b16 a[134:135], v222 offset:38912\n"
"v_mfma_f32_16x16x32_bf16 v[144:147], a[144:147], a[72:75], v[144:147]\n"
"s_add_u32 m0, s98, 0x800\n"
"s_add_u32 s93, s97, 0x8000\n"
"buffer_load_dwordx4 v59, s[68:71], s93 offen lds\n"
"v_mfma_f32_16x16x32_bf16 v[176:179], a[144:147], a[76:79], v[176:179]\n"
"s_waitcnt lgkmcnt(8)\n"
"v_mfma_f32_16x16x32_bf16 v[180:183], a[148:151], a[76:79], v[180:183]\n"
"ds_read_b64_tr_b16 a[136:137], v223 offset:36864\n"
"v_mfma_f32_16x16x32_bf16 v[148:151], a[148:151], a[72:75], v[148:151]\n"
"ds_read_b64_tr_b16 a[138:139], v223 offset:38912\n"
"v_mfma_f32_16x16x32_bf16 v[116:119], a[148:151], a[68:71], v[116:119]\n"
"buffer_load_dwordx4 a[244:247], v208, s[64:67], s81 offen offset:1024 sc1\n"
"v_mfma_f32_16x16x32_bf16 v[84:87], a[148:151], a[64:67], v[84:87]\n"
"s_waitcnt lgkmcnt(8)\n"
"v_mfma_f32_16x16x32_bf16 v[88:91], a[152:155], a[64:67], v[88:91]\n"
"ds_read_b64_tr_b16 a[140:141], v239 offset:36864\n"
"v_mfma_f32_16x16x32_bf16 v[120:123], a[152:155], a[68:71], v[120:123]\n"
"ds_read_b64_tr_b16 a[142:143], v239 offset:38912\n"
"v_mfma_f32_16x16x32_bf16 v[152:155], a[152:155], a[72:75], v[152:155]\n"
"v_mfma_f32_16x16x32_bf16 v[184:187], a[152:155], a[76:79], v[184:187]\n"
"s_waitcnt lgkmcnt(8)\n"
"v_mfma_f32_16x16x32_bf16 v[188:191], a[156:159], a[76:79], v[188:191]\n"
"ds_read_b64_tr_b16 a[144:145], v221 offset:53248\n"
"v_mfma_f32_16x16x32_bf16 v[156:159], a[156:159], a[72:75], v[156:159]\n"
"ds_read_b64_tr_b16 a[146:147], v221 offset:55296\n"
"v_mfma_f32_16x16x32_bf16 v[124:127], a[156:159], a[68:71], v[124:127]\n"
"s_add_u32 m0, s98, 0xc00\n"
"s_add_u32 s93, s97, 0xc000\n"
"buffer_load_dwordx4 v59, s[68:71], s93 offen lds\n"
"v_mfma_f32_16x16x32_bf16 v[92:95], a[156:159], a[64:67], v[92:95]\n"
"s_waitcnt lgkmcnt(8)\n"
"v_mfma_f32_16x16x32_bf16 v[64:67], a[128:131], a[80:83], v[64:67]\n"
"ds_read_b64_tr_b16 a[148:149], v222 offset:53248\n"
"v_mfma_f32_16x16x32_bf16 v[96:99], a[128:131], a[84:87], v[96:99]\n"
"ds_read_b64_tr_b16 a[150:151], v222 offset:55296\n"
"v_mfma_f32_16x16x32_bf16 v[128:131], a[128:131], a[88:91], v[128:131]\n"
"buffer_load_dwordx4 a[248:251], v209, s[64:67], s81 offen sc1\n"
"v_mfma_f32_16x16x32_bf16 v[160:163], a[128:131], a[92:95], v[160:163]\n"
"s_waitcnt lgkmcnt(8)\n"
"v_mfma_f32_16x16x32_bf16 v[164:167], a[132:135], a[92:95], v[164:167]\n"
"ds_read_b64_tr_b16 a[152:153], v223 offset:53248\n"
"v_mfma_f32_16x16x32_bf16 v[132:135], a[132:135], a[88:91], v[132:135]\n"
"ds_read_b64_tr_b16 a[154:155], v223 offset:55296\n"
"v_mfma_f32_16x16x32_bf16 v[100:103], a[132:135], a[84:87], v[100:103]\n"
"s_add_u32 m0, s98, 0x4000\n"
"s_nop 0\n"
"global_load_lds_dwordx4 v216, s[48:49]\n"
"v_mfma_f32_16x16x32_bf16 v[68:71], a[132:135], a[80:83], v[68:71]\n"
"s_waitcnt lgkmcnt(8)\n"
"v_mfma_f32_16x16x32_bf16 v[72:75], a[136:139], a[80:83], v[72:75]\n"
"ds_read_b64_tr_b16 a[156:157], v239 offset:53248\n"
"v_mfma_f32_16x16x32_bf16 v[104:107], a[136:139], a[84:87], v[104:107]\n"
"ds_read_b64_tr_b16 a[158:159], v239 offset:55296\n"
"v_mfma_f32_16x16x32_bf16 v[136:139], a[136:139], a[88:91], v[136:139]\n"
"buffer_load_dwordx4 a[252:255], v209, s[64:67], s81 offen offset:1024 sc1\n"
"v_mfma_f32_16x16x32_bf16 v[168:171], a[136:139], a[92:95], v[168:171]\n"
"s_add_u32 s81, s81, 0x1000\n"
"s_waitcnt lgkmcnt(8)\n"
"v_mfma_f32_16x16x32_bf16 v[172:175], a[140:143], a[92:95], v[172:175]\n"
"ds_read_b64_tr_b16 a[128:129], v221 offset:40960\n"
"v_mfma_f32_16x16x32_bf16 v[140:143], a[140:143], a[88:91], v[140:143]\n"
"ds_read_b64_tr_b16 a[130:131], v221 offset:43008\n"
"s_lshl_b32 s91, s78, 13\n"
"v_mfma_f32_16x16x32_bf16 v[108:111], a[140:143], a[84:87], v[108:111]\n"
"buffer_load_dwordx4 v[16:19], v208, s[64:67], s81 offen sc1\n"
"s_add_u32 s91, s91, 0x2080000\n"
"v_mfma_f32_16x16x32_bf16 v[76:79], a[140:143], a[80:83], v[76:79]\n"
"s_waitcnt lgkmcnt(8)\n"
"s_cmp_eq_u32 s54, 0\n"
"s_cselect_b32 s91, s91, 0x40000000\n"
"v_mfma_f32_16x16x32_bf16 v[80:83], a[144:147], a[80:83], v[80:83]\n"
"ds_read_b64_tr_b16 a[132:133], v222 offset:40960\n"
"v_lshlrev_b32 v238, 7, v236\n"
"v_mfma_f32_16x16x32_bf16 v[112:115], a[144:147], a[84:87], v[112:115]\n"
"ds_read_b64_tr_b16 a[134:135], v222 offset:43008\n"
"v_mfma_f32_16x16x32_bf16 v[144:147], a[144:147], a[88:91], v[144:147]\n"
"buffer_load_dwordx2 v[234:235], v238, s[64:67], s91 offen sc1\n"
"v_mfma_f32_16x16x32_bf16 v[176:179], a[144:147], a[92:95], v[176:179]\n"
"s_waitcnt lgkmcnt(8)\n"
"v_mfma_f32_16x16x32_bf16 v[180:183], a[148:151], a[92:95], v[180:183]\n"
"ds_read_b64_tr_b16 a[136:137], v223 offset:40960\n"
"v_mfma_f32_16x16x32_bf16 v[148:151], a[148:151], a[88:91], v[148:151]\n"
"ds_read_b64_tr_b16 a[138:139], v223 offset:43008\n"
"v_mfma_f32_16x16x32_bf16 v[116:119], a[148:151], a[84:87], v[116:119]\n"
"buffer_load_dwordx4 v[20:23], v208, s[64:67], s81 offen offset:1024 sc1\n"
"v_mfma_f32_16x16x32_bf16 v[84:87], a[148:151], a[80:83], v[84:87]\n"
"s_waitcnt lgkmcnt(8)\n"
"v_mfma_f32_16x16x32_bf16 v[88:91], a[152:155], a[80:83], v[88:91]\n"
"ds_read_b64_tr_b16 a[140:141], v239 offset:40960\n"
"v_mfma_f32_16x16x32_bf16 v[120:123], a[152:155], a[84:87], v[120:123]\n"
"ds_read_b64_tr_b16 a[142:143], v239 offset:43008\n"
"v_mfma_f32_16x16x32_bf16 v[152:155], a[152:155], a[88:91], v[152:155]\n"
"buffer_load_dwordx4 v[24:27], v209, s[64:67], s81 offen sc1\n"
"v_mfma_f32_16x16x32_bf16 v[184:187], a[152:155], a[92:95], v[184:187]\n"
"s_waitcnt lgkmcnt(8)\n"
"v_mfma_f32_16x16x32_bf16 v[188:191], a[156:159], a[92:95], v[188:191]\n"
"ds_read_b64_tr_b16 a[144:145], v221 offset:57344\n"
"v_mfma_f32_16x16x32_bf16 v[156:159], a[156:159], a[88:91], v[156:159]\n"
"ds_read_b64_tr_b16 a[146:147], v221 offset:59392\n"
"v_mfma_f32_16x16x32_bf16 v[124:127], a[156:159], a[84:87], v[124:127]\n"
"buffer_load_dwordx4 v[28:31], v209, s[64:67], s81 offen offset:1024 sc1\n"
"v_mfma_f32_16x16x32_bf16 v[92:95], a[156:159], a[80:83], v[92:95]\n"
"s_add_u32 s81, s81, 0x1000\n"
"s_waitcnt vmcnt(39) lgkmcnt(8)\n"
"v_mfma_f32_16x16x32_bf16 v[64:67], a[128:131], a[96:99], v[64:67]\n"
"ds_read_b64_tr_b16 a[148:149], v222 offset:57344\n"
"v_mfma_f32_16x16x32_bf16 v[96:99], a[128:131], a[100:103], v[96:99]\n"
"ds_read_b64_tr_b16 a[150:151], v222 offset:59392\n"
"v_mfma_f32_16x16x32_bf16 v[128:131], a[128:131], a[104:107], v[128:131]\n"
"buffer_load_dwordx4 v[32:35], v208, s[64:67], s81 offen sc1\n"
"v_mfma_f32_16x16x32_bf16 v[160:163], a[128:131], a[108:111], v[160:163]\n"
"s_waitcnt lgkmcnt(8)\n"
"v_mfma_f32_16x16x32_bf16 v[164:167], a[132:135], a[108:111], v[164:167]\n"
"ds_read_b64_tr_b16 a[152:153], v223 offset:57344\n"
"v_mfma_f32_16x16x32_bf16 v[132:135], a[132:135], a[104:107], v[132:135]\n"
"ds_read_b64_tr_b16 a[154:155], v223 offset:59392\n"
"v_mfma_f32_16x16x32_bf16 v[100:103], a[132:135], a[100:103], v[100:103]\n"
"buffer_load_dwordx4 v[36:39], v208, s[64:67], s81 offen offset:1024 sc1\n"
"v_mfma_f32_16x16x32_bf16 v[68:71], a[132:135], a[96:99], v[68:71]\n"
"s_waitcnt lgkmcnt(8)\n"
"v_mfma_f32_16x16x32_bf16 v[72:75], a[136:139], a[96:99], v[72:75]\n"
"ds_read_b64_tr_b16 a[156:157], v239 offset:57344\n"
"v_mfma_f32_16x16x32_bf16 v[104:107], a[136:139], a[100:103], v[104:107]\n"
"ds_read_b64_tr_b16 a[158:159], v239 offset:59392\n"
"v_mfma_f32_16x16x32_bf16 v[136:139], a[136:139], a[104:107], v[136:139]\n"
"v_mfma_f32_16x16x32_bf16 v[168:171], a[136:139], a[108:111], v[168:171]\n"
"s_waitcnt lgkmcnt(8)\n"
"v_mfma_f32_16x16x32_bf16 v[172:175], a[140:143], a[108:111], v[172:175]\n"
"ds_read_b64_tr_b16 a[128:129], v221 offset:45056\n"
"v_mfma_f32_16x16x32_bf16 v[140:143], a[140:143], a[104:107], v[140:143]\n"
"ds_read_b64_tr_b16 a[130:131], v221 offset:47104\n"
"v_mfma_f32_16x16x32_bf16 v[108:111], a[140:143], a[100:103], v[108:111]\n"
"buffer_load_dwordx4 v[48:51], v209, s[64:67], s81 offen sc1\n"
"v_mfma_f32_16x16x32_bf16 v[76:79], a[140:143], a[96:99], v[76:79]\n"
"s_waitcnt lgkmcnt(8)\n"
"v_mfma_f32_16x16x32_bf16 v[80:83], a[144:147], a[96:99], v[80:83]\n"
"ds_read_b64_tr_b16 a[132:133], v222 offset:45056\n"
"v_mfma_f32_16x16x32_bf16 v[112:115], a[144:147], a[100:103], v[112:115]\n"
"ds_read_b64_tr_b16 a[134:135], v222 offset:47104\n"
"v_mfma_f32_16x16x32_bf16 v[144:147], a[144:147], a[104:107], v[144:147]\n"
"buffer_load_dwordx4 v[52:55], v209, s[64:67], s81 offen offset:1024 sc1\n"
"v_mfma_f32_16x16x32_bf16 v[176:179], a[144:147], a[108:111], v[176:179]\n"
"s_add_u32 s81, s81, 0x1000\n"
"s_waitcnt lgkmcnt(8)\n"
"v_mfma_f32_16x16x32_bf16 v[180:183], a[148:151], a[108:111], v[180:183]\n"
"ds_read_b64_tr_b16 a[136:137], v223 offset:45056\n"
"v_mfma_f32_16x16x32_bf16 v[148:151], a[148:151], a[104:107], v[148:151]\n"
"ds_read_b64_tr_b16 a[138:139], v223 offset:47104\n"
"v_mfma_f32_16x16x32_bf16 v[116:119], a[148:151], a[100:103], v[116:119]\n"
"buffer_load_dwordx4 a[0:3], v208, s[64:67], s81 offen sc1\n"
"v_mfma_f32_16x16x32_bf16 v[84:87], a[148:151], a[96:99], v[84:87]\n"
"s_waitcnt lgkmcnt(8)\n"
"v_mfma_f32_16x16x32_bf16 v[88:91], a[152:155], a[96:99], v[88:91]\n"
"ds_read_b64_tr_b16 a[140:141], v239 offset:45056\n"
"v_mfma_f32_16x16x32_bf16 v[120:123], a[152:155], a[100:103], v[120:123]\n"
"ds_read_b64_tr_b16 a[142:143], v239 offset:47104\n"
"v_mfma_f32_16x16x32_bf16 v[152:155], a[152:155], a[104:107], v[152:155]\n"
"buffer_load_dwordx4 a[4:7], v208, s[64:67], s81 offen offset:1024 sc1\n"
"v_mfma_f32_16x16x32_bf16 v[184:187], a[152:155], a[108:111], v[184:187]\n"
"s_waitcnt lgkmcnt(8)\n"
"v_mfma_f32_16x16x32_bf16 v[188:191], a[156:159], a[108:111], v[188:191]\n"
"ds_read_b64_tr_b16 a[144:145], v221 offset:61440\n"
"v_mfma_f32_16x16x32_bf16 v[156:159], a[156:159], a[104:107], v[156:159]\n"
"ds_read_b64_tr_b16 a[146:147], v221 offset:63488\n"
"v_mfma_f32_16x16x32_bf16 v[124:127], a[156:159], a[100:103], v[124:127]\n"
"buffer_load_dwordx4 a[8:11], v209, s[64:67], s81 offen sc1\n"
"v_mfma_f32_16x16x32_bf16 v[92:95], a[156:159], a[96:99], v[92:95]\n"
"s_waitcnt vmcnt(42) lgkmcnt(8)\n"
"v_mfma_f32_16x16x32_bf16 v[64:67], a[128:131], a[112:115], v[64:67]\n"
"ds_read_b64_tr_b16 a[148:149], v222 offset:61440\n"
"v_mfma_f32_16x16x32_bf16 v[96:99], a[128:131], a[116:119], v[96:99]\n"
"ds_read_b64_tr_b16 a[150:151], v222 offset:63488\n"
"v_mfma_f32_16x16x32_bf16 v[128:131], a[128:131], a[120:123], v[128:131]\n"
"v_mfma_f32_16x16x32_bf16 v[160:163], a[128:131], a[124:127], v[160:163]\n"
"s_waitcnt lgkmcnt(8)\n"
"v_mfma_f32_16x16x32_bf16 v[164:167], a[132:135], a[124:127], v[164:167]\n"
"ds_read_b64_tr_b16 a[152:153], v223 offset:61440\n"
"v_mfma_f32_16x16x32_bf16 v[132:135], a[132:135], a[120:123], v[132:135]\n"
"ds_read_b64_tr_b16 a[154:155], v223 offset:63488\n"
"v_mfma_f32_16x16x32_bf16 v[100:103], a[132:135], a[116:119], v[100:103]\n"
"buffer_load_dwordx4 a[12:15], v209, s[64:67], s81 offen offset:1024 sc1\n"
"v_mfma_f32_16x16x32_bf16 v[68:71], a[132:135], a[112:115], v[68:71]\n"
"s_add_u32 s81, s81, 0x1000\n"
"s_waitcnt lgkmcnt(8)\n"
"v_mfma_f32_16x16x32_bf16 v[72:75], a[136:139], a[112:115], v[72:75]\n"
"ds_read_b64_tr_b16 a[156:157], v239 offset:61440\n"
"v_mfma_f32_16x16x32_bf16 v[104:107], a[136:139], a[116:119], v[104:107]\n"
"ds_read_b64_tr_b16 a[158:159], v239 offset:63488\n"
"v_mfma_f32_16x16x32_bf16 v[136:139], a[136:139], a[120:123], v[136:139]\n"
"buffer_load_dwordx4 a[16:19], v208, s[64:67], s81 offen sc1\n"
"v_mfma_f32_16x16x32_bf16 v[168:171], a[136:139], a[124:127], v[168:171]\n"
"s_waitcnt lgkmcnt(8)\n"
"v_mfma_f32_16x16x32_bf16 v[172:175], a[140:143], a[124:127], v[172:175]\n"
"ds_read_b64_tr_b16 a[128:129], v44\n"
"v_mfma_f32_16x16x32_bf16 v[140:143], a[140:143], a[120:123], v[140:143]\n"
"ds_read_b64_tr_b16 a[130:131], v44 offset:2048\n"
"v_mfma_f32_16x16x32_bf16 v[108:111], a[140:143], a[116:119], v[108:111]\n"
"buffer_load_dwordx4 a[20:23], v208, s[64:67], s81 offen offset:1024 sc1\n"
"v_mfma_f32_16x16x32_bf16 v[76:79], a[140:143], a[112:115], v[76:79]\n"
"s_waitcnt lgkmcnt(8)\n"
"v_mfma_f32_16x16x32_bf16 v[80:83], a[144:147], a[112:115], v[80:83]\n"
"ds_read_b64_tr_b16 a[132:133], v45\n"
"v_mfma_f32_16x16x32_bf16 v[112:115], a[144:147], a[116:119], v[112:115]\n"
"ds_read_b64_tr_b16 a[134:135], v45 offset:2048\n"
"v_mfma_f32_16x16x32_bf16 v[144:147], a[144:147], a[120:123], v[144:147]\n"
"buffer_load_dwordx4 a[24:27], v209, s[64:67], s81 offen sc1\n"
"v_mfma_f32_16x16x32_bf16 v[176:179], a[144:147], a[124:127], v[176:179]\n"
"s_waitcnt lgkmcnt(8)\n"
"s_waitcnt vmcnt(17)\n"
"ds_read_b64 v[192:193], v0\n"
"v_mfma_f32_16x16x32_bf16 v[180:183], a[148:151], a[124:127], v[180:183]\n"
"ds_read_b64_tr_b16 a[136:137], v46\n"
"v_mfma_f32_16x16x32_bf16 v[148:151], a[148:151], a[120:123], v[148:151]\n"
"ds_read_b64_tr_b16 a[138:139], v46 offset:2048\n"
"ds_read_b64 v[196:197], v1\n"
"v_mfma_f32_16x16x32_bf16 v[116:119], a[148:151], a[116:119], v[116:119]\n"
"buffer_load_dwordx4 a[28:31], v209, s[64:67], s81 offen offset:1024 sc1\n"
"v_mfma_f32_16x16x32_bf16 v[84:87], a[148:151], a[112:115], v[84:87]\n"
"s_add_u32 s81, s81, 0x1000\n"
"s_waitcnt lgkmcnt(10)\n"
"v_mfma_f32_16x16x32_bf16 v[88:91], a[152:155], a[112:115], v[88:91]\n"
"ds_read_b64_tr_b16 a[140:141], v47\n"
"ds_read_b64 v[200:201], v2\n"
"v_mfma_f32_16x16x32_bf16 v[120:123], a[152:155], a[116:119], v[120:123]\n"
"ds_read_b64_tr_b16 a[142:143], v47 offset:2048\n"
"ds_read_b64 v[204:205], v3\n"
"v_mfma_f32_16x16x32_bf16 v[152:155], a[152:155], a[120:123], v[152:155]\n"
"v_mfma_f32_16x16x32_bf16 v[184:187], a[152:155], a[124:127], v[184:187]\n"
"s_waitcnt lgkmcnt(12)\n"
"ds_read_b64 v[240:241], v0 offset:2048\n"
"v_mfma_f32_16x16x32_bf16 v[188:191], a[156:159], a[124:127], v[188:191]\n"
"ds_read_b64_tr_b16 a[144:145], v44 offset:16384\n"
"v_mfma_f32_16x16x32_bf16 v[156:159], a[156:159], a[120:123], v[156:159]\n"
"ds_read_b64_tr_b16 a[146:147], v44 offset:18432\n"
"s_waitcnt lgkmcnt(9)\n"
"ds_read_b64 v[244:245], v1 offset:2048\n"
"v_mfma_f32_16x16x32_bf16 v[124:127], a[156:159], a[116:119], v[124:127]\n"
"buffer_load_dwordx4 a[32:35], v208, s[64:67], s81 offen sc1\n"
"v_mfma_f32_16x16x32_bf16 v[92:95], a[156:159], a[112:115], v[92:95]\n"
"ds_read_b64 v[248:249], v2 offset:2048\n"
"s_barrier\n"
"v_mfma_f32_16x16x32_bf16 v[64:67], a[128:131], a[160:163], v[64:67]\n"
"ds_read_b64_tr_b16 a[148:149], v45 offset:16384\n"
"v_mfma_f32_16x16x32_bf16 v[96:99], a[128:131], a[164:167], v[96:99]\n"
"ds_read_b64_tr_b16 a[150:151], v45 offset:18432\n"
"s_add_u32 m0, s82, 0x18000\n"
"v_mfma_f32_16x16x32_bf16 v[128:131], a[128:131], a[168:171], v[128:131]\n"
"buffer_load_dwordx4 v217, s[60:63], s36 offen lds\n"
"ds_read_b64 v[252:253], v3 offset:2048\n"
"v_mfma_f32_16x16x32_bf16 v[160:163], a[128:131], a[172:175], v[160:163]\n"
"ds_read_b64 v[194:195], v8 offset:16384\n"
"v_mfma_f32_16x16x32_bf16 v[164:167], a[132:135], a[172:175], v[164:167]\n"
"s_waitcnt lgkmcnt(9)\n"
"ds_read_b64_tr_b16 a[152:153], v46 offset:16384\n"
"v_mfma_f32_16x16x32_bf16 v[132:135], a[132:135], a[168:171], v[132:135]\n"
"ds_read_b64_tr_b16 a[154:155], v46 offset:18432\n"
"s_add_u32 m0, s82, 0x18000\n"
"v_mfma_f32_16x16x32_bf16 v[100:103], a[132:135], a[164:167], v[100:103]\n"
"buffer_load_dwordx4 v218, s[60:63], s36 offen offset:1024 lds\n"
"ds_read_b64 v[198:199], v8 offset:16416\n"
"v_mfma_f32_16x16x32_bf16 v[68:71], a[132:135], a[160:163], v[68:71]\n"
"ds_read_b64 v[202:203], v8 offset:16448\n"
"v_mfma_f32_16x16x32_bf16 v[72:75], a[136:139], a[160:163], v[72:75]\n"
"ds_read_b64_tr_b16 a[156:157], v47 offset:16384\n"
"v_mfma_f32_16x16x32_bf16 v[104:107], a[136:139], a[164:167], v[104:107]\n"
"ds_read_b64_tr_b16 a[158:159], v47 offset:18432\n"
"s_waitcnt lgkmcnt(9)\n"
"ds_read_b64 v[206:207], v8 offset:16480\n"
"v_mfma_f32_16x16x32_bf16 v[136:139], a[136:139], a[168:171], v[136:139]\n"
"v_mfma_f32_16x16x32_bf16 v[168:171], a[136:139], a[172:175], v[168:171]\n"
"ds_read_b64 v[242:243], v8 offset:16384\n"
"v_mfma_f32_16x16x32_bf16 v[172:175], a[140:143], a[172:175], v[172:175]\n"
"ds_read_b64_tr_b16 a[128:129], v44 offset:4096\n"
"v_mfma_f32_16x16x32_bf16 v[140:143], a[140:143], a[168:171], v[140:143]\n"
"ds_read_b64_tr_b16 a[130:131], v44 offset:6144\n"
"s_add_u32 m0, s82, 0x18000\n"
"v_mfma_f32_16x16x32_bf16 v[108:111], a[140:143], a[164:167], v[108:111]\n"
"buffer_load_dwordx4 v219, s[60:63], s36 offen offset:2048 lds\n"
"ds_read_b64 v[246:247], v8 offset:16416\n"
"v_mfma_f32_16x16x32_bf16 v[76:79], a[140:143], a[160:163], v[76:79]\n"
"ds_read_b64 v[250:251], v8 offset:16448\n"
"v_mfma_f32_16x16x32_bf16 v[80:83], a[144:147], a[160:163], v[80:83]\n"
"s_waitcnt lgkmcnt(9)\n"
"ds_read_b64_tr_b16 a[132:133], v45 offset:4096\n"
"v_mfma_f32_16x16x32_bf16 v[112:115], a[144:147], a[164:167], v[112:115]\n"
"ds_read_b64_tr_b16 a[134:135], v45 offset:6144\n"
"s_add_u32 m0, s82, 0x18000\n"
"v_mfma_f32_16x16x32_bf16 v[144:147], a[144:147], a[168:171], v[144:147]\n"
"buffer_load_dwordx4 v220, s[60:63], s36 offen offset:3072 lds\n"
"ds_read_b64 v[254:255], v8 offset:16480\n"
"v_mfma_f32_16x16x32_bf16 v[176:179], a[144:147], a[172:175], v[176:179]\n"
"s_add_u32 s36, s36, 0x40000\n"
"s_waitcnt vmcnt(20)\n"
"s_cmp_lg_u32 s54, 0\n"
"s_cbranch_scc1 .Lok4_%=\n"
".Lchk4_%=:\n"
"v_cmp_ne_u64 vcc, s[84:85], v[234:235]\n"
"s_nop 1\n"
"s_cmp_eq_u64 vcc, 0\n"
"s_cbranch_scc1 .Lok4_%=\n"
"s_cmp_eq_u32 s94, 0\n"
"s_cbranch_scc1 .Lok4_%=\n"
"s_sub_u32 s94, s94, 1\n"
"s_sleep 1\n"
"buffer_load_dwordx2 v[234:235], v238, s[64:67], s91 offen sc1\n"
"s_waitcnt vmcnt(0)\n"
"s_branch .Lchk4_%=\n"
".Lok4_%=:\n"
"v_mfma_f32_16x16x32_bf16 v[180:183], a[148:151], a[172:175], v[180:183]\n"
"ds_read_b64_tr_b16 a[136:137], v46 offset:4096\n"
"v_mfma_f32_16x16x32_bf16 v[148:151], a[148:151], a[168:171], v[148:151]\n"
"ds_read_b64_tr_b16 a[138:139], v46 offset:6144\n"
"v_mfma_f32_16x16x32_bf16 v[116:119], a[148:151], a[164:167], v[116:119]\n"
"buffer_load_dwordx4 a[36:39], v208, s[64:67], s81 offen offset:1024 sc1\n"
"v_mfma_f32_16x16x32_bf16 v[84:87], a[148:151], a[160:163], v[84:87]\n"
"v_mfma_f32_16x16x32_bf16 v[88:91], a[152:155], a[160:163], v[88:91]\n"
"ds_read_b64_tr_b16 a[140:141], v47 offset:4096\n"
"v_mfma_f32_16x16x32_bf16 v[120:123], a[152:155], a[164:167], v[120:123]\n"
"s_waitcnt lgkmcnt(9)\n"
"ds_read_b64_tr_b16 a[142:143], v47 offset:6144\n"
"v_mfma_f32_16x16x32_bf16 v[152:155], a[152:155], a[168:171], v[152:155]\n"
"buffer_load_dwordx4 a[40:43], v209, s[64:67], s81 offen sc1\n"
"v_mfma_f32_16x16x32_bf16 v[184:187], a[152:155], a[172:175], v[184:187]\n"
"v_mfma_f32_16x16x32_bf16 v[188:191], a[156:159], a[172:175], v[188:191]\n"
"ds_read_b64_tr_b16 a[144:145], v44 offset:20480\n"
"s_waitcnt lgkmcnt(5)\n"
"s_nop 0\n"
"v_mfma_f32_16x16x32_bf16 v[156:159], a[156:159], a[168:171], v[156:159]\n"
"ds_read_b64_tr_b16 a[146:147], v44 offset:22528\n"
"v_mfma_f32_16x16x32_bf16 v[64:67], v[4:7], v[192:195], v[64:67]\n"
"v_mfma_f32_16x16x32_bf16 v[124:127], a[156:159], a[164:167], v[124:127]\n"
"buffer_load_dwordx4 a[44:47], v209, s[64:67], s81 offen offset:1024 sc1\n"
"v_mfma_f32_16x16x32_bf16 v[68:71], v[4:7], v[196:199], v[68:71]\n"
"v_mfma_f32_16x16x32_bf16 v[92:95], a[156:159], a[160:163], v[92:95]\n"
"s_add_u32 s81, s81, 0x1000\n"
"v_mfma_f32_16x16x32_bf16 v[72:75], v[4:7], v[200:203], v[72:75]\n"
"v_mfma_f32_16x16x32_bf16 v[64:67], a[128:131], a[176:179], v[64:67]\n"
"ds_read_b64_tr_b16 a[148:149], v45 offset:20480\n"
"v_mfma_f32_16x16x32_bf16 v[76:79], v[4:7], v[204:207], v[76:79]\n"
"v_mfma_f32_16x16x32_bf16 v[96:99], a[128:131], a[180:183], v[96:99]\n"
"ds_read_b64_tr_b16 a[150:151], v45 offset:22528\n"
"v_mfma_f32_16x16x32_bf16 v[96:99], v[4:7], v[240:243], v[96:99]\n"
"v_mfma_f32_16x16x32_bf16 v[128:131], a[128:131], a[184:187], v[128:131]\n"
"buffer_load_dwordx4 a[48:51], v208, s[64:67], s81 offen sc1\n"
"v_mfma_f32_16x16x32_bf16 v[100:103], v[4:7], v[244:247], v[100:103]\n"
"v_mfma_f32_16x16x32_bf16 v[160:163], a[128:131], a[188:191], v[160:163]\n"
"v_mfma_f32_16x16x32_bf16 v[104:107], v[4:7], v[248:251], v[104:107]\n"
"v_mfma_f32_16x16x32_bf16 v[164:167], a[132:135], a[188:191], v[164:167]\n"
"ds_read_b64_tr_b16 a[152:153], v46 offset:20480\n"
"v_mfma_f32_16x16x32_bf16 v[108:111], v[4:7], v[252:255], v[108:111]\n"
"v_mfma_f32_16x16x32_bf16 v[132:135], a[132:135], a[184:187], v[132:135]\n"
"ds_read_b64_tr_b16 a[154:155], v46 offset:22528\n"
"v_mfma_f32_16x16x32_bf16 v[100:103], a[132:135], a[180:183], v[100:103]\n"
"v_mfma_f32_16x16x32_bf16 v[68:71], a[132:135], a[176:179], v[68:71]\n"
"s_waitcnt lgkmcnt(8)\n"
"v_mfma_f32_16x16x32_bf16 v[72:75], a[136:139], a[176:179], v[72:75]\n"
"ds_read_b64_tr_b16 a[156:157], v47 offset:20480\n"
"v_mfma_f32_16x16x32_bf16 v[104:107], a[136:139], a[180:183], v[104:107]\n"
"ds_read_b64_tr_b16 a[158:159], v47 offset:22528\n"
"v_mfma_f32_16x16x32_bf16 v[136:139], a[136:139], a[184:187], v[136:139]\n"
"buffer_load_dwordx4 a[52:55], v208, s[64:67], s81 offen offset:1024 sc1\n"
"v_mfma_f32_16x16x32_bf16 v[168:171], a[136:139], a[188:191], v[168:171]\n"
"s_waitcnt lgkmcnt(8)\n"
"v_mfma_f32_16x16x32_bf16 v[172:175], a[140:143], a[188:191], v[172:175]\n"
"ds_read_b64_tr_b16 a[128:129], v44 offset:8192\n"
"v_mfma_f32_16x16x32_bf16 v[140:143], a[140:143], a[184:187], v[140:143]\n"
"ds_read_b64_tr_b16 a[130:131], v44 offset:10240\n"
"v_mfma_f32_16x16x32_bf16 v[108:111], a[140:143], a[180:183], v[108:111]\n"
"buffer_load_dwordx4 a[56:59], v209, s[64:67], s81 offen sc1\n"
"v_mfma_f32_16x16x32_bf16 v[76:79], a[140:143], a[176:179], v[76:79]\n"
"s_waitcnt lgkmcnt(8)\n"
"v_mfma_f32_16x16x32_bf16 v[80:83], a[144:147], a[176:179], v[80:83]\n"
"ds_read_b64_tr_b16 a[132:133], v45 offset:8192\n"
"v_mfma_f32_16x16x32_bf16 v[112:115], a[144:147], a[180:183], v[112:115]\n"
"ds_read_b64_tr_b16 a[134:135], v45 offset:10240\n"
"v_mfma_f32_16x16x32_bf16 v[144:147], a[144:147], a[184:187], v[144:147]\n"
"buffer_load_dwordx4 a[60:63], v209, s[64:67], s81 offen offset:1024 sc1\n"
"v_mfma_f32_16x16x32_bf16 v[176:179], a[144:147], a[188:191], v[176:179]\n"
"s_add_u32 s81, s81, 0x1000\n"
"s_waitcnt lgkmcnt(8)\n"
"v_mfma_f32_16x16x32_bf16 v[180:183], a[148:151], a[188:191], v[180:183]\n"
"ds_read_b64_tr_b16 a[136:137], v46 offset:8192\n"
"v_mfma_f32_16x16x32_bf16 v[148:151], a[148:151], a[184:187], v[148:151]\n"
"ds_read_b64_tr_b16 a[138:139], v46 offset:10240\n"
"v_mfma_f32_16x16x32_bf16 v[116:119], a[148:151], a[180:183], v[116:119]\n"
"buffer_load_dwordx4 a[64:67], v208, s[64:67], s81 offen sc1\n"
"v_mfma_f32_16x16x32_bf16 v[84:87], a[148:151], a[176:179], v[84:87]\n"
"s_waitcnt lgkmcnt(8)\n"
"v_mfma_f32_16x16x32_bf16 v[88:91], a[152:155], a[176:179], v[88:91]\n"
"ds_read_b64_tr_b16 a[140:141], v47 offset:8192\n"
"v_mfma_f32_16x16x32_bf16 v[120:123], a[152:155], a[180:183], v[120:123]\n"
"ds_read_b64_tr_b16 a[142:143], v47 offset:10240\n"
"v_mfma_f32_16x16x32_bf16 v[152:155], a[152:155], a[184:187], v[152:155]\n"
"buffer_load_dwordx4 a[68:71], v208, s[64:67], s81 offen offset:1024 sc1\n"
"v_mfma_f32_16x16x32_bf16 v[184:187], a[152:155], a[188:191], v[184:187]\n"
"s_waitcnt lgkmcnt(8)\n"
"v_mfma_f32_16x16x32_bf16 v[188:191], a[156:159], a[188:191], v[188:191]\n"
"ds_read_b64_tr_b16 a[144:145], v44 offset:24576\n"
"v_mfma_f32_16x16x32_bf16 v[156:159], a[156:159], a[184:187], v[156:159]\n"
"ds_read_b64_tr_b16 a[146:147], v44 offset:26624\n"
"v_mfma_f32_16x16x32_bf16 v[124:127], a[156:159], a[180:183], v[124:127]\n"
"v_mfma_f32_16x16x32_bf16 v[92:95], a[156:159], a[176:179], v[92:95]\n"
"s_waitcnt lgkmcnt(8)\n"
"v_mfma_f32_16x16x32_bf16 v[64:67], a[128:131], a[192:195], v[64:67]\n"
"ds_read_b64_tr_b16 a[148:149], v45 offset:24576\n"
"v_mfma_f32_16x16x32_bf16 v[96:99], a[128:131], a[196:199], v[96:99]\n"
"ds_read_b64_tr_b16 a[150:151], v45 offset:26624\n"
"v_mfma_f32_16x16x32_bf16 v[128:131], a[128:131], a[200:203], v[128:131]\n"
"buffer_load_dwordx4 a[72:75], v209, s[64:67], s81 offen sc1\n"
"v_mfma_f32_16x16x32_bf16 v[160:163], a[128:131], a[204:207], v[160:163]\n"
"s_waitcnt lgkmcnt(8)\n"
"v_mfma_f32_16x16x32_bf16 v[164:167], a[132:135], a[204:207], v[164:167]\n"
"ds_read_b64_tr_b16 a[152:153], v46 offset:24576\n"
"v_mfma_f32_16x16x32_bf16 v[132:135], a[132:135], a[200:203], v[132:135]\n"
"ds_read_b64_tr_b16 a[154:155], v46 offset:26624\n"
"v_mfma_f32_16x16x32_bf16 v[100:103], a[132:135], a[196:199], v[100:103]\n"
"buffer_load_dwordx4 a[76:79], v209, s[64:67], s81 offen offset:1024 sc1\n"
"v_mfma_f32_16x16x32_bf16 v[68:71], a[132:135], a[192:195], v[68:71]\n"
"s_add_u32 s81, s81, 0x1000\n"
"s_waitcnt lgkmcnt(8)\n"
"v_mfma_f32_16x16x32_bf16 v[72:75], a[136:139], a[192:195], v[72:75]\n"
"ds_read_b64_tr_b16 a[156:157], v47 offset:24576\n"
"v_mfma_f32_16x16x32_bf16 v[104:107], a[136:139], a[196:199], v[104:107]\n"
"ds_read_b64_tr_b16 a[158:159], v47 offset:26624\n"
"v_mfma_f32_16x16x32_bf16 v[136:139], a[136:139], a[200:203], v[136:139]\n"
"buffer_load_dwordx4 a[80:83], v208, s[64:67], s81 offen sc1\n"
"s_lshl_b32 s91, s35, 12\n"
"v_mfma_f32_16x16x32_bf16 v[168:171], a[136:139], a[204:207], v[168:171]\n"
"s_waitcnt lgkmcnt(8)\n"
"s_add_u32 s91, s91, 0x20c0000\n"
"v_mfma_f32_16x16x32_bf16 v[172:175], a[140:143], a[204:207], v[172:175]\n"
"ds_read_b64_tr_b16 a[128:129], v44 offset:12288\n"
"v_and_b32 v238, 31, v236\n"
"v_mfma_f32_16x16x32_bf16 v[140:143], a[140:143], a[200:203], v[140:143]\n"
"ds_read_b64_tr_b16 a[130:131], v44 offset:14336\n"
"v_lshlrev_b32 v238, 7, v238\n"
"v_mfma_f32_16x16x32_bf16 v[108:111], a[140:143], a[196:199], v[108:111]\n"
"buffer_load_dwordx2 v[234:235], v238, s[64:67], s91 offen sc1\n"
"v_mfma_f32_16x16x32_bf16 v[76:79], a[140:143], a[192:195], v[76:79]\n"
"s_waitcnt lgkmcnt(8)\n"
"v_mfma_f32_16x16x32_bf16 v[80:83], a[144:147], a[192:195], v[80:83]\n"
"ds_read_b64_tr_b16 a[132:133], v45 offset:12288\n"
"v_mfma_f32_16x16x32_bf16 v[112:115], a[144:147], a[196:199], v[112:115]\n"
"ds_read_b64_tr_b16 a[134:135], v45 offset:14336\n"
"v_mfma_f32_16x16x32_bf16 v[144:147], a[144:147], a[200:203], v[144:147]\n"
"buffer_load_dwordx4 a[84:87], v208, s[64:67], s81 offen offset:1024 sc1\n"
"v_mfma_f32_16x16x32_bf16 v[176:179], a[144:147], a[204:207], v[176:179]\n"
"s_waitcnt lgkmcnt(8)\n"
"v_mfma_f32_16x16x32_bf16 v[180:183], a[148:151], a[204:207], v[180:183]\n"
"ds_read_b64_tr_b16 a[136:137], v46 offset:12288\n"
"v_mfma_f32_16x16x32_bf16 v[148:151], a[148:151], a[200:203], v[148:151]\n"
"ds_read_b64_tr_b16 a[138:139], v46 offset:14336\n"
"v_mfma_f32_16x16x32_bf16 v[116:119], a[148:151], a[196:199], v[116:119]\n"
"buffer_load_dwordx4 a[88:91], v209, s[64:67], s81 offen sc1\n"
"v_mfma_f32_16x16x32_bf16 v[84:87], a[148:151], a[192:195], v[84:87]\n"
"s_waitcnt lgkmcnt(8)\n"
"v_mfma_f32_16x16x32_bf16 v[88:91], a[152:155], a[192:195], v[88:91]\n"
"ds_read_b64_tr_b16 a[140:141], v47 offset:12288\n"
"v_mfma_f32_16x16x32_bf16 v[120:123], a[152:155], a[196:199], v[120:123]\n"
"ds_read_b64_tr_b16 a[142:143], v47 offset:14336\n"
"v_mfma_f32_16x16x32_bf16 v[152:155], a[152:155], a[200:203], v[152:155]\n"
"buffer_load_dwordx4 a[92:95], v209, s[64:67], s81 offen offset:1024 sc1\n"
"v_mfma_f32_16x16x32_bf16 v[184:187], a[152:155], a[204:207], v[184:187]\n"
"s_add_u32 s81, s81, 0x1000\n"
"s_waitcnt lgkmcnt(8)\n"
"v_mfma_f32_16x16x32_bf16 v[188:191], a[156:159], a[204:207], v[188:191]\n"
"ds_read_b64_tr_b16 a[144:145], v44 offset:28672\n"
"v_mfma_f32_16x16x32_bf16 v[156:159], a[156:159], a[200:203], v[156:159]\n"
"ds_read_b64_tr_b16 a[146:147], v44 offset:30720\n"
"v_mfma_f32_16x16x32_bf16 v[124:127], a[156:159], a[196:199], v[124:127]\n"
"buffer_load_dwordx4 a[96:99], v208, s[64:67], s81 offen sc1\n"
"v_mfma_f32_16x16x32_bf16 v[92:95], a[156:159], a[192:195], v[92:95]\n"
"s_waitcnt lgkmcnt(8)\n"
"v_mfma_f32_16x16x32_bf16 v[64:67], a[128:131], a[208:211], v[64:67]\n"
"ds_read_b64_tr_b16 a[148:149], v45 offset:28672\n"
"v_mfma_f32_16x16x32_bf16 v[96:99], a[128:131], a[212:215], v[96:99]\n"
"ds_read_b64_tr_b16 a[150:151], v45 offset:30720\n"
"v_mfma_f32_16x16x32_bf16 v[128:131], a[128:131], a[216:219], v[128:131]\n"
"buffer_load_dwordx4 a[100:103], v208, s[64:67], s81 offen offset:1024 sc1\n"
"v_mfma_f32_16x16x32_bf16 v[160:163], a[128:131], a[220:223], v[160:163]\n"
"s_waitcnt lgkmcnt(8)\n"
"v_mfma_f32_16x16x32_bf16 v[164:167], a[132:135], a[220:223], v[164:167]\n"
"ds_read_b64_tr_b16 a[152:153], v46 offset:28672\n"
"v_mfma_f32_16x16x32_bf16 v[132:135], a[132:135], a[216:219], v[132:135]\n"
"ds_read_b64_tr_b16 a[154:155], v46 offset:30720\n"
"v_mfma_f32_16x16x32_bf16 v[100:103], a[132:135], a[212:215], v[100:103]\n"
"v_mfma_f32_16x16x32_bf16 v[68:71], a[132:135], a[208:211], v[68:71]\n"
"s_waitcnt lgkmcnt(8)\n"
"v_mfma_f32_16x16x32_bf16 v[72:75], a[136:139], a[208:211], v[72:75]\n"
"ds_read_b64_tr_b16 a[156:157], v47 offset:28672\n"
"v_mfma_f32_16x16x32_bf16 v[104:107], a[136:139], a[212:215], v[104:107]\n"
"ds_read_b64_tr_b16 a[158:159], v47 offset:30720\n"
"v_mfma_f32_16x16x32_bf16 v[136:139], a[136:139], a[216:219], v[136:139]\n"
"buffer_load_dwordx4 a[104:107], v209, s[64:67], s81 offen sc1\n"
"v_mfma_f32_16x16x32_bf16 v[168:171], a[136:139], a[220:223], v[168:171]\n"
"s_waitcnt lgkmcnt(8)\n"
"v_mfma_f32_16x16x32_bf16 v[172:175], a[140:143], a[220:223], v[172:175]\n"
"ds_read_b64_tr_b16 a[128:129], v40\n"
"v_mfma_f32_16x16x32_bf16 v[140:143], a[140:143], a[216:219], v[140:143]\n"
"ds_read_b64_tr_b16 a[130:131], v40 offset:2048\n"
"v_mfma_f32_16x16x32_bf16 v[108:111], a[140:143], a[212:215], v[108:111]\n"
"buffer_load_dwordx4 a[108:111], v209, s[64:67], s81 offen offset:1024 sc1\n"
"v_mfma_f32_16x16x32_bf16 v[76:79], a[140:143], a[208:211], v[76:79]\n"
"s_add_u32 s81, s81, 0x1000\n"
"s_waitcnt lgkmcnt(8)\n"
"v_mfma_f32_16x16x32_bf16 v[80:83], a[144:147], a[208:211], v[80:83]\n"
"ds_read_b64_tr_b16 a[132:133], v41\n"
"v_mfma_f32_16x16x32_bf16 v[112:115], a[144:147], a[212:215], v[112:115]\n"
"ds_read_b64_tr_b16 a[134:135], v41 offset:2048\n"
"s_add_u32 m0, s82, 0x1c000\n"
"v_mfma_f32_16x16x32_bf16 v[144:147], a[144:147], a[216:219], v[144:147]\n"
"buffer_load_dwordx4 v217, s[56:59], s37 offen lds\n"
"v_mfma_f32_16x16x32_bf16 v[176:179], a[144:147], a[220:223], v[176:179]\n"
"s_waitcnt lgkmcnt(8)\n"
"v_mfma_f32_16x16x32_bf16 v[180:183], a[148:151], a[220:223], v[180:183]\n"
"ds_read_b64_tr_b16 a[136:137], v42\n"
"v_mfma_f32_16x16x32_bf16 v[148:151], a[148:151], a[216:219], v[148:151]\n"
"ds_read_b64_tr_b16 a[138:139], v42 offset:2048\n"
"v_mfma_f32_16x16x32_bf16 v[116:119], a[148:151], a[212:215], v[116:119]\n"
"v_mfma_f32_16x16x32_bf16 v[84:87], a[148:151], a[208:211], v[84:87]\n"
"s_waitcnt lgkmcnt(8)\n"
"v_mfma_f32_16x16x32_bf16 v[88:91], a[152:155], a[208:211], v[88:91]\n"
"ds_read_b64_tr_b16 a[140:141], v43\n"
"v_mfma_f32_16x16x32_bf16 v[120:123], a[152:155], a[212:215], v[120:123]\n"
"ds_read_b64_tr_b16 a[142:143], v43 offset:2048\n"
"s_add_u32 m0, s82, 0x1c000\n"
"v_mfma_f32_16x16x32_bf16 v[152:155], a[152:155], a[216:219], v[152:155]\n"
"buffer_load_dwordx4 v218, s[56:59], s37 offen offset:1024 lds\n"
"v_mfma_f32_16x16x32_bf16 v[184:187], a[152:155], a[220:223], v[184:187]\n"
"s_waitcnt lgkmcnt(8)\n"
"v_mfma_f32_16x16x32_bf16 v[188:191], a[156:159], a[220:223], v[188:191]\n"
"ds_read_b64_tr_b16 a[144:145], v40 offset:16384\n"
"v_mfma_f32_16x16x32_bf16 v[156:159], a[156:159], a[216:219], v[156:159]\n"
".Lpad11_%=:\n"
"s_cbranch_execz .Lback0_%=\n"
"ds_read_b64_tr_b16 a[146:147], v40 offset:18432\n"
"s_add_u32 m0, s82, 0x1c000\n"
"v_mfma_f32_16x16x32_bf16 v[124:127], a[156:159], a[212:215], v[124:127]\n"
"buffer_load_dwordx4 v219, s[56:59], s37 offen offset:2048 lds\n"
"v_mfma_f32_16x16x32_bf16 v[92:95], a[156:159], a[208:211], v[92:95]\n"
"s_barrier\n"
"s_waitcnt lgkmcnt(8)\n"
"v_mfma_f32_16x16x32_bf16 v[64:67], a[128:131], a[224:227], v[64:67]\n"
"ds_read_b64_tr_b16 a[148:149], v41 offset:16384\n"
"s_mov_b32 s90, 0x2030000\n"
"v_mfma_f32_16x16x32_bf16 v[96:99], a[128:131], a[228:231], v[96:99]\n"
"ds_read_b64_tr_b16 a[150:151], v41 offset:18432\n"
"s_add_u32 m0, s82, 0x1c000\n"
"v_mfma_f32_16x16x32_bf16 v[128:131], a[128:131], a[232:235], v[128:131]\n"
"buffer_load_dwordx4 v220, s[56:59], s37 offen offset:3072 lds\n"
"s_cmp_eq_u32 s86, 0\n"
"s_cselect_b32 s90, s90, 0x40000000\n"
"v_mfma_f32_16x16x32_bf16 v[160:163], a[128:131], a[236:239], v[160:163]\n"
"s_add_u32 s37, s37, 0x40000\n"
"s_waitcnt lgkmcnt(8)\n"
"v_mfma_f32_16x16x32_bf16 v[164:167], a[132:135], a[236:239], v[164:167]\n"
"ds_read_b64_tr_b16 a[152:153], v42 offset:16384\n"
"v_mfma_f32_16x16x32_bf16 v[132:135], a[132:135], a[232:235], v[132:135]\n"
"ds_read_b64_tr_b16 a[154:155], v42 offset:18432\n"
"v_mfma_f32_16x16x32_bf16 v[100:103], a[132:135], a[228:231], v[100:103]\n"
"buffer_store_dwordx2 v[228:229], v225, s[64:67], s90 offen sc1\n"
"v_mfma_f32_16x16x32_bf16 v[68:71], a[132:135], a[224:227], v[68:71]\n"
"s_waitcnt lgkmcnt(8)\n"
"v_mfma_f32_16x16x32_bf16 v[72:75], a[136:139], a[224:227], v[72:75]\n"
"ds_read_b64_tr_b16 a[156:157], v43 offset:16384\n"
"v_mfma_f32_16x16x32_bf16 v[104:107], a[136:139], a[228:231], v[104:107]\n"
"ds_read_b64_tr_b16 a[158:159], v43 offset:18432\n"
"s_add_u32 m0, s82, 0x10000\n"
"v_mfma_f32_16x16x32_bf16 v[136:139], a[136:139], a[232:235], v[136:139]\n"
"buffer_load_dwordx4 v217, s[60:63], s80 offen lds\n"
"v_mfma_f32_16x16x32_bf16 v[168:171], a[136:139], a[236:239], v[168:171]\n"
"s_waitcnt lgkmcnt(8)\n"
"v_mfma_f32_16x16x32_bf16 v[172:175], a[140:143], a[236:239], v[172:175]\n"
"ds_read_b64_tr_b16 a[128:129], v40 offset:4096\n"
"v_mfma_f32_16x16x32_bf16 v[140:143], a[140:143], a[232:235], v[140:143]\n"
"ds_read_b64_tr_b16 a[130:131], v40 offset:6144\n"
"s_waitcnt vmcnt(32)\n"
"s_add_u32 m0, s82, 0x10000\n"
"v_mfma_f32_16x16x32_bf16 v[108:111], a[140:143], a[228:231], v[108:111]\n"
"buffer_load_dwordx4 v218, s[60:63], s80 offen offset:1024 lds\n"
"v_mfma_f32_16x16x32_bf16 v[76:79], a[140:143], a[224:227], v[76:79]\n"
"s_waitcnt lgkmcnt(8)\n"
"s_waitcnt vmcnt(14)\n"
".Lchk5_%=:\n"
"v_cmp_ne_u64 vcc, s[84:85], v[234:235]\n"
"s_nop 1\n"
"s_cmp_eq_u64 vcc, 0\n"
"s_cbranch_scc1 .Lok5_%=\n"
"s_cmp_eq_u32 s94, 0\n"
"s_cbranch_scc1 .Lok5_%=\n"
"s_sub_u32 s94, s94, 1\n"
"s_sleep 1\n"
"buffer_load_dwordx2 v[234:235], v238, s[64:67], s91 offen sc1\n"
"s_waitcnt vmcnt(0)\n"
"s_branch .Lchk5_%=\n"
".Lok5_%=:\n"
"v_mfma_f32_16x16x32_bf16 v[80:83], a[144:147], a[224:227], v[80:83]\n"
"ds_read_b64_tr_b16 a[132:133], v41 offset:4096\n"
"v_mfma_f32_16x16x32_bf16 v[112:115], a[144:147], a[228:231], v[112:115]\n"
"ds_read_b64_tr_b16 a[134:135], v41 offset:6144\n"
"s_add_u32 m0, s82, 0x10000\n"
"v_mfma_f32_16x16x32_bf16 v[144:147], a[144:147], a[232:235], v[144:147]\n"
"buffer_load_dwordx4 v219, s[60:63], s80 offen offset:2048 lds\n"
"v_mfma_f32_16x16x32_bf16 v[176:179], a[144:147], a[236:239], v[176:179]\n"
"s_waitcnt lgkmcnt(8)\n"
"v_mfma_f32_16x16x32_bf16 v[180:183], a[148:151], a[236:239], v[180:183]\n"
"ds_read_b64_tr_b16 a[136:137], v42 offset:4096\n"
"v_mfma_f32_16x16x32_bf16 v[148:151], a[148:151], a[232:235], v[148:151]\n"
"ds_read_b64_tr_b16 a[138:139], v42 offset:6144\n"
"s_add_u32 m0, s82, 0x10000\n"
"v_mfma_f32_16x16x32_bf16 v[116:119], a[148:151], a[228:231], v[116:119]\n"
"buffer_load_dwordx4 v220, s[60:63], s80 offen offset:3072 lds\n"
"v_mfma_f32_16x16x32_bf16 v[84:87], a[148:151], a[224:227], v[84:87]\n"
"s_add_u32 s80, s80, 0x40000\n"
"s_waitcnt lgkmcnt(8)\n"
"v_mfma_f32_16x16x32_bf16 v[88:91], a[152:155], a[224:227], v[88:91]\n"
"ds_read_b64_tr_b16 a[140:141], v43 offset:4096\n"
"v_mfma_f32_16x16x32_bf16 v[120:123], a[152:155], a[228:231], v[120:123]\n"
"ds_read_b64_tr_b16 a[142:143], v43 offset:6144\n"
"v_mfma_f32_16x16x32_bf16 v[152:155], a[152:155], a[232:235], v[152:155]\n"
"buffer_load_dwordx4 a[112:115], v208, s[64:67], s81 offen sc1\n"
"v_mfma_f32_16x16x32_bf16 v[184:187], a[152:155], a[236:239], v[184:187]\n"
"s_waitcnt lgkmcnt(8)\n"
"v_mfma_f32_16x16x32_bf16 v[188:191], a[156:159], a[236:239], v[188:191]\n"
"ds_read_b64_tr_b16 a[144:145], v40 offset:20480\n"
"v_mfma_f32_16x16x32_bf16 v[156:159], a[156:159], a[232:235], v[156:159]\n"
"ds_read_b64_tr_b16 a[146:147], v40 offset:22528\n"
"v_mfma_f32_16x16x32_bf16 v[124:127], a[156:159], a[228:231], v[124:127]\n"
"buffer_load_dwordx4 a[116:119], v208, s[64:67], s81 offen offset:1024 sc1\n"
"v_mfma_f32_16x16x32_bf16 v[92:95], a[156:159], a[224:227], v[92:95]\n"
"s_waitcnt lgkmcnt(8)\n"
"v_mfma_f32_16x16x32_bf16 v[64:67], a[128:131], a[240:243], v[64:67]\n"
"ds_read_b64_tr_b16 a[148:149], v41 offset:20480\n"
"v_mfma_f32_16x16x32_bf16 v[96:99], a[128:131], a[244:247], v[96:99]\n"
"ds_read_b64_tr_b16 a[150:151], v41 offset:22528\n"
"v_mfma_f32_16x16x32_bf16 v[128:131], a[128:131], a[248:251], v[128:131]\n"
"v_mfma_f32_16x16x32_bf16 v[160:163], a[128:131], a[252:255], v[160:163]\n"
"s_waitcnt lgkmcnt(8)\n"
"v_mfma_f32_16x16x32_bf16 v[164:167], a[132:135], a[252:255], v[164:167]\n"
"ds_read_b64_tr_b16 a[152:153], v42 offset:20480\n"
"v_mfma_f32_16x16x32_bf16 v[132:135], a[132:135], a[248:251], v[132:135]\n"
"ds_read_b64_tr_b16 a[154:155], v42 offset:22528\n"
"v_mfma_f32_16x16x32_bf16 v[100:103], a[132:135], a[244:247], v[100:103]\n"
"buffer_load_dwordx4 a[120:123], v209, s[64:67], s81 offen sc1\n"
"v_mfma_f32_16x16x32_bf16 v[68:71], a[132:135], a[240:243], v[68:71]\n"
"s_waitcnt lgkmcnt(8)\n"
"v_mfma_f32_16x16x32_bf16 v[72:75], a[136:139], a[240:243], v[72:75]\n"
"ds_read_b64_tr_b16 a[156:157], v43 offset:20480\n"
"v_mfma_f32_16x16x32_bf16 v[104:107], a[136:139], a[244:247], v[104:107]\n"
"ds_read_b64_tr_b16 a[158:159], v43 offset:22528\n"
"v_mfma_f32_16x16x32_bf16 v[136:139], a[136:139], a[248:251], v[136:139]\n"
"buffer_load_dwordx4 a[124:127], v209, s[64:67], s81 offen offset:1024 sc1\n"
"v_mfma_f32_16x16x32_bf16 v[168:171], a[136:139], a[252:255], v[168:171]\n"
"s_add_u32 s81, s81, 0x1000\n"
"s_waitcnt lgkmcnt(8)\n"
"v_mfma_f32_16x16x32_bf16 v[172:175], a[140:143], a[252:255], v[172:175]\n"
"ds_read_b64_tr_b16 a[128:129], v40 offset:8192\n"
"v_mfma_f32_16x16x32_bf16 v[140:143], a[140:143], a[248:251], v[140:143]\n"
"ds_read_b64_tr_b16 a[130:131], v40 offset:10240\n"
"v_mfma_f32_16x16x32_bf16 v[108:111], a[140:143], a[244:247], v[108:111]\n"
"buffer_load_dwordx4 a[160:163], v208, s[64:67], s81 offen sc1\n"
"v_mfma_f32_16x16x32_bf16 v[76:79], a[140:143], a[240:243], v[76:79]\n"
"s_waitcnt lgkmcnt(8)\n"
"v_mfma_f32_16x16x32_bf16 v[80:83], a[144:147], a[240:243], v[80:83]\n"
"ds_read_b64_tr_b16 a[132:133], v41 offset:8192\n"
"v_mfma_f32_16x16x32_bf16 v[112:115], a[144:147], a[244:247], v[112:115]\n"
"ds_read_b64_tr_b16 a[134:135], v41 offset:10240\n"
"v_mfma_f32_16x16x32_bf16 v[144:147], a[144:147], a[248:251], v[144:147]\n"
"buffer_load_dwordx4 a[164:167], v208, s[64:67], s81 offen offset:1024 sc1\n"
"v_mfma_f32_16x16x32_bf16 v[176:179], a[144:147], a[252:255], v[176:179]\n"
"s_waitcnt lgkmcnt(8)\n"
"v_mfma_f32_16x16x32_bf16 v[180:183], a[148:151], a[252:255], v[180:183]\n"
"ds_read_b64_tr_b16 a[136:137], v42 offset:8192\n"
"v_mfma_f32_16x16x32_bf16 v[148:151], a[148:151], a[248:251], v[148:151]\n"
"ds_read_b64_tr_b16 a[138:139], v42 offset:10240\n"
"v_mfma_f32_16x16x32_bf16 v[116:119], a[148:151], a[244:247], v[116:119]\n"
"buffer_load_dwordx4 a[168:171], v209, s[64:67], s81 offen sc1\n"
"v_mfma_f32_16x16x32_bf16 v[84:87], a[148:151], a[240:243], v[84:87]\n"
"s_waitcnt lgkmcnt(8)\n"
"v_mfma_f32_16x16x32_bf16 v[88:91], a[152:155], a[240:243], v[88:91]\n"
"ds_read_b64_tr_b16 a[140:141], v43 offset:8192\n"
"v_mfma_f32_16x16x32_bf16 v[120:123], a[152:155], a[244:247], v[120:123]\n"
"ds_read_b64_tr_b16 a[142:143], v43 offset:10240\n"
"v_mfma_f32_16x16x32_bf16 v[152:155], a[152:155], a[248:251], v[152:155]\n"
"v_mfma_f32_16x16x32_bf16 v[184:187], a[152:155], a[252:255], v[184:187]\n"
"s_waitcnt lgkmcnt(8)\n"
"v_mfma_f32_16x16x32_bf16 v[188:191], a[156:159], a[252:255], v[188:191]\n"
"ds_read_b64_tr_b16 a[144:145], v40 offset:24576\n"
"v_mfma_f32_16x16x32_bf16 v[156:159], a[156:159], a[248:251], v[156:159]\n"
"ds_read_b64_tr_b16 a[146:147], v40 offset:26624\n"
"v_mfma_f32_16x16x32_bf16 v[124:127], a[156:159], a[244:247], v[124:127]\n"
"buffer_load_dwordx4 a[172:175], v209, s[64:67], s81 offen offset:1024 sc1\n"
"v_mfma_f32_16x16x32_bf16 v[92:95], a[156:159], a[240:243], v[92:95]\n"
"s_add_u32 s81, s81, 0x1000\n"
"s_waitcnt lgkmcnt(8)\n"
"v_mfma_f32_16x16x32_bf16 v[64:67], a[128:131], v[16:19], v[64:67]\n"
"ds_read_b64_tr_b16 a[148:149], v41 offset:24576\n"
"v_mfma_f32_16x16x32_bf16 v[96:99], a[128:131], v[20:23], v[96:99]\n"
"ds_read_b64_tr_b16 a[150:151], v41 offset:26624\n"
"v_mfma_f32_16x16x32_bf16 v[128:131], a[128:131], v[24:27], v[128:131]\n"
"buffer_load_dwordx4 v[0:3], v208, s[64:67], s81 offen sc1\n"
"v_mfma_f32_16x16x32_bf16 v[160:163], a[128:131], v[28:31], v[160:163]\n"
"s_waitcnt lgkmcnt(8)\n"
"v_mfma_f32_16x16x32_bf16 v[164:167], a[132:135], v[28:31], v[164:167]\n"
"ds_read_b64_tr_b16 a[152:153], v42 offset:24576\n"
"v_mfma_f32_16x16x32_bf16 v[132:135], a[132:135], v[24:27], v[132:135]\n"
"ds_read_b64_tr_b16 a[154:155], v42 offset:26624\n"
"v_mfma_f32_16x16x32_bf16 v[100:103], a[132:135], v[20:23], v[100:103]\n"
"buffer_load_dwordx4 v[4:7], v208, s[64:67], s81 offen offset:1024 sc1\n"
"v_mfma_f32_16x16x32_bf16 v[68:71], a[132:135], v[16:19], v[68:71]\n"
"s_waitcnt lgkmcnt(8)\n"
"v_mfma_f32_16x16x32_bf16 v[72:75], a[136:139], v[16:19], v[72:75]\n"
"ds_read_b64_tr_b16 a[156:157], v43 offset:24576\n"
"v_mfma_f32_16x16x32_bf16 v[104:107], a[136:139], v[20:23], v[104:107]\n"
"ds_read_b64_tr_b16 a[158:159], v43 offset:26624\n"
"v_mfma_f32_16x16x32_bf16 v[136:139], a[136:139], v[24:27], v[136:139]\n"
"buffer_load_dwordx4 v[8:11], v209, s[64:67], s81 offen sc1\n"
"v_mfma_f32_16x16x32_bf16 v[168:171], a[136:139], v[28:31], v[168:171]\n"
"s_waitcnt lgkmcnt(8)\n"
"v_mfma_f32_16x16x32_bf16 v[172:175], a[140:143], v[28:31], v[172:175]\n"
"ds_read_b64_tr_b16 a[128:129], v40 offset:12288\n"
"v_mfma_f32_16x16x32_bf16 v[140:143], a[140:143], v[24:27], v[140:143]\n"
"ds_read_b64_tr_b16 a[130:131], v40 offset:14336\n"
"v_mfma_f32_16x16x32_bf16 v[108:111], a[140:143], v[20:23], v[108:111]\n"
"buffer_load_dwordx4 v[12:15], v209, s[64:67], s81 offen offset:1024 sc1\n"
"v_mfma_f32_16x16x32_bf16 v[76:79], a[140:143], v[16:19], v[76:79]\n"
"s_add_u32 s81, s81, 0x1000\n"
"s_waitcnt lgkmcnt(8)\n"
"v_mfma_f32_16x16x32_bf16 v[80:83], a[144:147], v[16:19], v[80:83]\n"
"ds_read_b64_tr_b16 a[132:133], v41 offset:12288\n"
"v_mfma_f32_16x16x32_bf16 v[112:115], a[144:147], v[20:23], v[112:115]\n"
"ds_read_b64_tr_b16 a[134:135], v41 offset:14336\n"
"v_mfma_f32_16x16x32_bf16 v[144:147], a[144:147], v[24:27], v[144:147]\n"
"v_mfma_f32_16x16x32_bf16 v[176:179], a[144:147], v[28:31], v[176:179]\n"
"s_waitcnt lgkmcnt(8)\n"
"v_mfma_f32_16x16x32_bf16 v[180:183], a[148:151], v[28:31], v[180:183]\n"
"ds_read_b64_tr_b16 a[136:137], v42 offset:12288\n"
"v_mfma_f32_16x16x32_bf16 v[148:151], a[148:151], v[24:27], v[148:151]\n"
"ds_read_b64_tr_b16 a[138:139], v42 offset:14336\n"
"v_mfma_f32_16x16x32_bf16 v[116:119], a[148:151], v[20:23], v[116:119]\n"
"buffer_load_dwordx4 v[192:195], v208, s[64:67], s81 offen sc1\n"
"v_mfma_f32_16x16x32_bf16 v[84:87], a[148:151], v[16:19], v[84:87]\n"
"s_waitcnt lgkmcnt(8)\n"
"v_mfma_f32_16x16x32_bf16 v[88:91], a[152:155], v[16:19], v[88:91]\n"
"ds_read_b64_tr_b16 a[140:141], v43 offset:12288\n"
"v_mfma_f32_16x16x32_bf16 v[120:123], a[152:155], v[20:23], v[120:123]\n"
"ds_read_b64_tr_b16 a[142:143], v43 offset:14336\n"
"v_mfma_f32_16x16x32_bf16 v[152:155], a[152:155], v[24:27], v[152:155]\n"
"buffer_load_dwordx4 v[196:199], v208, s[64:67], s81 offen offset:1024 sc1\n"
"v_mfma_f32_16x16x32_bf16 v[184:187], a[152:155], v[28:31], v[184:187]\n"
"s_waitcnt lgkmcnt(8)\n"
"v_mfma_f32_16x16x32_bf16 v[188:191], a[156:159], v[28:31], v[188:191]\n"
"ds_read_b64_tr_b16 a[144:145], v40 offset:28672\n"
"v_mfma_f32_16x16x32_bf16 v[156:159], a[156:159], v[24:27], v[156:159]\n"
"ds_read_b64_tr_b16 a[146:147], v40 offset:30720\n"
"v_mfma_f32_16x16x32_bf16 v[124:127], a[156:159], v[20:23], v[124:127]\n"
"buffer_load_dwordx4 v[200:203], v209, s[64:67], s81 offen sc1\n"
"v_mfma_f32_16x16x32_bf16 v[92:95], a[156:159], v[16:19], v[92:95]\n"
"s_waitcnt lgkmcnt(8)\n"
"v_mfma_f32_16x16x32_bf16 v[64:67], a[128:131], v[32:35], v[64:67]\n"
"ds_read_b64_tr_b16 a[148:149], v41 offset:28672\n"
"v_mfma_f32_16x16x32_bf16 v[96:99], a[128:131], v[36:39], v[96:99]\n"
"ds_read_b64_tr_b16 a[150:151], v41 offset:30720\n"
"v_mfma_f32_16x16x32_bf16 v[128:131], a[128:131], v[48:51], v[128:131]\n"
"buffer_load_dwordx4 v[204:207], v209, s[64:67], s81 offen offset:1024 sc1\n"
"v_mfma_f32_16x16x32_bf16 v[160:163], a[128:131], v[52:55], v[160:163]\n"
"s_add_u32 s81, s81, 0x1000\n"
"s_waitcnt lgkmcnt(8)\n"
"v_mfma_f32_16x16x32_bf16 v[164:167], a[132:135], v[52:55], v[164:167]\n"
"ds_read_b64_tr_b16 a[152:153], v42 offset:28672\n"
"v_mfma_f32_16x16x32_bf16 v[132:135], a[132:135], v[48:51], v[132:135]\n"
"ds_read_b64_tr_b16 a[154:155], v42 offset:30720\n"
"v_mfma_f32_16x16x32_bf16 v[100:103], a[132:135], v[36:39], v[100:103]\n"
"buffer_load_dwordx4 v[240:243], v208, s[64:67], s81 offen sc1\n"
"v_mfma_f32_16x16x32_bf16 v[68:71], a[132:135], v[32:35], v[68:71]\n"
"s_waitcnt lgkmcnt(8)\n"
"v_mfma_f32_16x16x32_bf16 v[72:75], a[136:139], v[32:35], v[72:75]\n"
"ds_read_b64_tr_b16 a[156:157], v43 offset:28672\n"
"v_mfma_f32_16x16x32_bf16 v[104:107], a[136:139], v[36:39], v[104:107]\n"
"ds_read_b64_tr_b16 a[158:159], v43 offset:30720\n"
"v_mfma_f32_16x16x32_bf16 v[136:139], a[136:139], v[48:51], v[136:139]\n"
"v_mfma_f32_16x16x32_bf16 v[168:171], a[136:139], v[52:55], v[168:171]\n"
"s_waitcnt lgkmcnt(8)\n"
"v_mfma_f32_16x16x32_bf16 v[172:175], a[140:143], v[52:55], v[172:175]\n"
"ds_read_b64_tr_b16 a[128:129], v40 offset:32768\n"
"v_mfma_f32_16x16x32_bf16 v[140:143], a[140:143], v[48:51], v[140:143]\n"
"ds_read_b64_tr_b16 a[130:131], v40 offset:34816\n"
"v_mfma_f32_16x16x32_bf16 v[108:111], a[140:143], v[36:39], v[108:111]\n"
"buffer_load_dwordx4 v[244:247], v208, s[64:67], s81 offen offset:1024 sc1\n"
"v_mfma_f32_16x16x32_bf16 v[76:79], a[140:143], v[32:35], v[76:79]\n"
"s_waitcnt lgkmcnt(8)\n"
"v_mfma_f32_16x16x32_bf16 v[80:83], a[144:147], v[32:35], v[80:83]\n"
"ds_read_b64_tr_b16 a[132:133], v41 offset:32768\n"
"v_mfma_f32_16x16x32_bf16 v[112:115], a[144:147], v[36:39], v[112:115]\n"
"ds_read_b64_tr_b16 a[134:135], v41 offset:34816\n"
"v_mfma_f32_16x16x32_bf16 v[144:147], a[144:147], v[48:51], v[144:147]\n"
"buffer_load_dwordx4 v[248:251], v209, s[64:67], s81 offen sc1\n"
"v_mfma_f32_16x16x32_bf16 v[176:179], a[144:147], v[52:55], v[176:179]\n"
"s_waitcnt lgkmcnt(8)\n"
"v_mfma_f32_16x16x32_bf16 v[180:183], a[148:151], v[52:55], v[180:183]\n"
"ds_read_b64_tr_b16 a[136:137], v42 offset:32768\n"
"v_mfma_f32_16x16x32_bf16 v[148:151], a[148:151], v[48:51], v[148:151]\n"
"ds_read_b64_tr_b16 a[138:139], v42 offset:34816\n"
"v_mfma_f32_16x16x32_bf16 v[116:119], a[148:151], v[36:39], v[116:119]\n"
"buffer_load_dwordx4 v[252:255], v209, s[64:67], s81 offen offset:1024 sc1\n"
"v_mfma_f32_16x16x32_bf16 v[84:87], a[148:151], v[32:35], v[84:87]\n"
"s_add_u32 s81, s81, 0x1000\n"
"s_waitcnt lgkmcnt(8)\n"
"v_mfma_f32_16x16x32_bf16 v[88:91], a[152:155], v[32:35], v[88:91]\n"
"ds_read_b64_tr_b16 a[140:141], v43 offset:32768\n"
"v_mfma_f32_16x16x32_bf16 v[120:123], a[152:155], v[36:39], v[120:123]\n"
"ds_read_b64_tr_b16 a[142:143], v43 offset:34816\n"
"v_mfma_f32_16x16x32_bf16 v[152:155], a[152:155], v[48:51], v[152:155]\n"
"buffer_load_dwordx4 a[176:179], v208, s[64:67], s29 offen sc1\n"
"v_mfma_f32_16x16x32_bf16 v[184:187], a[152:155], v[52:55], v[184:187]\n"
"s_waitcnt lgkmcnt(8)\n"
"v_mfma_f32_16x16x32_bf16 v[188:191], a[156:159], v[52:55], v[188:191]\n"
"ds_read_b64_tr_b16 a[144:145], v40 offset:36864\n"
"v_mfma_f32_16x16x32_bf16 v[156:159], a[156:159], v[48:51], v[156:159]\n"
"ds_read_b64_tr_b16 a[146:147], v40 offset:38912\n"
"v_mfma_f32_16x16x32_bf16 v[124:127], a[156:159], v[36:39], v[124:127]\n"
"buffer_load_dwordx4 a[180:183], v208, s[64:67], s29 offen offset:1024 sc1\n"
"v_mfma_f32_16x16x32_bf16 v[92:95], a[156:159], v[32:35], v[92:95]\n"
"s_barrier\n"
"s_waitcnt lgkmcnt(8)\n"
"v_mfma_f32_16x16x32_bf16 v[80:83], a[128:131], a[0:3], v[80:83]\n"
"ds_read_b64_tr_b16 a[148:149], v41 offset:36864\n"
"v_mfma_f32_16x16x32_bf16 v[112:115], a[128:131], a[4:7], v[112:115]\n"
"ds_read_b64_tr_b16 a[150:151], v41 offset:38912\n"
"v_mfma_f32_16x16x32_bf16 v[144:147], a[128:131], a[8:11], v[144:147]\n"
"v_mfma_f32_16x16x32_bf16 v[176:179], a[128:131], a[12:15], v[176:179]\n"
"s_waitcnt lgkmcnt(8)\n"
"v_mfma_f32_16x16x32_bf16 v[180:183], a[132:135], a[12:15], v[180:183]\n"
"ds_read_b64_tr_b16 a[152:153], v42 offset:36864\n"
"v_mfma_f32_16x16x32_bf16 v[148:151], a[132:135], a[8:11], v[148:151]\n"
"ds_read_b64_tr_b16 a[154:155], v42 offset:38912\n"
"v_mfma_f32_16x16x32_bf16 v[116:119], a[132:135], a[4:7], v[116:119]\n"
"buffer_load_dwordx4 a[184:187], v209, s[64:67], s29 offen sc1\n"
"v_mfma_f32_16x16x32_bf16 v[84:87], a[132:135], a[0:3], v[84:87]\n"
"s_waitcnt lgkmcnt(8)\n"
"v_mfma_f32_16x16x32_bf16 v[88:91], a[136:139], a[0:3], v[88:91]\n"
"ds_read_b64_tr_b16 a[156:157], v43 offset:36864\n"
"v_mfma_f32_16x16x32_bf16 v[120:123], a[136:139], a[4:7], v[120:123]\n"
"ds_read_b64_tr_b16 a[158:159], v43 offset:38912\n"
"v_mfma_f32_16x16x32_bf16 v[152:155], a[136:139], a[8:11], v[152:155]\n"
"buffer_load_dwordx4 a[188:191], v209, s[64:67], s29 offen offset:1024 sc1\n"
"v_mfma_f32_16x16x32_bf16 v[184:187], a[136:139], a[12:15], v[184:187]\n"
"s_add_u32 s29, s29, 0x1000\n"
"s_waitcnt lgkmcnt(8)\n"
"v_mfma_f32_16x16x32_bf16 v[188:191], a[140:143], a[12:15], v[188:191]\n"
"ds_read_b64_tr_b16 a[128:129], v40 offset:40960\n"
"v_mfma_f32_16x16x32_bf16 v[156:159], a[140:143], a[8:11], v[156:159]\n"
"ds_read_b64_tr_b16 a[130:131], v40 offset:43008\n"
"v_mfma_f32_16x16x32_bf16 v[124:127], a[140:143], a[4:7], v[124:127]\n"
"buffer_load_dwordx4 a[192:195], v208, s[64:67], s29 offen sc1\n"
"v_mfma_f32_16x16x32_bf16 v[92:95], a[140:143], a[0:3], v[92:95]\n"
"s_waitcnt lgkmcnt(8)\n"
"v_mfma_f32_16x16x32_bf16 v[80:83], a[144:147], a[16:19], v[80:83]\n"
"ds_read_b64_tr_b16 a[132:133], v41 offset:40960\n"
"v_mfma_f32_16x16x32_bf16 v[112:115], a[144:147], a[20:23], v[112:115]\n"
"ds_read_b64_tr_b16 a[134:135], v41 offset:43008\n"
"v_mfma_f32_16x16x32_bf16 v[144:147], a[144:147], a[24:27], v[144:147]\n"
"buffer_load_dwordx4 a[196:199], v208, s[64:67], s29 offen offset:1024 sc1\n"
"v_mfma_f32_16x16x32_bf16 v[176:179], a[144:147], a[28:31], v[176:179]\n"
"s_waitcnt lgkmcnt(8)\n"
"v_mfma_f32_16x16x32_bf16 v[180:183], a[148:151], a[28:31], v[180:183]\n"
"ds_read_b64_tr_b16 a[136:137], v42 offset:40960\n"
"v_mfma_f32_16x16x32_bf16 v[148:151], a[148:151], a[24:27], v[148:151]\n"
"ds_read_b64_tr_b16 a[138:139], v42 offset:43008\n"
"v_mfma_f32_16x16x32_bf16 v[116:119], a[148:151], a[20:23], v[116:119]\n"
"buffer_load_dwordx4 a[200:203], v209, s[64:67], s29 offen sc1\n"
"v_mfma_f32_16x16x32_bf16 v[84:87], a[148:151], a[16:19], v[84:87]\n"
"s_waitcnt lgkmcnt(8)\n"
"v_mfma_f32_16x16x32_bf16 v[88:91], a[152:155], a[16:19], v[88:91]\n"
"ds_read_b64_tr_b16 a[140:141], v43 offset:40960\n"
"v_mfma_f32_16x16x32_bf16 v[120:123], a[152:155], a[20:23], v[120:123]\n"
"ds_read_b64_tr_b16 a[142:143], v43 offset:43008\n"
"v_mfma_f32_16x16x32_bf16 v[152:155], a[152:155], a[24:27], v[152:155]\n"
"v_mfma_f32_16x16x32_bf16 v[184:187], a[152:155], a[28:31], v[184:187]\n"
"s_waitcnt lgkmcnt(8)\n"
"v_mfma_f32_16x16x32_bf16 v[188:191], a[156:159], a[28:31], v[188:191]\n"
"ds_read_b64_tr_b16 a[144:145], v40 offset:45056\n"
"v_mfma_f32_16x16x32_bf16 v[156:159], a[156:159], a[24:27], v[156:159]\n"
"ds_read_b64_tr_b16 a[146:147], v40 offset:47104\n"
"v_mfma_f32_16x16x32_bf16 v[124:127], a[156:159], a[20:23], v[124:127]\n"
"buffer_load_dwordx4 a[204:207], v209, s[64:67], s29 offen offset:1024 sc1\n"
"v_mfma_f32_16x16x32_bf16 v[92:95], a[156:159], a[16:19], v[92:95]\n"
"s_add_u32 s29, s29, 0x1000\n"
"s_waitcnt lgkmcnt(8)\n"
"v_mfma_f32_16x16x32_bf16 v[80:83], a[128:131], a[32:35], v[80:83]\n"
"ds_read_b64_tr_b16 a[148:149], v41 offset:45056\n"
"v_mfma_f32_16x16x32_bf16 v[112:115], a[128:131], a[36:39], v[112:115]\n"
"ds_read_b64_tr_b16 a[150:151], v41 offset:47104\n"
"v_mfma_f32_16x16x32_bf16 v[144:147], a[128:131], a[40:43], v[144:147]\n"
"buffer_load_dwordx4 a[208:211], v208, s[64:67], s29 offen sc1\n"
"v_mfma_f32_16x16x32_bf16 v[176:179], a[128:131], a[44:47], v[176:179]\n"
"s_waitcnt lgkmcnt(8)\n"
"v_mfma_f32_16x16x32_bf16 v[180:183], a[132:135], a[44:47], v[180:183]\n"
"ds_read_b64_tr_b16 a[152:153], v42 offset:45056\n"
"v_mfma_f32_16x16x32_bf16 v[148:151], a[132:135], a[40:43], v[148:151]\n"
"ds_read_b64_tr_b16 a[154:155], v42 offset:47104\n"
"v_mfma_f32_16x16x32_bf16 v[116:119], a[132:135], a[36:39], v[116:119]\n"
"buffer_load_dwordx4 a[212:215], v208, s[64:67], s29 offen offset:1024 sc1\n"
"v_mfma_f32_16x16x32_bf16 v[84:87], a[132:135], a[32:35], v[84:87]\n"
"s_waitcnt lgkmcnt(8)\n"
"v_mfma_f32_16x16x32_bf16 v[88:91], a[136:139], a[32:35], v[88:91]\n"
"ds_read_b64_tr_b16 a[156:157], v43 offset:45056\n"
"v_mfma_f32_16x16x32_bf16 v[120:123], a[136:139], a[36:39], v[120:123]\n"
"ds_read_b64_tr_b16 a[158:159], v43 offset:47104\n"
"v_mfma_f32_16x16x32_bf16 v[152:155], a[136:139], a[40:43], v[152:155]\n"
"s_waitcnt vmcnt(32)\n"
"buffer_load_dwordx4 a[216:219], v209, s[64:67], s29 offen sc1\n"
"v_mfma_f32_16x16x32_bf16 v[184:187], a[136:139], a[44:47], v[184:187]\n"
"s_waitcnt lgkmcnt(8)\n"
"v_mfma_f32_16x16x32_bf16 v[188:191], a[140:143], a[44:47], v[188:191]\n"
"ds_read_b64_tr_b16 a[128:129], v40 offset:49152\n"
"v_mfma_f32_16x16x32_bf16 v[156:159], a[140:143], a[40:43], v[156:159]\n"
"ds_read_b64_tr_b16 a[130:131], v40 offset:51200\n"
"v_mfma_f32_16x16x32_bf16 v[124:127], a[140:143], a[36:39], v[124:127]\n"
"buffer_load_dwordx4 a[220:223], v209, s[64:67], s29 offen offset:1024 sc1\n"
"v_mfma_f32_16x16x32_bf16 v[92:95], a[140:143], a[32:35], v[92:95]\n"
"s_add_u32 s29, s29, 0x1000\n"
"s_waitcnt lgkmcnt(8)\n"
"v_mfma_f32_16x16x32_bf16 v[80:83], a[144:147], a[48:51], v[80:83]\n"
"ds_read_b64_tr_b16 a[132:133], v41 offset:49152\n"
"v_mfma_f32_16x16x32_bf16 v[112:115], a[144:147], a[52:55], v[112:115]\n"
"ds_read_b64_tr_b16 a[134:135], v41 offset:51200\n"
"v_mfma_f32_16x16x32_bf16 v[144:147], a[144:147], a[56:59], v[144:147]\n"
"v_mfma_f32_16x16x32_bf16 v[176:179], a[144:147], a[60:63], v[176:179]\n"
"s_waitcnt lgkmcnt(8)\n"
"v_mfma_f32_16x16x32_bf16 v[180:183], a[148:151], a[60:63], v[180:183]\n"
"ds_read_b64_tr_b16 a[136:137], v42 offset:49152\n"
"v_mfma_f32_16x16x32_bf16 v[148:151], a[148:151], a[56:59], v[148:151]\n"
"ds_read_b64_tr_b16 a[138:139], v42 offset:51200\n"
"s_add_u32 m0, s82, 0x14000\n"
"v_mfma_f32_16x16x32_bf16 v[116:119], a[148:151], a[52:55], v[116:119]\n"
"buffer_load_dwordx4 v217, s[60:63], s80 offen lds\n"
"v_mfma_f32_16x16x32_bf16 v[84:87], a[148:151], a[48:51], v[84:87]\n"
"s_waitcnt lgkmcnt(8)\n"
"v_mfma_f32_16x16x32_bf16 v[88:91], a[152:155], a[48:51], v[88:91]\n"
"ds_read_b64_tr_b16 a[140:141], v43 offset:49152\n"
"v_mfma_f32_16x16x32_bf16 v[120:123], a[152:155], a[52:55], v[120:123]\n"
"ds_read_b64_tr_b16 a[142:143], v43 offset:51200\n"
"s_add_u32 m0, s82, 0x14000\n"
"v_mfma_f32_16x16x32_bf16 v[152:155], a[152:155], a[56:59], v[152:155]\n"
"buffer_load_dwordx4 v218, s[60:63], s80 offen offset:1024 lds\n"
"v_mfma_f32_16x16x32_bf16 v[184:187], a[152:155], a[60:63], v[184:187]\n"
"s_waitcnt lgkmcnt(8)\n"
"v_mfma_f32_16x16x32_bf16 v[188:191], a[156:159], a[60:63], v[188:191]\n"
"ds_read_b64_tr_b16 a[144:145], v40 offset:53248\n"
"v_mfma_f32_16x16x32_bf16 v[156:159], a[156:159], a[56:59], v[156:159]\n"
"ds_read_b64_tr_b16 a[146:147], v40 offset:55296\n"
"s_add_u32 m0, s82, 0x14000\n"
"v_mfma_f32_16x16x32_bf16 v[124:127], a[156:159], a[52:55], v[124:127]\n"
"buffer_load_dwordx4 v219, s[60:63], s80 offen offset:2048 lds\n"
"v_mfma_f32_16x16x32_bf16 v[92:95], a[156:159], a[48:51], v[92:95]\n"
"s_waitcnt lgkmcnt(8)\n"
"v_mfma_f32_16x16x32_bf16 v[80:83], a[128:131], a[64:67], v[80:83]\n"
"ds_read_b64_tr_b16 a[148:149], v41 offset:53248\n"
"v_mfma_f32_16x16x32_bf16 v[112:115], a[128:131], a[68:71], v[112:115]\n"
"ds_read_b64_tr_b16 a[150:151], v41 offset:55296\n"
"s_add_u32 m0, s82, 0x14000\n"
"v_mfma_f32_16x16x32_bf16 v[144:147], a[128:131], a[72:75], v[144:147]\n"
"buffer_load_dwordx4 v220, s[60:63], s80 offen offset:3072 lds\n"
"v_mfma_f32_16x16x32_bf16 v[176:179], a[128:131], a[76:79], v[176:179]\n"
"s_add_u32 s80, s80, 0x40000\n"
"s_waitcnt lgkmcnt(8)\n"
"v_mfma_f32_16x16x32_bf16 v[180:183], a[132:135], a[76:79], v[180:183]\n"
"ds_read_b64_tr_b16 a[152:153], v42 offset:53248\n"
"v_mfma_f32_16x16x32_bf16 v[148:151], a[132:135], a[72:75], v[148:151]\n"
"ds_read_b64_tr_b16 a[154:155], v42 offset:55296\n"
"v_mfma_f32_16x16x32_bf16 v[116:119], a[132:135], a[68:71], v[116:119]\n"
"v_mfma_f32_16x16x32_bf16 v[84:87], a[132:135], a[64:67], v[84:87]\n"
"s_waitcnt lgkmcnt(8)\n"
"v_mfma_f32_16x16x32_bf16 v[88:91], a[136:139], a[64:67], v[88:91]\n"
"ds_read_b64_tr_b16 a[156:157], v43 offset:53248\n"
"v_mfma_f32_16x16x32_bf16 v[120:123], a[136:139], a[68:71], v[120:123]\n"
"ds_read_b64_tr_b16 a[158:159], v43 offset:55296\n"
"s_add_u32 m0, s82, 0x20000\n"
"v_mfma_f32_16x16x32_bf16 v[152:155], a[136:139], a[72:75], v[152:155]\n"
"buffer_load_dwordx4 v217, s[60:63], s80 offen lds\n"
"v_mfma_f32_16x16x32_bf16 v[184:187], a[136:139], a[76:79], v[184:187]\n"
"s_waitcnt lgkmcnt(8)\n"
"v_mfma_f32_16x16x32_bf16 v[188:191], a[140:143], a[76:79], v[188:191]\n"
"ds_read_b64_tr_b16 a[128:129], v40 offset:57344\n"
"v_mfma_f32_16x16x32_bf16 v[156:159], a[140:143], a[72:75], v[156:159]\n"
"ds_read_b64_tr_b16 a[130:131], v40 offset:59392\n"
"s_add_u32 m0, s82, 0x20000\n"
"v_mfma_f32_16x16x32_bf16 v[124:127], a[140:143], a[68:71], v[124:127]\n"
"buffer_load_dwordx4 v218, s[60:63], s80 offen offset:1024 lds\n"
"v_mfma_f32_16x16x32_bf16 v[92:95], a[140:143], a[64:67], v[92:95]\n"
"s_waitcnt lgkmcnt(8)\n"
"v_mfma_f32_16x16x32_bf16 v[80:83], a[144:147], a[80:83], v[80:83]\n"
"ds_read_b64_tr_b16 a[132:133], v41 offset:57344\n"
"v_mfma_f32_16x16x32_bf16 v[112:115], a[144:147], a[84:87], v[112:115]\n"
"ds_read_b64_tr_b16 a[134:135], v41 offset:59392\n"
"s_add_u32 m0, s82, 0x20000\n"
"v_mfma_f32_16x16x32_bf16 v[144:147], a[144:147], a[88:91], v[144:147]\n"
"buffer_load_dwordx4 v219, s[60:63], s80 offen offset:2048 lds\n"
"v_mfma_f32_16x16x32_bf16 v[176:179], a[144:147], a[92:95], v[176:179]\n"
"s_waitcnt lgkmcnt(8)\n"
"v_mfma_f32_16x16x32_bf16 v[180:183], a[148:151], a[92:95], v[180:183]\n"
"ds_read_b64_tr_b16 a[136:137], v42 offset:57344\n"
"v_mfma_f32_16x16x32_bf16 v[148:151], a[148:151], a[88:91], v[148:151]\n"
"ds_read_b64_tr_b16 a[138:139], v42 offset:59392\n"
"s_add_u32 m0, s82, 0x20000\n"
"v_mfma_f32_16x16x32_bf16 v[116:119], a[148:151], a[84:87], v[116:119]\n"
"buffer_load_dwordx4 v220, s[60:63], s80 offen offset:3072 lds\n"
"v_mfma_f32_16x16x32_bf16 v[84:87], a[148:151], a[80:83], v[84:87]\n"
"s_add_u32 s80, s80, 0x40000\n"
"s_waitcnt lgkmcnt(8)\n"
"v_mfma_f32_16x16x32_bf16 v[88:91], a[152:155], a[80:83], v[88:91]\n"
"ds_read_b64_tr_b16 a[140:141], v43 offset:57344\n"
"v_mfma_f32_16x16x32_bf16 v[120:123], a[152:155], a[84:87], v[120:123]\n"
"ds_read_b64_tr_b16 a[142:143], v43 offset:59392\n"
"s_add_u32 m0, s82, 0x24000\n"
"v_mfma_f32_16x16x32_bf16 v[152:155], a[152:155], a[88:91], v[152:155]\n"
"buffer_load_dwordx4 v217, s[56:59], s31 offen lds\n"
"v_mfma_f32_16x16x32_bf16 v[184:187], a[152:155], a[92:95], v[184:187]\n"
"s_waitcnt lgkmcnt(8)\n"
"v_mfma_f32_16x16x32_bf16 v[188:191], a[156:159], a[92:95], v[188:191]\n"
"ds_read_b64_tr_b16 a[144:145], v40 offset:61440\n"
"v_mfma_f32_16x16x32_bf16 v[156:159], a[156:159], a[88:91], v[156:159]\n"
"ds_read_b64_tr_b16 a[146:147], v40 offset:63488\n"
"v_mfma_f32_16x16x32_bf16 v[124:127], a[156:159], a[84:87], v[124:127]\n"
"v_mfma_f32_16x16x32_bf16 v[92:95], a[156:159], a[80:83], v[92:95]\n"
"s_waitcnt lgkmcnt(8)\n"
"v_mfma_f32_16x16x32_bf16 v[80:83], a[128:131], a[96:99], v[80:83]\n"
"ds_read_b64_tr_b16 a[148:149], v41 offset:61440\n"
"v_mfma_f32_16x16x32_bf16 v[112:115], a[128:131], a[100:103], v[112:115]\n"
"ds_read_b64_tr_b16 a[150:151], v41 offset:63488\n"
"s_add_u32 m0, s82, 0x24000\n"
"v_mfma_f32_16x16x32_bf16 v[144:147], a[128:131], a[104:107], v[144:147]\n"
"buffer_load_dwordx4 v218, s[56:59], s31 offen offset:1024 lds\n"
"v_mfma_f32_16x16x32_bf16 v[176:179], a[128:131], a[108:111], v[176:179]\n"
"s_waitcnt lgkmcnt(8)\n"
"v_mfma_f32_16x16x32_bf16 v[180:183], a[132:135], a[108:111], v[180:183]\n"
"ds_read_b64_tr_b16 a[152:153], v42 offset:61440\n"
"v_mfma_f32_16x16x32_bf16 v[148:151], a[132:135], a[104:107], v[148:151]\n"
"ds_read_b64_tr_b16 a[154:155], v42 offset:63488\n"
"s_add_u32 m0, s82, 0x24000\n"
"v_mfma_f32_16x16x32_bf16 v[116:119], a[132:135], a[100:103], v[116:119]\n"
"buffer_load_dwordx4 v219, s[56:59], s31 offen offset:2048 lds\n"
"v_mfma_f32_16x16x32_bf16 v[84:87], a[132:135], a[96:99], v[84:87]\n"
"s_waitcnt lgkmcnt(8)\n"
"v_mfma_f32_16x16x32_bf16 v[88:91], a[136:139], a[96:99], v[88:91]\n"
"ds_read_b64_tr_b16 a[156:157], v43 offset:61440\n"
"v_mfma_f32_16x16x32_bf16 v[120:123], a[136:139], a[100:103], v[120:123]\n"
"ds_read_b64_tr_b16 a[158:159], v43 offset:63488\n"
"s_add_u32 m0, s82, 0x24000\n"
"v_mfma_f32_16x16x32_bf16 v[152:155], a[136:139], a[104:107], v[152:155]\n"
"buffer_load_dwordx4 v220, s[56:59], s31 offen offset:3072 lds\n"
"v_mfma_f32_16x16x32_bf16 v[184:187], a[136:139], a[108:111], v[184:187]\n"
"s_add_u32 s31, s31, 0x40000\n"
"s_waitcnt lgkmcnt(8)\n"
"v_mfma_f32_16x16x32_bf16 v[188:191], a[140:143], a[108:111], v[188:191]\n"
"ds_read_b64_tr_b16 a[128:129], v221 offset:32768\n"
"v_mfma_f32_16x16x32_bf16 v[156:159], a[140:143], a[104:107], v[156:159]\n"
"ds_read_b64_tr_b16 a[130:131], v221 offset:34816\n"
"v_mfma_f32_16x16x32_bf16 v[124:127], a[140:143], a[100:103], v[124:127]\n"
"s_waitcnt vmcnt(32)\n"
"buffer_load_dwordx4 a[224:227], v208, s[64:67], s29 offen sc1\n"
"v_mfma_f32_16x16x32_bf16 v[92:95], a[140:143], a[96:99], v[92:95]\n"
"s_waitcnt lgkmcnt(8)\n"
"v_mfma_f32_16x16x32_bf16 v[80:83], a[144:147], a[112:115], v[80:83]\n"
"ds_read_b64_tr_b16 a[132:133], v222 offset:32768\n"
"v_mfma_f32_16x16x32_bf16 v[112:115], a[144:147], a[116:119], v[112:115]\n"
"ds_read_b64_tr_b16 a[134:135], v222 offset:34816\n"
"v_mfma_f32_16x16x32_bf16 v[144:147], a[144:147], a[120:123], v[144:147]\n"
"buffer_load_dwordx4 a[228:231], v208, s[64:67], s29 offen offset:1024 sc1\n"
"v_mfma_f32_16x16x32_bf16 v[176:179], a[144:147], a[124:127], v[176:179]\n"
"s_waitcnt lgkmcnt(8)\n"
"v_mfma_f32_16x16x32_bf16 v[180:183], a[148:151], a[124:127], v[180:183]\n"
"ds_read_b64_tr_b16 a[136:137], v223 offset:32768\n"
"v_mfma_f32_16x16x32_bf16 v[148:151], a[148:151], a[120:123], v[148:151]\n"
"ds_read_b64_tr_b16 a[138:139], v223 offset:34816\n"
"v_mfma_f32_16x16x32_bf16 v[116:119], a[148:151], a[116:119], v[116:119]\n"
"buffer_load_dwordx4 a[232:235], v209, s[64:67], s29 offen sc1\n"
"v_mfma_f32_16x16x32_bf16 v[84:87], a[148:151], a[112:115], v[84:87]\n"
"s_waitcnt lgkmcnt(8)\n"
"v_mfma_f32_16x16x32_bf16 v[88:91], a[152:155], a[112:115], v[88:91]\n"
"ds_read_b64_tr_b16 a[140:141], v239 offset:32768\n"
"v_mfma_f32_16x16x32_bf16 v[120:123], a[152:155], a[116:119], v[120:123]\n"
"ds_read_b64_tr_b16 a[142:143], v239 offset:34816\n"
"v_mfma_f32_16x16x32_bf16 v[152:155], a[152:155], a[120:123], v[152:155]\n"
"v_mfma_f32_16x16x32_bf16 v[184:187], a[152:155], a[124:127], v[184:187]\n"
"s_waitcnt lgkmcnt(8)\n"
"v_mfma_f32_16x16x32_bf16 v[188:191], a[156:159], a[124:127], v[188:191]\n"
".Lpad12_%=:\n"
"s_cbranch_execz .Lback0_%=\n"
"ds_read_b64_tr_b16 a[144:145], v221 offset:36864\n"
"v_mfma_f32_16x16x32_bf16 v[156:159], a[156:159], a[120:123], v[156:159]\n"
"ds_read_b64_tr_b16 a[146:147], v221 offset:38912\n"
"v_mfma_f32_16x16x32_bf16 v[124:127], a[156:159], a[116:119], v[124:127]\n"
"buffer_load_dwordx4 a[236:239], v209, s[64:67], s29 offen offset:1024 sc1\n"
"v_mfma_f32_16x16x32_bf16 v[92:95], a[156:159], a[112:115], v[92:95]\n"
"s_add_u32 s29, s29, 0x1000\n"
"s_barrier\n"
"s_waitcnt lgkmcnt(8)\n"
"v_mfma_f32_16x16x32_bf16 v[80:83], a[128:131], a[160:163], v[80:83]\n"
"ds_read_b64_tr_b16 a[148:149], v222 offset:36864\n"
"v_mfma_f32_16x16x32_bf16 v[112:115], a[128:131], a[164:167], v[112:115]\n"
"ds_read_b64_tr_b16 a[150:151], v222 offset:38912\n"
"s_add_u32 m0, s82, 0\n"
"v_mfma_f32_16x16x32_bf16 v[144:147], a[128:131], a[168:171], v[144:147]\n"
"buffer_load_dwordx4 v217, s[56:59], s79 offen lds\n"
"v_mfma_f32_16x16x32_bf16 v[176:179], a[128:131], a[172:175], v[176:179]\n"
"s_waitcnt lgkmcnt(8)\n"
"v_mfma_f32_16x16x32_bf16 v[180:183], a[132:135], a[172:175], v[180:183]\n"
"ds_read_b64_tr_b16 a[152:153], v223 offset:36864\n"
"v_mfma_f32_16x16x32_bf16 v[148:151], a[132:135], a[168:171], v[148:151]\n"
"ds_read_b64_tr_b16 a[154:155], v223 offset:38912\n"
"s_add_u32 m0, s82, 0\n"
"v_mfma_f32_16x16x32_bf16 v[116:119], a[132:135], a[164:167], v[116:119]\n"
"buffer_load_dwordx4 v218, s[56:59], s79 offen offset:1024 lds\n"
"v_mfma_f32_16x16x32_bf16 v[84:87], a[132:135], a[160:163], v[84:87]\n"
"s_waitcnt lgkmcnt(8)\n"
"v_mfma_f32_16x16x32_bf16 v[88:91], a[136:139], a[160:163], v[88:91]\n"
"ds_read_b64_tr_b16 a[156:157], v239 offset:36864\n"
"v_mfma_f32_16x16x32_bf16 v[120:123], a[136:139], a[164:167], v[120:123]\n"
"ds_read_b64_tr_b16 a[158:159], v239 offset:38912\n"
"v_mfma_f32_16x16x32_bf16 v[152:155], a[136:139], a[168:171], v[152:155]\n"
"v_mfma_f32_16x16x32_bf16 v[184:187], a[136:139], a[172:175], v[184:187]\n"
"s_waitcnt lgkmcnt(8)\n"
"v_mfma_f32_16x16x32_bf16 v[188:191], a[140:143], a[172:175], v[188:191]\n"
"ds_read_b64_tr_b16 a[128:129], v221 offset:40960\n"
"v_mfma_f32_16x16x32_bf16 v[156:159], a[140:143], a[168:171], v[156:159]\n"
"ds_read_b64_tr_b16 a[130:131], v221 offset:43008\n"
"s_add_u32 m0, s82, 0\n"
"v_mfma_f32_16x16x32_bf16 v[124:127], a[140:143], a[164:167], v[124:127]\n"
"buffer_load_dwordx4 v219, s[56:59], s79 offen offset:2048 lds\n"
"v_mfma_f32_16x16x32_bf16 v[92:95], a[140:143], a[160:163], v[92:95]\n"
"s_waitcnt lgkmcnt(8)\n"
"v_mfma_f32_16x16x32_bf16 v[80:83], a[144:147], v[0:3], v[80:83]\n"
"ds_read_b64_tr_b16 a[132:133], v222 offset:40960\n"
"v_mfma_f32_16x16x32_bf16 v[112:115], a[144:147], v[4:7], v[112:115]\n"
"ds_read_b64_tr_b16 a[134:135], v222 offset:43008\n"
"s_add_u32 m0, s82, 0\n"
"v_mfma_f32_16x16x32_bf16 v[144:147], a[144:147], v[8:11], v[144:147]\n"
"buffer_load_dwordx4 v220, s[56:59], s79 offen offset:3072 lds\n"
"v_mfma_f32_16x16x32_bf16 v[176:179], a[144:147], v[12:15], v[176:179]\n"
"s_add_u32 s79, s79, 0x40000\n"
"s_waitcnt lgkmcnt(8)\n"
"v_mfma_f32_16x16x32_bf16 v[180:183], a[148:151], v[12:15], v[180:183]\n"
"ds_read_b64_tr_b16 a[136:137], v223 offset:40960\n"
"v_mfma_f32_16x16x32_bf16 v[148:151], a[148:151], v[8:11], v[148:151]\n"
"ds_read_b64_tr_b16 a[138:139], v223 offset:43008\n"
"s_add_u32 m0, s82, 0x4000\n"
"v_mfma_f32_16x16x32_bf16 v[116:119], a[148:151], v[4:7], v[116:119]\n"
"buffer_load_dwordx4 v217, s[56:59], s79 offen lds\n"
"v_mfma_f32_16x16x32_bf16 v[84:87], a[148:151], v[0:3], v[84:87]\n"
"s_waitcnt lgkmcnt(8)\n"
"v_mfma_f32_16x16x32_bf16 v[88:91], a[152:155], v[0:3], v[88:91]\n"
"ds_read_b64_tr_b16 a[140:141], v239 offset:40960\n"
"v_mfma_f32_16x16x32_bf16 v[120:123], a[152:155], v[4:7], v[120:123]\n"
"ds_read_b64_tr_b16 a[142:143], v239 offset:43008\n"
"s_add_u32 m0, s82, 0x4000\n"
"v_mfma_f32_16x16x32_bf16 v[152:155], a[152:155], v[8:11], v[152:155]\n"
"buffer_load_dwordx4 v218, s[56:59], s79 offen offset:1024 lds\n"
"v_mfma_f32_16x16x32_bf16 v[184:187], a[152:155], v[12:15], v[184:187]\n"
"s_waitcnt lgkmcnt(8)\n"
"v_mfma_f32_16x16x32_bf16 v[188:191], a[156:159], v[12:15], v[188:191]\n"
"ds_read_b64_tr_b16 a[144:145], v221 offset:45056\n"
"v_mfma_f32_16x16x32_bf16 v[156:159], a[156:159], v[8:11], v[156:159]\n"
"ds_read_b64_tr_b16 a[146:147], v221 offset:47104\n"
"s_add_u32 m0, s82, 0x4000\n"
"v_mfma_f32_16x16x32_bf16 v[124:127], a[156:159], v[4:7], v[124:127]\n"
"buffer_load_dwordx4 v219, s[56:59], s79 offen offset:2048 lds\n"
"v_mfma_f32_16x16x32_bf16 v[92:95], a[156:159], v[0:3], v[92:95]\n"
"s_waitcnt vmcnt(39) lgkmcnt(8)\n"
"v_mfma_f32_16x16x32_bf16 v[80:83], a[128:131], v[192:195], v[80:83]\n"
"ds_read_b64_tr_b16 a[148:149], v222 offset:45056\n"
"v_mfma_f32_16x16x32_bf16 v[112:115], a[128:131], v[196:199], v[112:115]\n"
"ds_read_b64_tr_b16 a[150:151], v222 offset:47104\n"
"v_mfma_f32_16x16x32_bf16 v[144:147], a[128:131], v[200:203], v[144:147]\n"
"v_mfma_f32_16x16x32_bf16 v[176:179], a[128:131], v[204:207], v[176:179]\n"
"s_waitcnt lgkmcnt(8)\n"
"v_mfma_f32_16x16x32_bf16 v[180:183], a[132:135], v[204:207], v[180:183]\n"
"ds_read_b64_tr_b16 a[152:153], v223 offset:45056\n"
"v_mfma_f32_16x16x32_bf16 v[148:151], a[132:135], v[200:203], v[148:151]\n"
"ds_read_b64_tr_b16 a[154:155], v223 offset:47104\n"
"s_add_u32 m0, s82, 0x4000\n"
"v_mfma_f32_16x16x32_bf16 v[116:119], a[132:135], v[196:199], v[116:119]\n"
"buffer_load_dwordx4 v220, s[56:59], s79 offen offset:3072 lds\n"
"v_mfma_f32_16x16x32_bf16 v[84:87], a[132:135], v[192:195], v[84:87]\n"
"s_add_u32 s79, s79, 0x40000\n"
"s_waitcnt lgkmcnt(8)\n"
"v_mfma_f32_16x16x32_bf16 v[88:91], a[136:139], v[192:195], v[88:91]\n"
"ds_read_b64_tr_b16 a[156:157], v239 offset:45056\n"
"v_mfma_f32_16x16x32_bf16 v[120:123], a[136:139], v[196:199], v[120:123]\n"
"ds_read_b64_tr_b16 a[158:159], v239 offset:47104\n"
"s_add_u32 m0, s82, 0x8000\n"
"v_mfma_f32_16x16x32_bf16 v[152:155], a[136:139], v[200:203], v[152:155]\n"
"buffer_load_dwordx4 v217, s[56:59], s79 offen lds\n"
"v_mfma_f32_16x16x32_bf16 v[184:187], a[136:139], v[204:207], v[184:187]\n"
"s_waitcnt lgkmcnt(8)\n"
"v_mfma_f32_16x16x32_bf16 v[188:191], a[140:143], v[204:207], v[188:191]\n"
"ds_read_b64_tr_b16 a[128:129], v221 offset:49152\n"
"v_mfma_f32_16x16x32_bf16 v[156:159], a[140:143], v[200:203], v[156:159]\n"
"ds_read_b64_tr_b16 a[130:131], v221 offset:51200\n"
"s_add_u32 m0, s82, 0x8000\n"
"v_mfma_f32_16x16x32_bf16 v[124:127], a[140:143], v[196:199], v[124:127]\n"
"buffer_load_dwordx4 v218, s[56:59], s79 offen offset:1024 lds\n"
"v_mfma_f32_16x16x32_bf16 v[92:95], a[140:143], v[192:195], v[92:95]\n"
"s_waitcnt vmcnt(38) lgkmcnt(8)\n"
"v_mfma_f32_16x16x32_bf16 v[80:83], a[144:147], v[240:243], v[80:83]\n"
"ds_read_b64_tr_b16 a[132:133], v222 offset:49152\n"
"v_mfma_f32_16x16x32_bf16 v[112:115], a[144:147], v[244:247], v[112:115]\n"
"ds_read_b64_tr_b16 a[134:135], v222 offset:51200\n"
"s_add_u32 m0, s82, 0x8000\n"
"v_mfma_f32_16x16x32_bf16 v[144:147], a[144:147], v[248:251], v[144:147]\n"
"buffer_load_dwordx4 v219, s[56:59], s79 offen offset:2048 lds\n"
"v_mfma_f32_16x16x32_bf16 v[176:179], a[144:147], v[252:255], v[176:179]\n"
"s_waitcnt lgkmcnt(8)\n"
"v_mfma_f32_16x16x32_bf16 v[180:183], a[148:151], v[252:255], v[180:183]\n"
"ds_read_b64_tr_b16 a[136:137], v223 offset:49152\n"
"v_mfma_f32_16x16x32_bf16 v[148:151], a[148:151], v[248:251], v[148:151]\n"
"ds_read_b64_tr_b16 a[138:139], v223 offset:51200\n"
"s_add_u32 m0, s82, 0x8000\n"
"v_mfma_f32_16x16x32_bf16 v[116:119], a[148:151], v[244:247], v[116:119]\n"
"buffer_load_dwordx4 v220, s[56:59], s79 offen offset:3072 lds\n"
"v_mfma_f32_16x16x32_bf16 v[84:87], a[148:151], v[240:243], v[84:87]\n"
"s_add_u32 s79, s79, 0x40000\n"
"s_waitcnt lgkmcnt(8)\n"
"v_mfma_f32_16x16x32_bf16 v[88:91], a[152:155], v[240:243], v[88:91]\n"
"ds_read_b64_tr_b16 a[140:141], v239 offset:49152\n"
"v_mfma_f32_16x16x32_bf16 v[120:123], a[152:155], v[244:247], v[120:123]\n"
"ds_read_b64_tr_b16 a[142:143], v239 offset:51200\n"
"v_mfma_f32_16x16x32_bf16 v[152:155], a[152:155], v[248:251], v[152:155]\n"
"v_mfma_f32_16x16x32_bf16 v[184:187], a[152:155], v[252:255], v[184:187]\n"
"s_waitcnt lgkmcnt(8)\n"
"v_mfma_f32_16x16x32_bf16 v[188:191], a[156:159], v[252:255], v[188:191]\n"
"ds_read_b64_tr_b16 a[144:145], v221 offset:53248\n"
"v_mfma_f32_16x16x32_bf16 v[156:159], a[156:159], v[248:251], v[156:159]\n"
"ds_read_b64_tr_b16 a[146:147], v221 offset:55296\n"
"s_add_u32 m0, s82, 0xc000\n"
"v_mfma_f32_16x16x32_bf16 v[124:127], a[156:159], v[244:247], v[124:127]\n"
"buffer_load_dwordx4 v217, s[56:59], s79 offen lds\n"
"v_mfma_f32_16x16x32_bf16 v[92:95], a[156:159], v[240:243], v[92:95]\n"
"s_waitcnt vmcnt(37) lgkmcnt(8)\n"
"v_mfma_f32_16x16x32_bf16 v[80:83], a[128:131], a[176:179], v[80:83]\n"
"ds_read_b64_tr_b16 a[148:149], v222 offset:53248\n"
"v_mfma_f32_16x16x32_bf16 v[112:115], a[128:131], a[180:183], v[112:115]\n"
"ds_read_b64_tr_b16 a[150:151], v222 offset:55296\n"
"s_add_u32 m0, s82, 0xc000\n"
"v_mfma_f32_16x16x32_bf16 v[144:147], a[128:131], a[184:187], v[144:147]\n"
"buffer_load_dwordx4 v218, s[56:59], s79 offen offset:1024 lds\n"
"v_mfma_f32_16x16x32_bf16 v[176:179], a[128:131], a[188:191], v[176:179]\n"
"s_waitcnt lgkmcnt(8)\n"
"v_mfma_f32_16x16x32_bf16 v[180:183], a[132:135], a[188:191], v[180:183]\n"
"ds_read_b64_tr_b16 a[152:153], v223 offset:53248\n"
"v_mfma_f32_16x16x32_bf16 v[148:151], a[132:135], a[184:187], v[148:151]\n"
"ds_read_b64_tr_b16 a[154:155], v223 offset:55296\n"
"s_add_u32 m0, s82, 0xc000\n"
"v_mfma_f32_16x16x32_bf16 v[116:119], a[132:135], a[180:183], v[116:119]\n"
"buffer_load_dwordx4 v219, s[56:59], s79 offen offset:2048 lds\n"
"v_mfma_f32_16x16x32_bf16 v[84:87], a[132:135], a[176:179], v[84:87]\n"
"s_waitcnt lgkmcnt(8)\n"
"v_mfma_f32_16x16x32_bf16 v[88:91], a[136:139], a[176:179], v[88:91]\n"
"ds_read_b64_tr_b16 a[156:157], v239 offset:53248\n"
"v_mfma_f32_16x16x32_bf16 v[120:123], a[136:139], a[180:183], v[120:123]\n"
"ds_read_b64_tr_b16 a[158:159], v239 offset:55296\n"
"s_add_u32 m0, s82, 0xc000\n"
"v_mfma_f32_16x16x32_bf16 v[152:155], a[136:139], a[184:187], v[152:155]\n"
"buffer_load_dwordx4 v220, s[56:59], s79 offen offset:3072 lds\n"
"v_mfma_f32_16x16x32_bf16 v[184:187], a[136:139], a[188:191], v[184:187]\n"
"s_add_u32 s79, s79, 0x40000\n"
"s_waitcnt lgkmcnt(8)\n"
"v_mfma_f32_16x16x32_bf16 v[188:191], a[140:143], a[188:191], v[188:191]\n"
"ds_read_b64_tr_b16 a[128:129], v221 offset:57344\n"
"v_mfma_f32_16x16x32_bf16 v[156:159], a[140:143], a[184:187], v[156:159]\n"
"ds_read_b64_tr_b16 a[130:131], v221 offset:59392\n"
"v_mfma_f32_16x16x32_bf16 v[124:127], a[140:143], a[180:183], v[124:127]\n"
"buffer_load_dwordx4 a[240:243], v208, s[64:67], s95 offen sc1\n"
"v_mfma_f32_16x16x32_bf16 v[92:95], a[140:143], a[176:179], v[92:95]\n"
"s_waitcnt vmcnt(37) lgkmcnt(8)\n"
"v_mfma_f32_16x16x32_bf16 v[80:83], a[144:147], a[192:195], v[80:83]\n"
"ds_read_b64_tr_b16 a[132:133], v222 offset:57344\n"
"v_mfma_f32_16x16x32_bf16 v[112:115], a[144:147], a[196:199], v[112:115]\n"
"ds_read_b64_tr_b16 a[134:135], v222 offset:59392\n"
"v_mfma_f32_16x16x32_bf16 v[144:147], a[144:147], a[200:203], v[144:147]\n"
"buffer_load_dwordx4 a[244:247], v208, s[64:67], s95 offen offset:1024 sc1\n"
"v_mfma_f32_16x16x32_bf16 v[176:179], a[144:147], a[204:207], v[176:179]\n"
"s_add_u32 s95, s95, 0x1000\n"
"s_waitcnt lgkmcnt(8)\n"
"v_mfma_f32_16x16x32_bf16 v[180:183], a[148:151], a[204:207], v[180:183]\n"
"ds_read_b64_tr_b16 a[136:137], v223 offset:57344\n"
"v_mfma_f32_16x16x32_bf16 v[148:151], a[148:151], a[200:203], v[148:151]\n"
"ds_read_b64_tr_b16 a[138:139], v223 offset:59392\n"
"v_mfma_f32_16x16x32_bf16 v[116:119], a[148:151], a[196:199], v[116:119]\n"
"v_mfma_f32_16x16x32_bf16 v[84:87], a[148:151], a[192:195], v[84:87]\n"
"s_waitcnt lgkmcnt(8)\n"
"v_mfma_f32_16x16x32_bf16 v[88:91], a[152:155], a[192:195], v[88:91]\n"
"ds_read_b64_tr_b16 a[140:141], v239 offset:57344\n"
"v_mfma_f32_16x16x32_bf16 v[120:123], a[152:155], a[196:199], v[120:123]\n"
"ds_read_b64_tr_b16 a[142:143], v239 offset:59392\n"
"v_mfma_f32_16x16x32_bf16 v[152:155], a[152:155], a[200:203], v[152:155]\n"
"buffer_load_dwordx4 a[248:251], v208, s[64:67], s95 offen sc1\n"
"v_mfma_f32_16x16x32_bf16 v[184:187], a[152:155], a[204:207], v[184:187]\n"
"s_waitcnt lgkmcnt(8)\n"
"v_mfma_f32_16x16x32_bf16 v[188:191], a[156:159], a[204:207], v[188:191]\n"
"ds_read_b64_tr_b16 a[144:145], v221 offset:61440\n"
"v_mfma_f32_16x16x32_bf16 v[156:159], a[156:159], a[200:203], v[156:159]\n"
"ds_read_b64_tr_b16 a[146:147], v221 offset:63488\n"
"v_mfma_f32_16x16x32_bf16 v[124:127], a[156:159], a[196:199], v[124:127]\n"
"buffer_load_dwordx4 a[252:255], v208, s[64:67], s95 offen offset:1024 sc1\n"
"v_mfma_f32_16x16x32_bf16 v[92:95], a[156:159], a[192:195], v[92:95]\n"
"s_add_u32 s95, s95, 0x1000\n"
"s_waitcnt vmcnt(36) lgkmcnt(8)\n"
"v_mfma_f32_16x16x32_bf16 v[80:83], a[128:131], a[208:211], v[80:83]\n"
"ds_read_b64_tr_b16 a[148:149], v222 offset:61440\n"
"v_mfma_f32_16x16x32_bf16 v[112:115], a[128:131], a[212:215], v[112:115]\n"
"ds_read_b64_tr_b16 a[150:151], v222 offset:63488\n"
"v_mfma_f32_16x16x32_bf16 v[144:147], a[128:131], a[216:219], v[144:147]\n"
"buffer_load_dwordx4 v[16:19], v208, s[64:67], s95 offen sc1\n"
"v_mfma_f32_16x16x32_bf16 v[176:179], a[128:131], a[220:223], v[176:179]\n"
"s_waitcnt lgkmcnt(8)\n"
"v_mfma_f32_16x16x32_bf16 v[180:183], a[132:135], a[220:223], v[180:183]\n"
"ds_read_b64_tr_b16 a[152:153], v223 offset:61440\n"
"v_mfma_f32_16x16x32_bf16 v[148:151], a[132:135], a[216:219], v[148:151]\n"
"ds_read_b64_tr_b16 a[154:155], v223 offset:63488\n"
"v_mfma_f32_16x16x32_bf16 v[116:119], a[132:135], a[212:215], v[116:119]\n"
"buffer_load_dwordx4 v[20:23], v208, s[64:67], s95 offen offset:1024 sc1\n"
"v_mfma_f32_16x16x32_bf16 v[84:87], a[132:135], a[208:211], v[84:87]\n"
"s_add_u32 s95, s95, 0x1000\n"
"s_waitcnt lgkmcnt(8)\n"
"v_mfma_f32_16x16x32_bf16 v[88:91], a[136:139], a[208:211], v[88:91]\n"
"ds_read_b64_tr_b16 a[156:157], v239 offset:61440\n"
"s_lshl_b32 s93, s86, 14\n"
"v_mfma_f32_16x16x32_bf16 v[120:123], a[136:139], a[212:215], v[120:123]\n"
"ds_read_b64_tr_b16 a[158:159], v239 offset:63488\n"
"s_add_u32 s93, s93, 0x2800000\n"
"v_mfma_f32_16x16x32_bf16 v[152:155], a[136:139], a[216:219], v[152:155]\n"
"buffer_load_dwordx4 v[24:27], v208, s[64:67], s95 offen sc1\n"
"s_add_u32 s90, s93, 0x3000\n"
"v_mfma_f32_16x16x32_bf16 v[184:187], a[136:139], a[220:223], v[184:187]\n"
"s_waitcnt lgkmcnt(8)\n"
"s_lshl_b32 s55, s86, 7\n"
"v_mfma_f32_16x16x32_bf16 v[188:191], a[140:143], a[220:223], v[188:191]\n"
"ds_read_b64_tr_b16 a[128:129], v221\n"
"s_add_u32 s55, s55, 0x2100000\n"
"v_mfma_f32_16x16x32_bf16 v[156:159], a[140:143], a[216:219], v[156:159]\n"
"ds_read_b64_tr_b16 a[130:131], v221 offset:2048\n"
"v_and_b32 v60, 3, v236\n"
"v_mfma_f32_16x16x32_bf16 v[124:127], a[140:143], a[212:215], v[124:127]\n"
"v_max_u32 v61, 2, v60\n"
"v_mfma_f32_16x16x32_bf16 v[92:95], a[140:143], a[208:211], v[92:95]\n"
"s_waitcnt vmcnt(23) lgkmcnt(8)\n"
"v_lshlrev_b32 v61, 10, v61\n"
"v_mfma_f32_16x16x32_bf16 v[80:83], a[144:147], a[224:227], v[80:83]\n"
"ds_read_b64_tr_b16 a[132:133], v222\n"
"v_xor_b32 v61, s55, v61\n"
"v_mfma_f32_16x16x32_bf16 v[112:115], a[144:147], a[228:231], v[112:115]\n"
"ds_read_b64_tr_b16 a[134:135], v222 offset:2048\n"
"v_max_u32 v60, 1, v60\n"
"v_mfma_f32_16x16x32_bf16 v[144:147], a[144:147], a[232:235], v[144:147]\n"
"buffer_load_dwordx4 v[28:31], v208, s[64:67], s95 offen offset:1024 sc1\n"
"v_min_u32 v60, 3, v60\n"
"v_mfma_f32_16x16x32_bf16 v[176:179], a[144:147], a[236:239], v[176:179]\n"
"s_add_u32 s95, s95, 0x1000\n"
"s_waitcnt lgkmcnt(8)\n"
"v_mfma_f32_16x16x32_bf16 v[180:183], a[148:151], a[236:239], v[180:183]\n"
"ds_read_b64_tr_b16 a[136:137], v223\n"
"v_lshlrev_b32 v60, 10, v60\n"
"v_mfma_f32_16x16x32_bf16 v[148:151], a[148:151], a[232:235], v[148:151]\n"
"ds_read_b64_tr_b16 a[138:139], v223 offset:2048\n"
"v_xor_b32 v238, s55, v60\n"
"v_mfma_f32_16x16x32_bf16 v[116:119], a[148:151], a[228:231], v[116:119]\n"
"buffer_load_dwordx4 v[32:35], v208, s[64:67], s95 offen sc1\n"
"v_mfma_f32_16x16x32_bf16 v[84:87], a[148:151], a[224:227], v[84:87]\n"
"s_waitcnt lgkmcnt(8)\n"
"v_mfma_f32_16x16x32_bf16 v[88:91], a[152:155], a[224:227], v[88:91]\n"
"ds_read_b64_tr_b16 a[140:141], v239\n"
"v_mfma_f32_16x16x32_bf16 v[120:123], a[152:155], a[228:231], v[120:123]\n"
"ds_read_b64_tr_b16 a[142:143], v239 offset:2048\n"
"v_mfma_f32_16x16x32_bf16 v[152:155], a[152:155], a[232:235], v[152:155]\n"
"buffer_load_dwordx4 v[36:39], v208, s[64:67], s95 offen offset:1024 sc1\n"
"v_mfma_f32_16x16x32_bf16 v[184:187], a[152:155], a[236:239], v[184:187]\n"
"s_add_u32 s95, s95, 0x1000\n"
"s_waitcnt lgkmcnt(8)\n"
"v_mfma_f32_16x16x32_bf16 v[188:191], a[156:159], a[236:239], v[188:191]\n"
"ds_read_b64_tr_b16 a[144:145], v221 offset:4096\n"
"v_mfma_f32_16x16x32_bf16 v[156:159], a[156:159], a[232:235], v[156:159]\n"
"ds_read_b64_tr_b16 a[146:147], v221 offset:6144\n"
"v_mfma_f32_16x16x32_bf16 v[124:127], a[156:159], a[228:231], v[124:127]\n"
"buffer_load_dwordx4 v[48:51], v208, s[64:67], s95 offen sc1\n"
"v_mfma_f32_16x16x32_bf16 v[92:95], a[156:159], a[224:227], v[92:95]\n"
"s_waitcnt vmcnt(11)\n"
"s_barrier\n"
"s_waitcnt lgkmcnt(8)\n"
"v_mfma_f32_16x16x32_bf16 v[128:131], a[128:131], a[8:11], v[128:131]\n"
"ds_read_b64_tr_b16 a[148:149], v222 offset:4096\n"
"v_mfma_f32_16x16x32_bf16 v[160:163], a[128:131], a[12:15], v[160:163]\n"
"ds_read_b64_tr_b16 a[150:151], v222 offset:6144\n"
"s_waitcnt lgkmcnt(8)\n"
"v_mfma_f32_16x16x32_bf16 v[164:167], a[132:135], a[12:15], v[164:167]\n"
"ds_read_b64_tr_b16 a[152:153], v223 offset:4096\n"
"v_mfma_f32_16x16x32_bf16 v[132:135], a[132:135], a[8:11], v[132:135]\n"
"ds_read_b64_tr_b16 a[154:155], v223 offset:6144\n"
"s_waitcnt lgkmcnt(6)\n"
"v_mfma_f32_16x16x32_bf16 v[136:139], a[136:139], a[8:11], v[136:139]\n"
"ds_read_b64_tr_b16 a[156:157], v239 offset:4096\n"
"v_mfma_f32_16x16x32_bf16 v[168:171], a[136:139], a[12:15], v[168:171]\n"
"ds_read_b64_tr_b16 a[158:159], v239 offset:6144\n"
"v_mfma_f32_16x16x32_bf16 v[172:175], a[140:143], a[12:15], v[172:175]\n"
"ds_read_b64_tr_b16 a[128:129], v221 offset:8192\n"
"v_cvt_pk_bf16_f32 v144, v144, v145\n"
"v_mfma_f32_16x16x32_bf16 v[140:143], a[140:143], a[8:11], v[140:143]\n"
"ds_read_b64_tr_b16 a[130:131], v221 offset:10240\n"
"s_waitcnt lgkmcnt(6)\n"
"v_mfma_f32_16x16x32_bf16 v[128:131], a[144:147], a[24:27], v[128:131]\n"
"ds_read_b64_tr_b16 a[132:133], v222 offset:8192\n"
"v_cvt_pk_bf16_f32 v145, v146, v147\n"
"v_mfma_f32_16x16x32_bf16 v[160:163], a[144:147], a[28:31], v[160:163]\n"
"ds_read_b64_tr_b16 a[134:135], v222 offset:10240\n"
"v_cvt_pk_bf16_f32 v146, v148, v149\n"
"v_mfma_f32_16x16x32_bf16 v[164:167], a[148:151], a[28:31], v[164:167]\n"
"ds_read_b64_tr_b16 a[136:137], v223 offset:8192\n"
"v_cvt_pk_bf16_f32 v147, v150, v151\n"
"v_mfma_f32_16x16x32_bf16 v[132:135], a[148:151], a[24:27], v[132:135]\n"
"ds_read_b64_tr_b16 a[138:139], v223 offset:10240\n"
"s_waitcnt lgkmcnt(6)\n"
"v_mfma_f32_16x16x32_bf16 v[136:139], a[152:155], a[24:27], v[136:139]\n"
"ds_read_b64_tr_b16 a[140:141], v239 offset:8192\n"
"buffer_store_dwordx4 v[144:147], v224, s[64:67], s90 offen sc1\n"
"v_mfma_f32_16x16x32_bf16 v[168:171], a[152:155], a[28:31], v[168:171]\n"
"ds_read_b64_tr_b16 a[142:143], v239 offset:10240\n"
"v_cvt_pk_bf16_f32 v152, v152, v153\n"
"v_mfma_f32_16x16x32_bf16 v[172:175], a[156:159], a[28:31], v[172:175]\n"
"ds_read_b64_tr_b16 a[144:145], v221 offset:12288\n"
"v_cvt_pk_bf16_f32 v153, v154, v155\n"
"v_mfma_f32_16x16x32_bf16 v[140:143], a[156:159], a[24:27], v[140:143]\n"
"ds_read_b64_tr_b16 a[146:147], v221 offset:14336\n"
"s_waitcnt lgkmcnt(6)\n"
"v_mfma_f32_16x16x32_bf16 v[128:131], a[128:131], a[40:43], v[128:131]\n"
"ds_read_b64_tr_b16 a[148:149], v222 offset:12288\n"
"v_cvt_pk_bf16_f32 v154, v156, v157\n"
"v_mfma_f32_16x16x32_bf16 v[160:163], a[128:131], a[44:47], v[160:163]\n"
"ds_read_b64_tr_b16 a[150:151], v222 offset:14336\n"
"v_cvt_pk_bf16_f32 v155, v158, v159\n"
"v_mfma_f32_16x16x32_bf16 v[164:167], a[132:135], a[44:47], v[164:167]\n"
"ds_read_b64_tr_b16 a[152:153], v223 offset:12288\n"
"buffer_store_dwordx4 v[152:155], v224, s[64:67], s90 offen offset:1024 sc1\n"
"v_mfma_f32_16x16x32_bf16 v[132:135], a[132:135], a[40:43], v[132:135]\n"
"ds_read_b64_tr_b16 a[154:155], v223 offset:14336\n"
"s_waitcnt lgkmcnt(6)\n"
"v_mfma_f32_16x16x32_bf16 v[136:139], a[136:139], a[40:43], v[136:139]\n"
"ds_read_b64_tr_b16 a[156:157], v239 offset:12288\n"
"v_cvt_pk_bf16_f32 v176, v176, v177\n"
"v_mfma_f32_16x16x32_bf16 v[168:171], a[136:139], a[44:47], v[168:171]\n"
"ds_read_b64_tr_b16 a[158:159], v239 offset:14336\n"
"v_cvt_pk_bf16_f32 v177, v178, v179\n"
"v_mfma_f32_16x16x32_bf16 v[172:175], a[140:143], a[44:47], v[172:175]\n"
"ds_read_b64_tr_b16 a[128:129], v221 offset:16384\n"
"v_cvt_pk_bf16_f32 v178, v180, v181\n"
"v_mfma_f32_16x16x32_bf16 v[140:143], a[140:143], a[40:43], v[140:143]\n"
"ds_read_b64_tr_b16 a[130:131], v221 offset:18432\n"
"s_waitcnt lgkmcnt(6)\n"
"v_mfma_f32_16x16x32_bf16 v[128:131], a[144:147], a[56:59], v[128:131]\n"
"ds_read_b64_tr_b16 a[132:133], v222 offset:16384\n"
"v_cvt_pk_bf16_f32 v179, v182, v183\n"
"v_mfma_f32_16x16x32_bf16 v[160:163], a[144:147], a[60:63], v[160:163]\n"
"ds_read_b64_tr_b16 a[134:135], v222 offset:18432\n"
"v_mfma_f32_16x16x32_bf16 v[164:167], a[148:151], a[60:63], v[164:167]\n"
"ds_read_b64_tr_b16 a[136:137], v223 offset:16384\n"
"buffer_store_dwordx4 v[176:179], v224, s[64:67], s90 offen offset:2048 sc1\n"
"v_mfma_f32_16x16x32_bf16 v[132:135], a[148:151], a[56:59], v[132:135]\n"
"ds_read_b64_tr_b16 a[138:139], v223 offset:18432\n"
"s_waitcnt lgkmcnt(6)\n"
"v_mfma_f32_16x16x32_bf16 v[136:139], a[152:155], a[56:59], v[136:139]\n"
"ds_read_b64_tr_b16 a[140:141], v239 offset:16384\n"
"v_cvt_pk_bf16_f32 v184, v184, v185\n"
"v_mfma_f32_16x16x32_bf16 v[168:171], a[152:155], a[60:63], v[168:171]\n"
"ds_read_b64_tr_b16 a[142:143], v239 offset:18432\n"
"v_cvt_pk_bf16_f32 v185, v186, v187\n"
"v_mfma_f32_16x16x32_bf16 v[172:175], a[156:159], a[60:63], v[172:175]\n"
"ds_read_b64_tr_b16 a[144:145], v221 offset:20480\n"
"v_cvt_pk_bf16_f32 v186, v188, v189\n"
"v_mfma_f32_16x16x32_bf16 v[140:143], a[156:159], a[56:59], v[140:143]\n"
"ds_read_b64_tr_b16 a[146:147], v221 offset:22528\n"
"s_waitcnt lgkmcnt(6)\n"
"v_cvt_pk_bf16_f32 v187, v190, v191\n"
"v_mfma_f32_16x16x32_bf16 v[128:131], a[128:131], a[72:75], v[128:131]\n"
"ds_read_b64_tr_b16 a[148:149], v222 offset:20480\n"
"buffer_store_dwordx4 v[184:187], v224, s[64:67], s90 offen offset:3072 sc1\n"
"v_mfma_f32_16x16x32_bf16 v[160:163], a[128:131], a[76:79], v[160:163]\n"
"ds_read_b64_tr_b16 a[150:151], v222 offset:22528\n"
"s_sub_u32 s90, s90, 0x1000\n"
"v_mfma_f32_16x16x32_bf16 v[164:167], a[132:135], a[76:79], v[164:167]\n"
"ds_read_b64_tr_b16 a[152:153], v223 offset:20480\n"
"v_cvt_pk_bf16_f32 v80, v80, v81\n"
"v_mfma_f32_16x16x32_bf16 v[132:135], a[132:135], a[72:75], v[132:135]\n"
"ds_read_b64_tr_b16 a[154:155], v223 offset:22528\n"
"s_waitcnt lgkmcnt(6)\n"
"v_cvt_pk_bf16_f32 v81, v82, v83\n"
"v_mfma_f32_16x16x32_bf16 v[136:139], a[136:139], a[72:75], v[136:139]\n"
"ds_read_b64_tr_b16 a[156:157], v239 offset:20480\n"
"v_cvt_pk_bf16_f32 v82, v84, v85\n"
"v_mfma_f32_16x16x32_bf16 v[168:171], a[136:139], a[76:79], v[168:171]\n"
"ds_read_b64_tr_b16 a[158:159], v239 offset:22528\n"
"v_cvt_pk_bf16_f32 v83, v86, v87\n"
"v_mfma_f32_16x16x32_bf16 v[172:175], a[140:143], a[76:79], v[172:175]\n"
"ds_read_b64_tr_b16 a[128:129], v221 offset:24576\n"
"buffer_store_dwordx4 v[80:83], v224, s[64:67], s90 offen sc1\n"
"v_mfma_f32_16x16x32_bf16 v[140:143], a[140:143], a[72:75], v[140:143]\n"
"ds_read_b64_tr_b16 a[130:131], v221 offset:26624\n"
"s_waitcnt lgkmcnt(6)\n"
"v_cvt_pk_bf16_f32 v88, v88, v89\n"
"v_mfma_f32_16x16x32_bf16 v[128:131], a[144:147], a[88:91], v[128:131]\n"
"ds_read_b64_tr_b16 a[132:133], v222 offset:24576\n"
"v_cvt_pk_bf16_f32 v89, v90, v91\n"
"v_mfma_f32_16x16x32_bf16 v[160:163], a[144:147], a[92:95], v[160:163]\n"
"ds_read_b64_tr_b16 a[134:135], v222 offset:26624\n"
"v_cvt_pk_bf16_f32 v90, v92, v93\n"
"v_mfma_f32_16x16x32_bf16 v[164:167], a[148:151], a[92:95], v[164:167]\n"
"ds_read_b64_tr_b16 a[136:137], v223 offset:24576\n"
"v_cvt_pk_bf16_f32 v91, v94, v95\n"
"v_mfma_f32_16x16x32_bf16 v[132:135], a[148:151], a[88:91], v[132:135]\n"
"ds_read_b64_tr_b16 a[138:139], v223 offset:26624\n"
"s_waitcnt lgkmcnt(6)\n"
"v_mfma_f32_16x16x32_bf16 v[136:139], a[152:155], a[88:91], v[136:139]\n"
"ds_read_b64_tr_b16 a[140:141], v239 offset:24576\n"
"buffer_store_dwordx4 v[88:91], v224, s[64:67], s90 offen offset:1024 sc1\n"
"v_mfma_f32_16x16x32_bf16 v[168:171], a[152:155], a[92:95], v[168:171]\n"
"ds_read_b64_tr_b16 a[142:143], v239 offset:26624\n"
"v_cvt_pk_bf16_f32 v112, v112, v113\n"
"v_mfma_f32_16x16x32_bf16 v[172:175], a[156:159], a[92:95], v[172:175]\n"
"ds_read_b64_tr_b16 a[144:145], v221 offset:28672\n"
"v_cvt_pk_bf16_f32 v113, v114, v115\n"
"v_mfma_f32_16x16x32_bf16 v[140:143], a[156:159], a[88:91], v[140:143]\n"
"ds_read_b64_tr_b16 a[146:147], v221 offset:30720\n"
"s_waitcnt lgkmcnt(6)\n"
"v_cvt_pk_bf16_f32 v114, v116, v117\n"
"v_mfma_f32_16x16x32_bf16 v[128:131], a[128:131], a[104:107], v[128:131]\n"
"ds_read_b64_tr_b16 a[148:149], v222 offset:28672\n"
"v_cvt_pk_bf16_f32 v115, v118, v119\n"
"v_mfma_f32_16x16x32_bf16 v[160:163], a[128:131], a[108:111], v[160:163]\n"
"ds_read_b64_tr_b16 a[150:151], v222 offset:30720\n"
"v_mfma_f32_16x16x32_bf16 v[164:167], a[132:135], a[108:111], v[164:167]\n"
"ds_read_b64_tr_b16 a[152:153], v223 offset:28672\n"
"buffer_store_dwordx4 v[112:115], v224, s[64:67], s90 offen offset:2048 sc1\n"
"v_mfma_f32_16x16x32_bf16 v[132:135], a[132:135], a[104:107], v[132:135]\n"
"ds_read_b64_tr_b16 a[154:155], v223 offset:30720\n"
"s_waitcnt lgkmcnt(6)\n"
"v_cvt_pk_bf16_f32 v120, v120, v121\n"
"v_mfma_f32_16x16x32_bf16 v[136:139], a[136:139], a[104:107], v[136:139]\n"
"ds_read_b64_tr_b16 a[156:157], v239 offset:28672\n"
"v_cvt_pk_bf16_f32 v121, v122, v123\n"
"v_mfma_f32_16x16x32_bf16 v[168:171], a[136:139], a[108:111], v[168:171]\n"
"ds_read_b64_tr_b16 a[158:159], v239 offset:30720\n"
"v_cvt_pk_bf16_f32 v122, v124, v125\n"
"v_mfma_f32_16x16x32_bf16 v[172:175], a[140:143], a[108:111], v[172:175]\n"
"ds_read_b64_tr_b16 a[128:129], v44\n"
"v_cvt_pk_bf16_f32 v123, v126, v127\n"
"v_mfma_f32_16x16x32_bf16 v[140:143], a[140:143], a[104:107], v[140:143]\n"
"ds_read_b64_tr_b16 a[130:131], v44 offset:2048\n"
"s_waitcnt lgkmcnt(6)\n"
"v_mfma_f32_16x16x32_bf16 v[128:131], a[144:147], a[120:123], v[128:131]\n"
"ds_read_b64_tr_b16 a[132:133], v45\n"
"buffer_store_dwordx4 v[120:123], v224, s[64:67], s90 offen offset:3072 sc1\n"
"v_mfma_f32_16x16x32_bf16 v[160:163], a[144:147], a[124:127], v[160:163]\n"
"ds_read_b64_tr_b16 a[134:135], v45 offset:2048\n"
"s_sub_u32 s90, s90, 0x1000\n"
"v_mfma_f32_16x16x32_bf16 v[164:167], a[148:151], a[124:127], v[164:167]\n"
"ds_read_b64_tr_b16 a[136:137], v46\n"
"v_mfma_f32_16x16x32_bf16 v[132:135], a[148:151], a[120:123], v[132:135]\n"
"ds_read_b64_tr_b16 a[138:139], v46 offset:2048\n"
"s_waitcnt lgkmcnt(6)\n"
"v_mfma_f32_16x16x32_bf16 v[136:139], a[152:155], a[120:123], v[136:139]\n"
"ds_read_b64_tr_b16 a[140:141], v47\n"
"v_mfma_f32_16x16x32_bf16 v[168:171], a[152:155], a[124:127], v[168:171]\n"
"ds_read_b64_tr_b16 a[142:143], v47 offset:2048\n"
"v_mfma_f32_16x16x32_bf16 v[172:175], a[156:159], a[124:127], v[172:175]\n"
"ds_read_b64_tr_b16 a[144:145], v44 offset:4096\n"
"v_mfma_f32_16x16x32_bf16 v[140:143], a[156:159], a[120:123], v[140:143]\n"
"ds_read_b64_tr_b16 a[146:147], v44 offset:6144\n"
"s_waitcnt lgkmcnt(6)\n"
"v_mfma_f32_16x16x32_bf16 v[128:131], a[128:131], a[168:171], v[128:131]\n"
"ds_read_b64_tr_b16 a[148:149], v45 offset:4096\n"
"v_mfma_f32_16x16x32_bf16 v[160:163], a[128:131], a[172:175], v[160:163]\n"
"ds_read_b64_tr_b16 a[150:151], v45 offset:6144\n"
"v_mfma_f32_16x16x32_bf16 v[164:167], a[132:135], a[172:175], v[164:167]\n"
"ds_read_b64_tr_b16 a[152:153], v46 offset:4096\n"
"v_mfma_f32_16x16x32_bf16 v[132:135], a[132:135], a[168:171], v[132:135]\n"
"ds_read_b64_tr_b16 a[154:155], v46 offset:6144\n"
"s_waitcnt lgkmcnt(6)\n"
"v_mfma_f32_16x16x32_bf16 v[136:139], a[136:139], a[168:171], v[136:139]\n"
"ds_read_b64_tr_b16 a[156:157], v47 offset:4096\n"
"v_mfma_f32_16x16x32_bf16 v[168:171], a[136:139], a[172:175], v[168:171]\n"
"ds_read_b64_tr_b16 a[158:159], v47 offset:6144\n"
"v_mfma_f32_16x16x32_bf16 v[172:175], a[140:143], a[172:175], v[172:175]\n"
"ds_read_b64_tr_b16 a[128:129], v44 offset:8192\n"
"v_mfma_f32_16x16x32_bf16 v[140:143], a[140:143], a[168:171], v[140:143]\n"
"ds_read_b64_tr_b16 a[130:131], v44 offset:10240\n"
"s_waitcnt lgkmcnt(6)\n"
"v_mfma_f32_16x16x32_bf16 v[128:131], a[144:147], v[8:11], v[128:131]\n"
"ds_read_b64_tr_b16 a[132:133], v45 offset:8192\n"
"v_mfma_f32_16x16x32_bf16 v[160:163], a[144:147], v[12:15], v[160:163]\n"
"ds_read_b64_tr_b16 a[134:135], v45 offset:10240\n"
"v_mfma_f32_16x16x32_bf16 v[164:167], a[148:151], v[12:15], v[164:167]\n"
"ds_read_b64_tr_b16 a[136:137], v46 offset:8192\n"
"v_mfma_f32_16x16x32_bf16 v[132:135], a[148:151], v[8:11], v[132:135]\n"
"ds_read_b64_tr_b16 a[138:139], v46 offset:10240\n"
"s_waitcnt lgkmcnt(6)\n"
"v_mfma_f32_16x16x32_bf16 v[136:139], a[152:155], v[8:11], v[136:139]\n"
"ds_read_b64_tr_b16 a[140:141], v47 offset:8192\n"
"v_mfma_f32_16x16x32_bf16 v[168:171], a[152:155], v[12:15], v[168:171]\n"
"ds_read_b64_tr_b16 a[142:143], v47 offset:10240\n"
"v_mfma_f32_16x16x32_bf16 v[172:175], a[156:159], v[12:15], v[172:175]\n"
"ds_read_b64_tr_b16 a[144:145], v44 offset:12288\n"
"v_mfma_f32_16x16x32_bf16 v[140:143], a[156:159], v[8:11], v[140:143]\n"
"ds_read_b64_tr_b16 a[146:147], v44 offset:14336\n"
"s_waitcnt lgkmcnt(6)\n"
"v_mfma_f32_16x16x32_bf16 v[128:131], a[128:131], v[200:203], v[128:131]\n"
"ds_read_b64_tr_b16 a[148:149], v45 offset:12288\n"
"v_mfma_f32_16x16x32_bf16 v[160:163], a[128:131], v[204:207], v[160:163]\n"
"ds_read_b64_tr_b16 a[150:151], v45 offset:14336\n"
"v_mfma_f32_16x16x32_bf16 v[164:167], a[132:135], v[204:207], v[164:167]\n"
"ds_read_b64_tr_b16 a[152:153], v46 offset:12288\n"
"v_mfma_f32_16x16x32_bf16 v[132:135], a[132:135], v[200:203], v[132:135]\n"
"ds_read_b64_tr_b16 a[154:155], v46 offset:14336\n"
"s_waitcnt lgkmcnt(6)\n"
"v_mfma_f32_16x16x32_bf16 v[136:139], a[136:139], v[200:203], v[136:139]\n"
"ds_read_b64_tr_b16 a[156:157], v47 offset:12288\n"
"v_mfma_f32_16x16x32_bf16 v[168:171], a[136:139], v[204:207], v[168:171]\n"
"ds_read_b64_tr_b16 a[158:159], v47 offset:14336\n"
"v_mfma_f32_16x16x32_bf16 v[172:175], a[140:143], v[204:207], v[172:175]\n"
"ds_read_b64_tr_b16 a[128:129], v44 offset:16384\n"
"v_mfma_f32_16x16x32_bf16 v[140:143], a[140:143], v[200:203], v[140:143]\n"
"ds_read_b64_tr_b16 a[130:131], v44 offset:18432\n"
"s_waitcnt lgkmcnt(6)\n"
"v_mfma_f32_16x16x32_bf16 v[128:131], a[144:147], v[248:251], v[128:131]\n"
"ds_read_b64_tr_b16 a[132:133], v45 offset:16384\n"
"v_mfma_f32_16x16x32_bf16 v[160:163], a[144:147], v[252:255], v[160:163]\n"
"ds_read_b64_tr_b16 a[134:135], v45 offset:18432\n"
"v_mfma_f32_16x16x32_bf16 v[164:167], a[148:151], v[252:255], v[164:167]\n"
"ds_read_b64_tr_b16 a[136:137], v46 offset:16384\n"
"v_mfma_f32_16x16x32_bf16 v[132:135], a[148:151], v[248:251], v[132:135]\n"
".Lpad13_%=:\n"
"s_cbranch_execz .Lback0_%=\n"
"ds_read_b64_tr_b16 a[138:139], v46 offset:18432\n"
"s_waitcnt lgkmcnt(6)\n"
"v_mfma_f32_16x16x32_bf16 v[136:139], a[152:155], v[248:251], v[136:139]\n"
"ds_read_b64_tr_b16 a[140:141], v47 offset:16384\n"
"v_mfma_f32_16x16x32_bf16 v[168:171], a[152:155], v[252:255], v[168:171]\n"
"ds_read_b64_tr_b16 a[142:143], v47 offset:18432\n"
"v_mfma_f32_16x16x32_bf16 v[172:175], a[156:159], v[252:255], v[172:175]\n"
"ds_read_b64_tr_b16 a[144:145], v44 offset:20480\n"
"v_mfma_f32_16x16x32_bf16 v[140:143], a[156:159], v[248:251], v[140:143]\n"
"ds_read_b64_tr_b16 a[146:147], v44 offset:22528\n"
"s_waitcnt lgkmcnt(6)\n"
"v_mfma_f32_16x16x32_bf16 v[128:131], a[128:131], a[184:187], v[128:131]\n"
"ds_read_b64_tr_b16 a[148:149], v45 offset:20480\n"
"v_mfma_f32_16x16x32_bf16 v[160:163], a[128:131], a[188:191], v[160:163]\n"
"ds_read_b64_tr_b16 a[150:151], v45 offset:22528\n"
"v_mfma_f32_16x16x32_bf16 v[164:167], a[132:135], a[188:191], v[164:167]\n"
"ds_read_b64_tr_b16 a[152:153], v46 offset:20480\n"
"v_mfma_f32_16x16x32_bf16 v[132:135], a[132:135], a[184:187], v[132:135]\n"
"ds_read_b64_tr_b16 a[154:155], v46 offset:22528\n"
"s_waitcnt lgkmcnt(6)\n"
"v_mfma_f32_16x16x32_bf16 v[136:139], a[136:139], a[184:187], v[136:139]\n"
"ds_read_b64_tr_b16 a[156:157], v47 offset:20480\n"
"v_mfma_f32_16x16x32_bf16 v[168:171], a[136:139], a[188:191], v[168:171]\n"
"ds_read_b64_tr_b16 a[158:159], v47 offset:22528\n"
"v_mfma_f32_16x16x32_bf16 v[172:175], a[140:143], a[188:191], v[172:175]\n"
"ds_read_b64_tr_b16 a[128:129], v44 offset:24576\n"
"v_mfma_f32_16x16x32_bf16 v[140:143], a[140:143], a[184:187], v[140:143]\n"
"ds_read_b64_tr_b16 a[130:131], v44 offset:26624\n"
"s_waitcnt lgkmcnt(6)\n"
"v_mfma_f32_16x16x32_bf16 v[128:131], a[144:147], a[200:203], v[128:131]\n"
"ds_read_b64_tr_b16 a[132:133], v45 offset:24576\n"
"buffer_load_dwordx4 v[52:55], v208, s[64:67], s95 offen offset:1024 sc1\n"
"v_mfma_f32_16x16x32_bf16 v[160:163], a[144:147], a[204:207], v[160:163]\n"
"ds_read_b64_tr_b16 a[134:135], v45 offset:26624\n"
"s_add_u32 s95, s95, 0x1000\n"
"v_mfma_f32_16x16x32_bf16 v[164:167], a[148:151], a[204:207], v[164:167]\n"
"ds_read_b64_tr_b16 a[136:137], v46 offset:24576\n"
"v_mfma_f32_16x16x32_bf16 v[132:135], a[148:151], a[200:203], v[132:135]\n"
"ds_read_b64_tr_b16 a[138:139], v46 offset:26624\n"
"s_waitcnt lgkmcnt(6)\n"
"v_mfma_f32_16x16x32_bf16 v[136:139], a[152:155], a[200:203], v[136:139]\n"
"ds_read_b64_tr_b16 a[140:141], v47 offset:24576\n"
"buffer_load_dwordx4 a[8:11], v208, s[64:67], s95 offen sc1\n"
"v_mfma_f32_16x16x32_bf16 v[168:171], a[152:155], a[204:207], v[168:171]\n"
"ds_read_b64_tr_b16 a[142:143], v47 offset:26624\n"
"v_mfma_f32_16x16x32_bf16 v[172:175], a[156:159], a[204:207], v[172:175]\n"
"ds_read_b64_tr_b16 a[144:145], v44 offset:28672\n"
"v_mfma_f32_16x16x32_bf16 v[140:143], a[156:159], a[200:203], v[140:143]\n"
"ds_read_b64_tr_b16 a[146:147], v44 offset:30720\n"
"s_waitcnt lgkmcnt(6)\n"
"v_mfma_f32_16x16x32_bf16 v[128:131], a[128:131], a[216:219], v[128:131]\n"
"ds_read_b64_tr_b16 a[148:149], v45 offset:28672\n"
"v_mfma_f32_16x16x32_bf16 v[160:163], a[128:131], a[220:223], v[160:163]\n"
"ds_read_b64_tr_b16 a[150:151], v45 offset:30720\n"
"v_mfma_f32_16x16x32_bf16 v[164:167], a[132:135], a[220:223], v[164:167]\n"
"ds_read_b64_tr_b16 a[152:153], v46 offset:28672\n"
"buffer_load_dwordx4 a[12:15], v208, s[64:67], s95 offen offset:1024 sc1\n"
"v_mfma_f32_16x16x32_bf16 v[132:135], a[132:135], a[216:219], v[132:135]\n"
"ds_read_b64_tr_b16 a[154:155], v46 offset:30720\n"
"s_add_u32 s95, s95, 0x1000\n"
"s_waitcnt lgkmcnt(6)\n"
"v_mfma_f32_16x16x32_bf16 v[136:139], a[136:139], a[216:219], v[136:139]\n"
"ds_read_b64_tr_b16 a[156:157], v47 offset:28672\n"
"v_mfma_f32_16x16x32_bf16 v[168:171], a[136:139], a[220:223], v[168:171]\n"
"ds_read_b64_tr_b16 a[158:159], v47 offset:30720\n"
"v_mfma_f32_16x16x32_bf16 v[172:175], a[140:143], a[220:223], v[172:175]\n"
"ds_read_b64_tr_b16 a[128:129], v221\n"
"buffer_load_dwordx4 a[24:27], v208, s[64:67], s95 offen sc1\n"
"v_mfma_f32_16x16x32_bf16 v[140:143], a[140:143], a[216:219], v[140:143]\n"
"ds_read_b64_tr_b16 a[130:131], v221 offset:2048\n"
"s_waitcnt lgkmcnt(6)\n"
"v_mfma_f32_16x16x32_bf16 v[128:131], a[144:147], a[232:235], v[128:131]\n"
"ds_read_b64_tr_b16 a[132:133], v222\n"
"v_mfma_f32_16x16x32_bf16 v[160:163], a[144:147], a[236:239], v[160:163]\n"
"ds_read_b64_tr_b16 a[134:135], v222 offset:2048\n"
"v_mfma_f32_16x16x32_bf16 v[164:167], a[148:151], a[236:239], v[164:167]\n"
"ds_read_b64_tr_b16 a[136:137], v223\n"
"buffer_load_dwordx4 a[28:31], v208, s[64:67], s95 offen offset:1024 sc1\n"
"v_mfma_f32_16x16x32_bf16 v[132:135], a[148:151], a[232:235], v[132:135]\n"
"ds_read_b64_tr_b16 a[138:139], v223 offset:2048\n"
"s_add_u32 s95, s95, 0x1000\n"
"s_waitcnt lgkmcnt(6)\n"
"v_mfma_f32_16x16x32_bf16 v[136:139], a[152:155], a[232:235], v[136:139]\n"
"ds_read_b64_tr_b16 a[140:141], v239\n"
"v_mfma_f32_16x16x32_bf16 v[168:171], a[152:155], a[236:239], v[168:171]\n"
"ds_read_b64_tr_b16 a[142:143], v239 offset:2048\n"
"v_mfma_f32_16x16x32_bf16 v[172:175], a[156:159], a[236:239], v[172:175]\n"
"ds_read_b64_tr_b16 a[144:145], v221 offset:4096\n"
"v_mfma_f32_16x16x32_bf16 v[140:143], a[156:159], a[232:235], v[140:143]\n"
"ds_read_b64_tr_b16 a[146:147], v221 offset:6144\n"
"s_waitcnt lgkmcnt(6)\n"
"v_mfma_f32_16x16x32_bf16 v[64:67], a[128:131], a[0:3], v[64:67]\n"
"ds_read_b64_tr_b16 a[148:149], v222 offset:4096\n"
"buffer_load_dwordx4 a[40:43], v208, s[64:67], s95 offen sc1\n"
"v_mfma_f32_16x16x32_bf16 v[96:99], a[128:131], a[4:7], v[96:99]\n"
"ds_read_b64_tr_b16 a[150:151], v222 offset:6144\n"
"v_mfma_f32_16x16x32_bf16 v[100:103], a[132:135], a[4:7], v[100:103]\n"
"ds_read_b64_tr_b16 a[152:153], v223 offset:4096\n"
"v_mfma_f32_16x16x32_bf16 v[68:71], a[132:135], a[0:3], v[68:71]\n"
"ds_read_b64_tr_b16 a[154:155], v223 offset:6144\n"
"s_waitcnt lgkmcnt(6)\n"
"v_mfma_f32_16x16x32_bf16 v[72:75], a[136:139], a[0:3], v[72:75]\n"
"ds_read_b64_tr_b16 a[156:157], v239 offset:4096\n"
"buffer_load_dwordx4 a[44:47], v208, s[64:67], s95 offen offset:1024 sc1\n"
"v_mfma_f32_16x16x32_bf16 v[104:107], a[136:139], a[4:7], v[104:107]\n"
"ds_read_b64_tr_b16 a[158:159], v239 offset:6144\n"
"s_add_u32 s95, s95, 0x1000\n"
"v_mfma_f32_16x16x32_bf16 v[108:111], a[140:143], a[4:7], v[108:111]\n"
"ds_read_b64_tr_b16 a[128:129], v221 offset:8192\n"
"v_cvt_pk_bf16_f32 v128, v128, v129\n"
"v_mfma_f32_16x16x32_bf16 v[76:79], a[140:143], a[0:3], v[76:79]\n"
"ds_read_b64_tr_b16 a[130:131], v221 offset:10240\n"
"s_waitcnt lgkmcnt(6)\n"
"v_mfma_f32_16x16x32_bf16 v[64:67], a[144:147], a[16:19], v[64:67]\n"
"ds_read_b64_tr_b16 a[132:133], v222 offset:8192\n"
"v_cvt_pk_bf16_f32 v129, v130, v131\n"
"v_mfma_f32_16x16x32_bf16 v[96:99], a[144:147], a[20:23], v[96:99]\n"
"ds_read_b64_tr_b16 a[134:135], v222 offset:10240\n"
"v_cvt_pk_bf16_f32 v130, v132, v133\n"
"v_mfma_f32_16x16x32_bf16 v[100:103], a[148:151], a[20:23], v[100:103]\n"
"ds_read_b64_tr_b16 a[136:137], v223 offset:8192\n"
"buffer_load_dwordx4 a[56:59], v208, s[64:67], s95 offen sc1\n"
"v_mfma_f32_16x16x32_bf16 v[68:71], a[148:151], a[16:19], v[68:71]\n"
"ds_read_b64_tr_b16 a[138:139], v223 offset:10240\n"
"s_waitcnt lgkmcnt(6)\n"
"v_mfma_f32_16x16x32_bf16 v[72:75], a[152:155], a[16:19], v[72:75]\n"
"ds_read_b64_tr_b16 a[140:141], v239 offset:8192\n"
"v_cvt_pk_bf16_f32 v131, v134, v135\n"
"v_mfma_f32_16x16x32_bf16 v[104:107], a[152:155], a[20:23], v[104:107]\n"
"ds_read_b64_tr_b16 a[142:143], v239 offset:10240\n"
"v_mfma_f32_16x16x32_bf16 v[108:111], a[156:159], a[20:23], v[108:111]\n"
"ds_read_b64_tr_b16 a[144:145], v221 offset:12288\n"
"buffer_store_dwordx4 v[128:131], v224, s[64:67], s90 offen sc1\n"
"v_mfma_f32_16x16x32_bf16 v[76:79], a[156:159], a[16:19], v[76:79]\n"
"ds_read_b64_tr_b16 a[146:147], v221 offset:14336\n"
"s_waitcnt lgkmcnt(6)\n"
"v_mfma_f32_16x16x32_bf16 v[64:67], a[128:131], a[32:35], v[64:67]\n"
"ds_read_b64_tr_b16 a[148:149], v222 offset:12288\n"
"v_cvt_pk_bf16_f32 v136, v136, v137\n"
"v_mfma_f32_16x16x32_bf16 v[96:99], a[128:131], a[36:39], v[96:99]\n"
"ds_read_b64_tr_b16 a[150:151], v222 offset:14336\n"
"v_cvt_pk_bf16_f32 v137, v138, v139\n"
"v_mfma_f32_16x16x32_bf16 v[100:103], a[132:135], a[36:39], v[100:103]\n"
"ds_read_b64_tr_b16 a[152:153], v223 offset:12288\n"
"buffer_load_dwordx4 a[60:63], v208, s[64:67], s95 offen offset:1024 sc1\n"
"v_mfma_f32_16x16x32_bf16 v[68:71], a[132:135], a[32:35], v[68:71]\n"
"ds_read_b64_tr_b16 a[154:155], v223 offset:14336\n"
"s_add_u32 s95, s95, 0x1000\n"
"s_waitcnt lgkmcnt(6)\n"
"v_mfma_f32_16x16x32_bf16 v[72:75], a[136:139], a[32:35], v[72:75]\n"
"ds_read_b64_tr_b16 a[156:157], v239 offset:12288\n"
"v_cvt_pk_bf16_f32 v138, v140, v141\n"
"v_mfma_f32_16x16x32_bf16 v[104:107], a[136:139], a[36:39], v[104:107]\n"
"ds_read_b64_tr_b16 a[158:159], v239 offset:14336\n"
"v_cvt_pk_bf16_f32 v139, v142, v143\n"
"v_mfma_f32_16x16x32_bf16 v[108:111], a[140:143], a[36:39], v[108:111]\n"
"ds_read_b64_tr_b16 a[128:129], v221 offset:16384\n"
"buffer_store_dwordx4 v[136:139], v224, s[64:67], s90 offen offset:1024 sc1\n"
"v_mfma_f32_16x16x32_bf16 v[76:79], a[140:143], a[32:35], v[76:79]\n"
"ds_read_b64_tr_b16 a[130:131], v221 offset:18432\n"
"s_waitcnt lgkmcnt(6)\n"
"v_cvt_pk_bf16_f32 v160, v160, v161\n"
"v_mfma_f32_16x16x32_bf16 v[64:67], a[144:147], a[48:51], v[64:67]\n"
"ds_read_b64_tr_b16 a[132:133], v222 offset:16384\n"
"v_cvt_pk_bf16_f32 v161, v162, v163\n"
"v_mfma_f32_16x16x32_bf16 v[96:99], a[144:147], a[52:55], v[96:99]\n"
"ds_read_b64_tr_b16 a[134:135], v222 offset:18432\n"
"v_cvt_pk_bf16_f32 v162, v164, v165\n"
"v_mfma_f32_16x16x32_bf16 v[100:103], a[148:151], a[52:55], v[100:103]\n"
"ds_read_b64_tr_b16 a[136:137], v223 offset:16384\n"
"buffer_load_dwordx4 a[72:75], v208, s[64:67], s95 offen sc1\n"
"v_cvt_pk_bf16_f32 v163, v166, v167\n"
"v_mfma_f32_16x16x32_bf16 v[68:71], a[148:151], a[48:51], v[68:71]\n"
"ds_read_b64_tr_b16 a[138:139], v223 offset:18432\n"
"s_waitcnt lgkmcnt(6)\n"
"v_mfma_f32_16x16x32_bf16 v[72:75], a[152:155], a[48:51], v[72:75]\n"
"ds_read_b64_tr_b16 a[140:141], v239 offset:16384\n"
"v_mfma_f32_16x16x32_bf16 v[104:107], a[152:155], a[52:55], v[104:107]\n"
"ds_read_b64_tr_b16 a[142:143], v239 offset:18432\n"
"v_mfma_f32_16x16x32_bf16 v[108:111], a[156:159], a[52:55], v[108:111]\n"
"ds_read_b64_tr_b16 a[144:145], v221 offset:20480\n"
"buffer_store_dwordx4 v[160:163], v224, s[64:67], s90 offen offset:2048 sc1\n"
"v_mfma_f32_16x16x32_bf16 v[76:79], a[156:159], a[48:51], v[76:79]\n"
"ds_read_b64_tr_b16 a[146:147], v221 offset:22528\n"
"s_waitcnt lgkmcnt(6)\n"
"v_cvt_pk_bf16_f32 v168, v168, v169\n"
"v_mfma_f32_16x16x32_bf16 v[64:67], a[128:131], a[64:67], v[64:67]\n"
"ds_read_b64_tr_b16 a[148:149], v222 offset:20480\n"
"v_cvt_pk_bf16_f32 v169, v170, v171\n"
"v_mfma_f32_16x16x32_bf16 v[96:99], a[128:131], a[68:71], v[96:99]\n"
"ds_read_b64_tr_b16 a[150:151], v222 offset:22528\n"
"v_cvt_pk_bf16_f32 v170, v172, v173\n"
"v_mfma_f32_16x16x32_bf16 v[100:103], a[132:135], a[68:71], v[100:103]\n"
"ds_read_b64_tr_b16 a[152:153], v223 offset:20480\n"
"buffer_load_dwordx4 a[76:79], v208, s[64:67], s95 offen offset:1024 sc1\n"
"v_cvt_pk_bf16_f32 v171, v174, v175\n"
"v_mfma_f32_16x16x32_bf16 v[68:71], a[132:135], a[64:67], v[68:71]\n"
"ds_read_b64_tr_b16 a[154:155], v223 offset:22528\n"
"s_add_u32 s95, s95, 0x1000\n"
"s_waitcnt lgkmcnt(6)\n"
"v_mfma_f32_16x16x32_bf16 v[72:75], a[136:139], a[64:67], v[72:75]\n"
"ds_read_b64_tr_b16 a[156:157], v239 offset:20480\n"
"v_mfma_f32_16x16x32_bf16 v[104:107], a[136:139], a[68:71], v[104:107]\n"
"ds_read_b64_tr_b16 a[158:159], v239 offset:22528\n"
"v_mfma_f32_16x16x32_bf16 v[108:111], a[140:143], a[68:71], v[108:111]\n"
"ds_read_b64_tr_b16 a[128:129], v221 offset:24576\n"
"buffer_store_dwordx4 v[168:171], v224, s[64:67], s90 offen offset:3072 sc1\n"
"v_mfma_f32_16x16x32_bf16 v[76:79], a[140:143], a[64:67], v[76:79]\n"
"ds_read_b64_tr_b16 a[130:131], v221 offset:26624\n"
"s_waitcnt lgkmcnt(6)\n"
"s_sub_u32 s90, s90, 0x1000\n"
"v_mfma_f32_16x16x32_bf16 v[64:67], a[144:147], a[80:83], v[64:67]\n"
"ds_read_b64_tr_b16 a[132:133], v222 offset:24576\n"
"v_mfma_f32_16x16x32_bf16 v[96:99], a[144:147], a[84:87], v[96:99]\n"
"ds_read_b64_tr_b16 a[134:135], v222 offset:26624\n"
"v_mfma_f32_16x16x32_bf16 v[100:103], a[148:151], a[84:87], v[100:103]\n"
"ds_read_b64_tr_b16 a[136:137], v223 offset:24576\n"
"buffer_load_dwordx4 a[88:91], v208, s[64:67], s95 offen sc1\n"
"v_mfma_f32_16x16x32_bf16 v[68:71], a[148:151], a[80:83], v[68:71]\n"
"ds_read_b64_tr_b16 a[138:139], v223 offset:26624\n"
"s_waitcnt lgkmcnt(6)\n"
"v_mfma_f32_16x16x32_bf16 v[72:75], a[152:155], a[80:83], v[72:75]\n"
"ds_read_b64_tr_b16 a[140:141], v239 offset:24576\n"
"v_mfma_f32_16x16x32_bf16 v[104:107], a[152:155], a[84:87], v[104:107]\n"
"ds_read_b64_tr_b16 a[142:143], v239 offset:26624\n"
"v_mfma_f32_16x16x32_bf16 v[108:111], a[156:159], a[84:87], v[108:111]\n"
"ds_read_b64_tr_b16 a[144:145], v221 offset:28672\n"
"buffer_load_dwordx4 a[92:95], v208, s[64:67], s95 offen offset:1024 sc1\n"
"v_mfma_f32_16x16x32_bf16 v[76:79], a[156:159], a[80:83], v[76:79]\n"
"ds_read_b64_tr_b16 a[146:147], v221 offset:30720\n"
"s_add_u32 s95, s95, 0x1000\n"
"s_waitcnt lgkmcnt(6)\n"
"v_mfma_f32_16x16x32_bf16 v[64:67], a[128:131], a[96:99], v[64:67]\n"
"ds_read_b64_tr_b16 a[148:149], v222 offset:28672\n"
"v_mfma_f32_16x16x32_bf16 v[96:99], a[128:131], a[100:103], v[96:99]\n"
"ds_read_b64_tr_b16 a[150:151], v222 offset:30720\n"
"v_mfma_f32_16x16x32_bf16 v[100:103], a[132:135], a[100:103], v[100:103]\n"
"ds_read_b64_tr_b16 a[152:153], v223 offset:28672\n"
"buffer_load_dwordx4 a[104:107], v208, s[64:67], s95 offen sc1\n"
"v_mfma_f32_16x16x32_bf16 v[68:71], a[132:135], a[96:99], v[68:71]\n"
"ds_read_b64_tr_b16 a[154:155], v223 offset:30720\n"
"s_waitcnt lgkmcnt(6)\n"
"v_mfma_f32_16x16x32_bf16 v[72:75], a[136:139], a[96:99], v[72:75]\n"
"ds_read_b64_tr_b16 a[156:157], v239 offset:28672\n"
"v_mfma_f32_16x16x32_bf16 v[104:107], a[136:139], a[100:103], v[104:107]\n"
"ds_read_b64_tr_b16 a[158:159], v239 offset:30720\n"
"v_mfma_f32_16x16x32_bf16 v[108:111], a[140:143], a[100:103], v[108:111]\n"
"ds_read_b64_tr_b16 a[128:129], v44\n"
"buffer_load_dwordx4 a[108:111], v208, s[64:67], s95 offen offset:1024 sc1\n"
"v_mfma_f32_16x16x32_bf16 v[76:79], a[140:143], a[96:99], v[76:79]\n"
"ds_read_b64_tr_b16 a[130:131], v44 offset:2048\n"
"s_add_u32 s95, s95, 0x1000\n"
"s_waitcnt lgkmcnt(6)\n"
"v_mfma_f32_16x16x32_bf16 v[64:67], a[144:147], a[112:115], v[64:67]\n"
"ds_read_b64_tr_b16 a[132:133], v45\n"
"v_mfma_f32_16x16x32_bf16 v[96:99], a[144:147], a[116:119], v[96:99]\n"
"ds_read_b64_tr_b16 a[134:135], v45 offset:2048\n"
"v_mfma_f32_16x16x32_bf16 v[100:103], a[148:151], a[116:119], v[100:103]\n"
"ds_read_b64_tr_b16 a[136:137], v46\n"
"buffer_load_dwordx4 a[120:123], v208, s[64:67], s95 offen sc1\n"
"v_mfma_f32_16x16x32_bf16 v[68:71], a[148:151], a[112:115], v[68:71]\n"
"ds_read_b64_tr_b16 a[138:139], v46 offset:2048\n"
"s_waitcnt lgkmcnt(6)\n"
"v_mfma_f32_16x16x32_bf16 v[72:75], a[152:155], a[112:115], v[72:75]\n"
"ds_read_b64_tr_b16 a[140:141], v47\n"
"v_mfma_f32_16x16x32_bf16 v[104:107], a[152:155], a[116:119], v[104:107]\n"
"ds_read_b64_tr_b16 a[142:143], v47 offset:2048\n"
"v_mfma_f32_16x16x32_bf16 v[108:111], a[156:159], a[116:119], v[108:111]\n"
"ds_read_b64_tr_b16 a[144:145], v44 offset:4096\n"
"v_mfma_f32_16x16x32_bf16 v[76:79], a[156:159], a[112:115], v[76:79]\n"
"ds_read_b64_tr_b16 a[146:147], v44 offset:6144\n"
"s_waitcnt lgkmcnt(6)\n"
"v_mfma_f32_16x16x32_bf16 v[64:67], a[128:131], a[160:163], v[64:67]\n"
"ds_read_b64_tr_b16 a[148:149], v45 offset:4096\n"
"buffer_load_dwordx4 a[124:127], v208, s[64:67], s95 offen offset:1024 sc1\n"
"v_mfma_f32_16x16x32_bf16 v[96:99], a[128:131], a[164:167], v[96:99]\n"
"ds_read_b64_tr_b16 a[150:151], v45 offset:6144\n"
"s_add_u32 s95, s95, 0x1000\n"
"v_mfma_f32_16x16x32_bf16 v[100:103], a[132:135], a[164:167], v[100:103]\n"
"ds_read_b64_tr_b16 a[152:153], v46 offset:4096\n"
"v_mfma_f32_16x16x32_bf16 v[68:71], a[132:135], a[160:163], v[68:71]\n"
"ds_read_b64_tr_b16 a[154:155], v46 offset:6144\n"
"s_waitcnt lgkmcnt(6)\n"
"v_mfma_f32_16x16x32_bf16 v[72:75], a[136:139], a[160:163], v[72:75]\n"
"ds_read_b64_tr_b16 a[156:157], v47 offset:4096\n"
"buffer_load_dwordx4 a[168:171], v208, s[64:67], s95 offen sc1\n"
"v_mfma_f32_16x16x32_bf16 v[104:107], a[136:139], a[164:167], v[104:107]\n"
"ds_read_b64_tr_b16 a[158:159], v47 offset:6144\n"
"v_mfma_f32_16x16x32_bf16 v[108:111], a[140:143], a[164:167], v[108:111]\n"
"ds_read_b64_tr_b16 a[128:129], v44 offset:8192\n"
"v_mfma_f32_16x16x32_bf16 v[76:79], a[140:143], a[160:163], v[76:79]\n"
"ds_read_b64_tr_b16 a[130:131], v44 offset:10240\n"
"s_waitcnt lgkmcnt(6)\n"
"v_mfma_f32_16x16x32_bf16 v[64:67], a[144:147], v[0:3], v[64:67]\n"
"ds_read_b64_tr_b16 a[132:133], v45 offset:8192\n"
"buffer_load_dwordx4 a[172:175], v208, s[64:67], s95 offen offset:1024 sc1\n"
"v_mfma_f32_16x16x32_bf16 v[96:99], a[144:147], v[4:7], v[96:99]\n"
"ds_read_b64_tr_b16 a[134:135], v45 offset:10240\n"
"s_add_u32 s95, s95, 0x1000\n"
"v_mfma_f32_16x16x32_bf16 v[100:103], a[148:151], v[4:7], v[100:103]\n"
"ds_read_b64_tr_b16 a[136:137], v46 offset:8192\n"
"v_mfma_f32_16x16x32_bf16 v[68:71], a[148:151], v[0:3], v[68:71]\n"
"ds_read_b64_tr_b16 a[138:139], v46 offset:10240\n"
"s_waitcnt lgkmcnt(6)\n"
"v_mfma_f32_16x16x32_bf16 v[72:75], a[152:155], v[0:3], v[72:75]\n"
"ds_read_b64_tr_b16 a[140:141], v47 offset:8192\n"
"v_mfma_f32_16x16x32_bf16 v[104:107], a[152:155], v[4:7], v[104:107]\n"
"ds_read_b64_tr_b16 a[142:143], v47 offset:10240\n"
"v_mfma_f32_16x16x32_bf16 v[108:111], a[156:159], v[4:7], v[108:111]\n"
"ds_read_b64_tr_b16 a[144:145], v44 offset:12288\n"
"s_waitcnt vmcnt(8)\n"
"buffer_store_dwordx2 v[228:229], v225, s[64:67], s55 offen sc1\n"
"v_mfma_f32_16x16x32_bf16 v[76:79], a[156:159], v[0:3], v[76:79]\n"
"ds_read_b64_tr_b16 a[146:147], v44 offset:14336\n"
"s_waitcnt lgkmcnt(6)\n"
"v_mfma_f32_16x16x32_bf16 v[64:67], a[128:131], v[192:195], v[64:67]\n"
"ds_read_b64_tr_b16 a[148:149], v45 offset:12288\n"
"v_mfma_f32_16x16x32_bf16 v[96:99], a[128:131], v[196:199], v[96:99]\n"
"ds_read_b64_tr_b16 a[150:151], v45 offset:14336\n"
"v_mfma_f32_16x16x32_bf16 v[100:103], a[132:135], v[196:199], v[100:103]\n"
"ds_read_b64_tr_b16 a[152:153], v46 offset:12288\n"
"buffer_load_dwordx4 v[8:11], v208, s[64:67], s95 offen sc1\n"
"v_mfma_f32_16x16x32_bf16 v[68:71], a[132:135], v[192:195], v[68:71]\n"
"ds_read_b64_tr_b16 a[154:155], v46 offset:14336\n"
"s_waitcnt lgkmcnt(6)\n"
"v_mfma_f32_16x16x32_bf16 v[72:75], a[136:139], v[192:195], v[72:75]\n"
"ds_read_b64_tr_b16 a[156:157], v47 offset:12288\n"
"v_mfma_f32_16x16x32_bf16 v[104:107], a[136:139], v[196:199], v[104:107]\n"
"ds_read_b64_tr_b16 a[158:159], v47 offset:14336\n"
"v_mfma_f32_16x16x32_bf16 v[108:111], a[140:143], v[196:199], v[108:111]\n"
"ds_read_b64_tr_b16 a[128:129], v44 offset:16384\n"
"buffer_load_dwordx4 v[12:15], v208, s[64:67], s95 offen offset:1024 sc1\n"
"v_mfma_f32_16x16x32_bf16 v[76:79], a[140:143], v[192:195], v[76:79]\n"
"ds_read_b64_tr_b16 a[130:131], v44 offset:18432\n"
"s_add_u32 s95, s95, 0x1000\n"
"s_waitcnt lgkmcnt(6)\n"
"v_mfma_f32_16x16x32_bf16 v[64:67], a[144:147], v[240:243], v[64:67]\n"
"ds_read_b64_tr_b16 a[132:133], v45 offset:16384\n"
"v_mfma_f32_16x16x32_bf16 v[96:99], a[144:147], v[244:247], v[96:99]\n"
"ds_read_b64_tr_b16 a[134:135], v45 offset:18432\n"
"v_mfma_f32_16x16x32_bf16 v[100:103], a[148:151], v[244:247], v[100:103]\n"
"ds_read_b64_tr_b16 a[136:137], v46 offset:16384\n"
"v_mfma_f32_16x16x32_bf16 v[68:71], a[148:151], v[240:243], v[68:71]\n"
"ds_read_b64_tr_b16 a[138:139], v46 offset:18432\n"
"s_waitcnt lgkmcnt(6)\n"
"v_mfma_f32_16x16x32_bf16 v[72:75], a[152:155], v[240:243], v[72:75]\n"
"ds_read_b64_tr_b16 a[140:141], v47 offset:16384\n"
"v_mfma_f32_16x16x32_bf16 v[104:107], a[152:155], v[244:247], v[104:107]\n"
"ds_read_b64_tr_b16 a[142:143], v47 offset:18432\n"
"v_mfma_f32_16x16x32_bf16 v[108:111], a[156:159], v[244:247], v[108:111]\n"
"ds_read_b64_tr_b16 a[144:145], v44 offset:20480\n"
"v_mfma_f32_16x16x32_bf16 v[76:79], a[156:159], v[240:243], v[76:79]\n"
"ds_read_b64_tr_b16 a[146:147], v44 offset:22528\n"
"s_waitcnt lgkmcnt(6)\n"
"v_mfma_f32_16x16x32_bf16 v[64:67], a[128:131], a[176:179], v[64:67]\n"
"ds_read_b64_tr_b16 a[148:149], v45 offset:20480\n"
"v_mfma_f32_16x16x32_bf16 v[96:99], a[128:131], a[180:183], v[96:99]\n"
"ds_read_b64_tr_b16 a[150:151], v45 offset:22528\n"
"v_mfma_f32_16x16x32_bf16 v[100:103], a[132:135], a[180:183], v[100:103]\n"
"ds_read_b64_tr_b16 a[152:153], v46 offset:20480\n"
"v_mfma_f32_16x16x32_bf16 v[68:71], a[132:135], a[176:179], v[68:71]\n"
"ds_read_b64_tr_b16 a[154:155], v46 offset:22528\n"
"s_waitcnt lgkmcnt(6)\n"
"v_mfma_f32_16x16x32_bf16 v[72:75], a[136:139], a[176:179], v[72:75]\n"
"ds_read_b64_tr_b16 a[156:157], v47 offset:20480\n"
"v_mfma_f32_16x16x32_bf16 v[104:107], a[136:139], a[180:183], v[104:107]\n"
"ds_read_b64_tr_b16 a[158:159], v47 offset:22528\n"
"v_mfma_f32_16x16x32_bf16 v[108:111], a[140:143], a[180:183], v[108:111]\n"
"ds_read_b64_tr_b16 a[128:129], v44 offset:24576\n"
"v_mfma_f32_16x16x32_bf16 v[76:79], a[140:143], a[176:179], v[76:79]\n"
"ds_read_b64_tr_b16 a[130:131], v44 offset:26624\n"
"s_waitcnt lgkmcnt(6)\n"
"v_mfma_f32_16x16x32_bf16 v[64:67], a[144:147], a[192:195], v[64:67]\n"
"ds_read_b64_tr_b16 a[132:133], v45 offset:24576\n"
"v_mfma_f32_16x16x32_bf16 v[96:99], a[144:147], a[196:199], v[96:99]\n"
"ds_read_b64_tr_b16 a[134:135], v45 offset:26624\n"
"v_mfma_f32_16x16x32_bf16 v[100:103], a[148:151], a[196:199], v[100:103]\n"
"ds_read_b64_tr_b16 a[136:137], v46 offset:24576\n"
"v_mfma_f32_16x16x32_bf16 v[68:71], a[148:151], a[192:195], v[68:71]\n"
"ds_read_b64_tr_b16 a[138:139], v46 offset:26624\n"
"s_waitcnt lgkmcnt(6)\n"
"v_mfma_f32_16x16x32_bf16 v[72:75], a[152:155], a[192:195], v[72:75]\n"
"ds_read_b64_tr_b16 a[140:141], v47 offset:24576\n"
"v_mfma_f32_16x16x32_bf16 v[104:107], a[152:155], a[196:199], v[104:107]\n"
"ds_read_b64_tr_b16 a[142:143], v47 offset:26624\n"
"v_mfma_f32_16x16x32_bf16 v[108:111], a[156:159], a[196:199], v[108:111]\n"
"ds_read_b64_tr_b16 a[144:145], v44 offset:28672\n"
"v_mfma_f32_16x16x32_bf16 v[76:79], a[156:159], a[192:195], v[76:79]\n"
"ds_read_b64_tr_b16 a[146:147], v44 offset:30720\n"
"s_waitcnt lgkmcnt(6)\n"
"v_mfma_f32_16x16x32_bf16 v[64:67], a[128:131], a[208:211], v[64:67]\n"
"ds_read_b64_tr_b16 a[148:149], v45 offset:28672\n"
"v_mfma_f32_16x16x32_bf16 v[96:99], a[128:131], a[212:215], v[96:99]\n"
"ds_read_b64_tr_b16 a[150:151], v45 offset:30720\n"
"v_mfma_f32_16x16x32_bf16 v[100:103], a[132:135], a[212:215], v[100:103]\n"
"ds_read_b64_tr_b16 a[152:153], v46 offset:28672\n"
"v_mfma_f32_16x16x32_bf16 v[68:71], a[132:135], a[208:211], v[68:71]\n"
"ds_read_b64_tr_b16 a[154:155], v46 offset:30720\n"
"s_waitcnt lgkmcnt(6)\n"
"v_mfma_f32_16x16x32_bf16 v[72:75], a[136:139], a[208:211], v[72:75]\n"
"ds_read_b64_tr_b16 a[156:157], v47 offset:28672\n"
"v_mfma_f32_16x16x32_bf16 v[104:107], a[136:139], a[212:215], v[104:107]\n"
"ds_read_b64_tr_b16 a[158:159], v47 offset:30720\n"
"v_mfma_f32_16x16x32_bf16 v[108:111], a[140:143], a[212:215], v[108:111]\n"
"ds_read_b64_tr_b16 a[128:129], v40\n"
"v_mfma_f32_16x16x32_bf16 v[76:79], a[140:143], a[208:211], v[76:79]\n"
"ds_read_b64_tr_b16 a[130:131], v40 offset:2048\n"
"s_waitcnt lgkmcnt(6)\n"
"v_mfma_f32_16x16x32_bf16 v[64:67], a[144:147], a[224:227], v[64:67]\n"
"ds_read_b64_tr_b16 a[132:133], v41\n"
"v_mfma_f32_16x16x32_bf16 v[96:99], a[144:147], a[228:231], v[96:99]\n"
"ds_read_b64_tr_b16 a[134:135], v41 offset:2048\n"
"v_mfma_f32_16x16x32_bf16 v[100:103], a[148:151], a[228:231], v[100:103]\n"
"ds_read_b64_tr_b16 a[136:137], v42\n"
"v_mfma_f32_16x16x32_bf16 v[68:71], a[148:151], a[224:227], v[68:71]\n"
"ds_read_b64_tr_b16 a[138:139], v42 offset:2048\n"
"s_waitcnt lgkmcnt(6)\n"
"v_mfma_f32_16x16x32_bf16 v[72:75], a[152:155], a[224:227], v[72:75]\n"
"ds_read_b64_tr_b16 a[140:141], v43\n"
"v_mfma_f32_16x16x32_bf16 v[104:107], a[152:155], a[228:231], v[104:107]\n"
"ds_read_b64_tr_b16 a[142:143], v43 offset:2048\n"
"v_mfma_f32_16x16x32_bf16 v[108:111], a[156:159], a[228:231], v[108:111]\n"
"ds_read_b64_tr_b16 a[144:145], v40 offset:4096\n"
"v_mfma_f32_16x16x32_bf16 v[76:79], a[156:159], a[224:227], v[76:79]\n"
"ds_read_b64_tr_b16 a[146:147], v40 offset:6144\n"
"s_waitcnt lgkmcnt(6)\n"
"v_mfma_f32_16x16x32_bf16 v[64:67], a[128:131], a[240:243], v[64:67]\n"
"ds_read_b64_tr_b16 a[148:149], v41 offset:4096\n"
"v_mfma_f32_16x16x32_bf16 v[96:99], a[128:131], a[244:247], v[96:99]\n"
"ds_read_b64_tr_b16 a[150:151], v41 offset:6144\n"
"v_mfma_f32_16x16x32_bf16 v[100:103], a[132:135], a[244:247], v[100:103]\n"
"ds_read_b64_tr_b16 a[152:153], v42 offset:4096\n"
"v_mfma_f32_16x16x32_bf16 v[68:71], a[132:135], a[240:243], v[68:71]\n"
"ds_read_b64_tr_b16 a[154:155], v42 offset:6144\n"
"s_waitcnt lgkmcnt(6)\n"
"v_mfma_f32_16x16x32_bf16 v[72:75], a[136:139], a[240:243], v[72:75]\n"
"ds_read_b64_tr_b16 a[156:157], v43 offset:4096\n"
"v_mfma_f32_16x16x32_bf16 v[104:107], a[136:139], a[244:247], v[104:107]\n"
"ds_read_b64_tr_b16 a[158:159], v43 offset:6144\n"
"v_mfma_f32_16x16x32_bf16 v[108:111], a[140:143], a[244:247], v[108:111]\n"
"ds_read_b64_tr_b16 a[128:129], v40 offset:8192\n"
"v_mfma_f32_16x16x32_bf16 v[76:79], a[140:143], a[240:243], v[76:79]\n"
"ds_read_b64_tr_b16 a[130:131], v40 offset:10240\n"
"s_waitcnt lgkmcnt(6)\n"
"v_mfma_f32_16x16x32_bf16 v[64:67], a[144:147], a[248:251], v[64:67]\n"
"ds_read_b64_tr_b16 a[132:133], v41 offset:8192\n"
"v_mfma_f32_16x16x32_bf16 v[96:99], a[144:147], a[252:255], v[96:99]\n"
"ds_read_b64_tr_b16 a[134:135], v41 offset:10240\n"
"v_mfma_f32_16x16x32_bf16 v[100:103], a[148:151], a[252:255], v[100:103]\n"
"ds_read_b64_tr_b16 a[136:137], v42 offset:8192\n"
"v_mfma_f32_16x16x32_bf16 v[68:71], a[148:151], a[248:251], v[68:71]\n"
"ds_read_b64_tr_b16 a[138:139], v42 offset:10240\n"
"s_waitcnt lgkmcnt(6)\n"
"v_mfma_f32_16x16x32_bf16 v[72:75], a[152:155], a[248:251], v[72:75]\n"
"ds_read_b64_tr_b16 a[140:141], v43 offset:8192\n"
"v_mfma_f32_16x16x32_bf16 v[104:107], a[152:155], a[252:255], v[104:107]\n"
"ds_read_b64_tr_b16 a[142:143], v43 offset:10240\n"
"v_mfma_f32_16x16x32_bf16 v[108:111], a[156:159], a[252:255], v[108:111]\n"
"ds_read_b64_tr_b16 a[144:145], v40 offset:12288\n"
"v_mfma_f32_16x16x32_bf16 v[76:79], a[156:159], a[248:251], v[76:79]\n"
"ds_read_b64_tr_b16 a[146:147], v40 offset:14336\n"
"s_waitcnt lgkmcnt(6)\n"
"v_mfma_f32_16x16x32_bf16 v[64:67], a[128:131], v[16:19], v[64:67]\n"
"ds_read_b64_tr_b16 a[148:149], v41 offset:12288\n"
"v_mfma_f32_16x16x32_bf16 v[96:99], a[128:131], v[20:23], v[96:99]\n"
"ds_read_b64_tr_b16 a[150:151], v41 offset:14336\n"
"v_mfma_f32_16x16x32_bf16 v[100:103], a[132:135], v[20:23], v[100:103]\n"
"ds_read_b64_tr_b16 a[152:153], v42 offset:12288\n"
"buffer_load_dwordx2 v[234:235], v238, s[64:67], 0 offen sc1\n"
"v_mfma_f32_16x16x32_bf16 v[68:71], a[132:135], v[16:19], v[68:71]\n"
"ds_read_b64_tr_b16 a[154:155], v42 offset:14336\n"
"s_waitcnt lgkmcnt(6)\n"
"v_mfma_f32_16x16x32_bf16 v[72:75], a[136:139], v[16:19], v[72:75]\n"
"ds_read_b64_tr_b16 a[156:157], v43 offset:12288\n"
"v_mfma_f32_16x16x32_bf16 v[104:107], a[136:139], v[20:23], v[104:107]\n"
"ds_read_b64_tr_b16 a[158:159], v43 offset:14336\n"
"v_mfma_f32_16x16x32_bf16 v[108:111], a[140:143], v[20:23], v[108:111]\n"
"ds_read_b64_tr_b16 a[128:129], v40 offset:16384\n"
"v_mfma_f32_16x16x32_bf16 v[76:79], a[140:143], v[16:19], v[76:79]\n"
"ds_read_b64_tr_b16 a[130:131], v40 offset:18432\n"
"s_waitcnt lgkmcnt(6)\n"
"v_mfma_f32_16x16x32_bf16 v[64:67], a[144:147], v[24:27], v[64:67]\n"
"ds_read_b64_tr_b16 a[132:133], v41 offset:16384\n"
"v_mfma_f32_16x16x32_bf16 v[96:99], a[144:147], v[28:31], v[96:99]\n"
"ds_read_b64_tr_b16 a[134:135], v41 offset:18432\n"
"v_mfma_f32_16x16x32_bf16 v[100:103], a[148:151], v[28:31], v[100:103]\n"
"ds_read_b64_tr_b16 a[136:137], v42 offset:16384\n"
"v_mfma_f32_16x16x32_bf16 v[68:71], a[148:151], v[24:27], v[68:71]\n"
"ds_read_b64_tr_b16 a[138:139], v42 offset:18432\n"
"s_waitcnt lgkmcnt(6)\n"
"v_mfma_f32_16x16x32_bf16 v[72:75], a[152:155], v[24:27], v[72:75]\n"
"ds_read_b64_tr_b16 a[140:141], v43 offset:16384\n"
"v_mfma_f32_16x16x32_bf16 v[104:107], a[152:155], v[28:31], v[104:107]\n"
"ds_read_b64_tr_b16 a[142:143], v43 offset:18432\n"
"v_mfma_f32_16x16x32_bf16 v[108:111], a[156:159], v[28:31], v[108:111]\n"
"ds_read_b64_tr_b16 a[144:145], v40 offset:20480\n"
"v_mfma_f32_16x16x32_bf16 v[76:79], a[156:159], v[24:27], v[76:79]\n"
"ds_read_b64_tr_b16 a[146:147], v40 offset:22528\n"
"s_waitcnt lgkmcnt(6)\n"
"v_mfma_f32_16x16x32_bf16 v[64:67], a[128:131], v[32:35], v[64:67]\n"
"ds_read_b64_tr_b16 a[148:149], v41 offset:20480\n"
"v_mfma_f32_16x16x32_bf16 v[96:99], a[128:131], v[36:39], v[96:99]\n"
"ds_read_b64_tr_b16 a[150:151], v41 offset:22528\n"
"v_mfma_f32_16x16x32_bf16 v[100:103], a[132:135], v[36:39], v[100:103]\n"
"ds_read_b64_tr_b16 a[152:153], v42 offset:20480\n"
"v_mfma_f32_16x16x32_bf16 v[68:71], a[132:135], v[32:35], v[68:71]\n"
"ds_read_b64_tr_b16 a[154:155], v42 offset:22528\n"
"s_waitcnt lgkmcnt(6)\n"
"v_mfma_f32_16x16x32_bf16 v[72:75], a[136:139], v[32:35], v[72:75]\n"
"ds_read_b64_tr_b16 a[156:157], v43 offset:20480\n"
"v_mfma_f32_16x16x32_bf16 v[104:107], a[136:139], v[36:39], v[104:107]\n"
"ds_read_b64_tr_b16 a[158:159], v43 offset:22528\n"
"v_mfma_f32_16x16x32_bf16 v[108:111], a[140:143], v[36:39], v[108:111]\n"
"ds_read_b64_tr_b16 a[128:129], v40 offset:24576\n"
"v_mfma_f32_16x16x32_bf16 v[76:79], a[140:143], v[32:35], v[76:79]\n"
"ds_read_b64_tr_b16 a[130:131], v40 offset:26624\n"
"s_waitcnt lgkmcnt(6)\n"
"v_mfma_f32_16x16x32_bf16 v[64:67], a[144:147], v[48:51], v[64:67]\n"
"ds_read_b64_tr_b16 a[132:133], v41 offset:24576\n"
"v_mfma_f32_16x16x32_bf16 v[96:99], a[144:147], v[52:55], v[96:99]\n"
"ds_read_b64_tr_b16 a[134:135], v41 offset:26624\n"
"v_mfma_f32_16x16x32_bf16 v[100:103], a[148:151], v[52:55], v[100:103]\n"
"ds_read_b64_tr_b16 a[136:137], v42 offset:24576\n"
"v_mfma_f32_16x16x32_bf16 v[68:71], a[148:151], v[48:51], v[68:71]\n"
"ds_read_b64_tr_b16 a[138:139], v42 offset:26624\n"
"s_waitcnt lgkmcnt(6)\n"
"v_mfma_f32_16x16x32_bf16 v[72:75], a[152:155], v[48:51], v[72:75]\n"
"ds_read_b64_tr_b16 a[140:141], v43 offset:24576\n"
"s_waitcnt vmcnt(0)\n"
".Lchk6_%=:\n"
"v_cmp_ne_u64 vcc, s[84:85], v[234:235]\n"
"s_nop 1\n"
"s_cmp_eq_u64 vcc, 0\n"
"s_cbranch_scc1 .Lok6_%=\n"
"s_cmp_eq_u32 s94, 0\n"
"s_cbranch_scc1 .Lok6_%=\n"
"s_sub_u32 s94, s94, 1\n"
"s_sleep 1\n"
"buffer_load_dwordx2 v[234:235], v238, s[64:67], 0 offen sc1\n"
"s_waitcnt vmcnt(0)\n"
"s_branch .Lchk6_%=\n"
".Lok6_%=:\n"
"v_mfma_f32_16x16x32_bf16 v[104:107], a[152:155], v[52:55], v[104:107]\n"
"ds_read_b64_tr_b16 a[142:143], v43 offset:26624\n"
"s_xor_b32 s92, s93, 0x63000\n"
"v_mfma_f32_16x16x32_bf16 v[108:111], a[156:159], v[52:55], v[108:111]\n"
".Lpad14_%=:\n"
"s_cbranch_execz .Lback0_%=\n"
"ds_read_b64_tr_b16 a[144:145], v40 offset:28672\n"
"buffer_load_dwordx4 v[112:115], v224, s[64:67], s92 offen sc1\n"
"v_mfma_f32_16x16x32_bf16 v[76:79], a[156:159], v[48:51], v[76:79]\n"
"ds_read_b64_tr_b16 a[146:147], v40 offset:30720\n"
"s_waitcnt lgkmcnt(6)\n"
"v_mfma_f32_16x16x32_bf16 v[64:67], a[128:131], a[8:11], v[64:67]\n"
"ds_read_b64_tr_b16 a[148:149], v41 offset:28672\n"
"v_mfma_f32_16x16x32_bf16 v[96:99], a[128:131], a[12:15], v[96:99]\n"
"ds_read_b64_tr_b16 a[150:151], v41 offset:30720\n"
"v_mfma_f32_16x16x32_bf16 v[100:103], a[132:135], a[12:15], v[100:103]\n"
"ds_read_b64_tr_b16 a[152:153], v42 offset:28672\n"
"buffer_load_dwordx4 v[116:119], v224, s[64:67], s92 offen offset:1024 sc1\n"
"v_mfma_f32_16x16x32_bf16 v[68:71], a[132:135], a[8:11], v[68:71]\n"
"ds_read_b64_tr_b16 a[154:155], v42 offset:30720\n"
"s_waitcnt lgkmcnt(6)\n"
"v_mfma_f32_16x16x32_bf16 v[72:75], a[136:139], a[8:11], v[72:75]\n"
"ds_read_b64_tr_b16 a[156:157], v43 offset:28672\n"
"v_mfma_f32_16x16x32_bf16 v[104:107], a[136:139], a[12:15], v[104:107]\n"
"ds_read_b64_tr_b16 a[158:159], v43 offset:30720\n"
"v_mfma_f32_16x16x32_bf16 v[108:111], a[140:143], a[12:15], v[108:111]\n"
"ds_read_b64_tr_b16 a[128:129], v40 offset:32768\n"
"buffer_load_dwordx4 v[120:123], v224, s[64:67], s92 offen offset:2048 sc1\n"
"v_mfma_f32_16x16x32_bf16 v[76:79], a[140:143], a[8:11], v[76:79]\n"
"ds_read_b64_tr_b16 a[130:131], v40 offset:34816\n"
"s_waitcnt lgkmcnt(6)\n"
"v_mfma_f32_16x16x32_bf16 v[64:67], a[144:147], a[24:27], v[64:67]\n"
"ds_read_b64_tr_b16 a[132:133], v41 offset:32768\n"
"v_mfma_f32_16x16x32_bf16 v[96:99], a[144:147], a[28:31], v[96:99]\n"
"ds_read_b64_tr_b16 a[134:135], v41 offset:34816\n"
"v_mfma_f32_16x16x32_bf16 v[100:103], a[148:151], a[28:31], v[100:103]\n"
"ds_read_b64_tr_b16 a[136:137], v42 offset:32768\n"
"buffer_load_dwordx4 v[124:127], v224, s[64:67], s92 offen offset:3072 sc1\n"
"v_mfma_f32_16x16x32_bf16 v[68:71], a[148:151], a[24:27], v[68:71]\n"
"ds_read_b64_tr_b16 a[138:139], v42 offset:34816\n"
"s_waitcnt lgkmcnt(6)\n"
"s_xor_b32 s92, s93, 0x42000\n"
"v_mfma_f32_16x16x32_bf16 v[72:75], a[152:155], a[24:27], v[72:75]\n"
"ds_read_b64_tr_b16 a[140:141], v43 offset:32768\n"
"v_mfma_f32_16x16x32_bf16 v[104:107], a[152:155], a[28:31], v[104:107]\n"
"ds_read_b64_tr_b16 a[142:143], v43 offset:34816\n"
"v_mfma_f32_16x16x32_bf16 v[108:111], a[156:159], a[28:31], v[108:111]\n"
"ds_read_b64_tr_b16 a[144:145], v40 offset:36864\n"
"buffer_load_dwordx4 v[80:83], v224, s[64:67], s92 offen sc1\n"
"v_mfma_f32_16x16x32_bf16 v[76:79], a[156:159], a[24:27], v[76:79]\n"
"ds_read_b64_tr_b16 a[146:147], v40 offset:38912\n"
"s_waitcnt lgkmcnt(6)\n"
"v_mfma_f32_16x16x32_bf16 v[64:67], a[128:131], a[40:43], v[64:67]\n"
"ds_read_b64_tr_b16 a[148:149], v41 offset:36864\n"
"v_mfma_f32_16x16x32_bf16 v[96:99], a[128:131], a[44:47], v[96:99]\n"
"ds_read_b64_tr_b16 a[150:151], v41 offset:38912\n"
"v_mfma_f32_16x16x32_bf16 v[100:103], a[132:135], a[44:47], v[100:103]\n"
"ds_read_b64_tr_b16 a[152:153], v42 offset:36864\n"
"buffer_load_dwordx4 v[84:87], v224, s[64:67], s92 offen offset:1024 sc1\n"
"v_mfma_f32_16x16x32_bf16 v[68:71], a[132:135], a[40:43], v[68:71]\n"
"ds_read_b64_tr_b16 a[154:155], v42 offset:38912\n"
"s_waitcnt lgkmcnt(6)\n"
"v_mfma_f32_16x16x32_bf16 v[72:75], a[136:139], a[40:43], v[72:75]\n"
"ds_read_b64_tr_b16 a[156:157], v43 offset:36864\n"
"v_mfma_f32_16x16x32_bf16 v[104:107], a[136:139], a[44:47], v[104:107]\n"
"ds_read_b64_tr_b16 a[158:159], v43 offset:38912\n"
"v_mfma_f32_16x16x32_bf16 v[108:111], a[140:143], a[44:47], v[108:111]\n"
"ds_read_b64_tr_b16 a[128:129], v40 offset:40960\n"
"buffer_load_dwordx4 v[88:91], v224, s[64:67], s92 offen offset:2048 sc1\n"
"v_mfma_f32_16x16x32_bf16 v[76:79], a[140:143], a[40:43], v[76:79]\n"
"ds_read_b64_tr_b16 a[130:131], v40 offset:43008\n"
"s_waitcnt lgkmcnt(6)\n"
"v_mfma_f32_16x16x32_bf16 v[64:67], a[144:147], a[56:59], v[64:67]\n"
"ds_read_b64_tr_b16 a[132:133], v41 offset:40960\n"
"v_mfma_f32_16x16x32_bf16 v[96:99], a[144:147], a[60:63], v[96:99]\n"
"ds_read_b64_tr_b16 a[134:135], v41 offset:43008\n"
"v_mfma_f32_16x16x32_bf16 v[100:103], a[148:151], a[60:63], v[100:103]\n"
"ds_read_b64_tr_b16 a[136:137], v42 offset:40960\n"
"buffer_load_dwordx4 v[92:95], v224, s[64:67], s92 offen offset:3072 sc1\n"
"v_mfma_f32_16x16x32_bf16 v[68:71], a[148:151], a[56:59], v[68:71]\n"
"ds_read_b64_tr_b16 a[138:139], v42 offset:43008\n"
"s_waitcnt lgkmcnt(6)\n"
"s_xor_b32 s92, s93, 0x21000\n"
"v_mfma_f32_16x16x32_bf16 v[72:75], a[152:155], a[56:59], v[72:75]\n"
"ds_read_b64_tr_b16 a[140:141], v43 offset:40960\n"
"v_mfma_f32_16x16x32_bf16 v[104:107], a[152:155], a[60:63], v[104:107]\n"
"ds_read_b64_tr_b16 a[142:143], v43 offset:43008\n"
"v_mfma_f32_16x16x32_bf16 v[108:111], a[156:159], a[60:63], v[108:111]\n"
"ds_read_b64_tr_b16 a[144:145], v40 offset:45056\n"
"buffer_load_dwordx4 v[128:131], v224, s[64:67], s92 offen sc1\n"
"v_mfma_f32_16x16x32_bf16 v[76:79], a[156:159], a[56:59], v[76:79]\n"
"ds_read_b64_tr_b16 a[146:147], v40 offset:47104\n"
"s_waitcnt lgkmcnt(6)\n"
"v_mfma_f32_16x16x32_bf16 v[64:67], a[128:131], a[72:75], v[64:67]\n"
"ds_read_b64_tr_b16 a[148:149], v41 offset:45056\n"
"v_mfma_f32_16x16x32_bf16 v[96:99], a[128:131], a[76:79], v[96:99]\n"
"ds_read_b64_tr_b16 a[150:151], v41 offset:47104\n"
"v_mfma_f32_16x16x32_bf16 v[100:103], a[132:135], a[76:79], v[100:103]\n"
"ds_read_b64_tr_b16 a[152:153], v42 offset:45056\n"
"buffer_load_dwordx4 v[132:135], v224, s[64:67], s92 offen offset:1024 sc1\n"
"v_mfma_f32_16x16x32_bf16 v[68:71], a[132:135], a[72:75], v[68:71]\n"
"ds_read_b64_tr_b16 a[154:155], v42 offset:47104\n"
"s_waitcnt lgkmcnt(6)\n"
"v_mfma_f32_16x16x32_bf16 v[72:75], a[136:139], a[72:75], v[72:75]\n"
"ds_read_b64_tr_b16 a[156:157], v43 offset:45056\n"
"v_mfma_f32_16x16x32_bf16 v[104:107], a[136:139], a[76:79], v[104:107]\n"
"ds_read_b64_tr_b16 a[158:159], v43 offset:47104\n"
"v_mfma_f32_16x16x32_bf16 v[108:111], a[140:143], a[76:79], v[108:111]\n"
"ds_read_b64_tr_b16 a[128:129], v40 offset:49152\n"
"buffer_load_dwordx4 v[136:139], v224, s[64:67], s92 offen offset:2048 sc1\n"
"v_mfma_f32_16x16x32_bf16 v[76:79], a[140:143], a[72:75], v[76:79]\n"
"ds_read_b64_tr_b16 a[130:131], v40 offset:51200\n"
"s_waitcnt lgkmcnt(6)\n"
"v_mfma_f32_16x16x32_bf16 v[64:67], a[144:147], a[88:91], v[64:67]\n"
"ds_read_b64_tr_b16 a[132:133], v41 offset:49152\n"
"v_mfma_f32_16x16x32_bf16 v[96:99], a[144:147], a[92:95], v[96:99]\n"
"ds_read_b64_tr_b16 a[134:135], v41 offset:51200\n"
"v_mfma_f32_16x16x32_bf16 v[100:103], a[148:151], a[92:95], v[100:103]\n"
"ds_read_b64_tr_b16 a[136:137], v42 offset:49152\n"
"buffer_load_dwordx4 v[140:143], v224, s[64:67], s92 offen offset:3072 sc1\n"
"v_mfma_f32_16x16x32_bf16 v[68:71], a[148:151], a[88:91], v[68:71]\n"
"ds_read_b64_tr_b16 a[138:139], v42 offset:51200\n"
"s_waitcnt lgkmcnt(6)\n"
"v_mfma_f32_16x16x32_bf16 v[72:75], a[152:155], a[88:91], v[72:75]\n"
"ds_read_b64_tr_b16 a[140:141], v43 offset:49152\n"
"v_mfma_f32_16x16x32_bf16 v[104:107], a[152:155], a[92:95], v[104:107]\n"
"ds_read_b64_tr_b16 a[142:143], v43 offset:51200\n"
"v_mfma_f32_16x16x32_bf16 v[108:111], a[156:159], a[92:95], v[108:111]\n"
"ds_read_b64_tr_b16 a[144:145], v40 offset:53248\n"
"v_mfma_f32_16x16x32_bf16 v[76:79], a[156:159], a[88:91], v[76:79]\n"
"ds_read_b64_tr_b16 a[146:147], v40 offset:55296\n"
"s_waitcnt lgkmcnt(6)\n"
"v_mfma_f32_16x16x32_bf16 v[64:67], a[128:131], a[104:107], v[64:67]\n"
"ds_read_b64_tr_b16 a[148:149], v41 offset:53248\n"
"v_mfma_f32_16x16x32_bf16 v[96:99], a[128:131], a[108:111], v[96:99]\n"
"ds_read_b64_tr_b16 a[150:151], v41 offset:55296\n"
"v_mfma_f32_16x16x32_bf16 v[100:103], a[132:135], a[108:111], v[100:103]\n"
"ds_read_b64_tr_b16 a[152:153], v42 offset:53248\n"
"v_mfma_f32_16x16x32_bf16 v[68:71], a[132:135], a[104:107], v[68:71]\n"
"ds_read_b64_tr_b16 a[154:155], v42 offset:55296\n"
"s_waitcnt lgkmcnt(6)\n"
"v_mfma_f32_16x16x32_bf16 v[72:75], a[136:139], a[104:107], v[72:75]\n"
"ds_read_b64_tr_b16 a[156:157], v43 offset:53248\n"
"v_mfma_f32_16x16x32_bf16 v[104:107], a[136:139], a[108:111], v[104:107]\n"
"ds_read_b64_tr_b16 a[158:159], v43 offset:55296\n"
"v_mfma_f32_16x16x32_bf16 v[108:111], a[140:143], a[108:111], v[108:111]\n"
"ds_read_b64_tr_b16 a[128:129], v40 offset:57344\n"
"v_mfma_f32_16x16x32_bf16 v[76:79], a[140:143], a[104:107], v[76:79]\n"
"ds_read_b64_tr_b16 a[130:131], v40 offset:59392\n"
"s_waitcnt lgkmcnt(6)\n"
"v_mfma_f32_16x16x32_bf16 v[64:67], a[144:147], a[120:123], v[64:67]\n"
"ds_read_b64_tr_b16 a[132:133], v41 offset:57344\n"
"v_mfma_f32_16x16x32_bf16 v[96:99], a[144:147], a[124:127], v[96:99]\n"
"ds_read_b64_tr_b16 a[134:135], v41 offset:59392\n"
"v_mfma_f32_16x16x32_bf16 v[100:103], a[148:151], a[124:127], v[100:103]\n"
"ds_read_b64_tr_b16 a[136:137], v42 offset:57344\n"
"v_mfma_f32_16x16x32_bf16 v[68:71], a[148:151], a[120:123], v[68:71]\n"
"ds_read_b64_tr_b16 a[138:139], v42 offset:59392\n"
"s_waitcnt lgkmcnt(6)\n"
"v_mfma_f32_16x16x32_bf16 v[72:75], a[152:155], a[120:123], v[72:75]\n"
"ds_read_b64_tr_b16 a[140:141], v43 offset:57344\n"
"v_mfma_f32_16x16x32_bf16 v[104:107], a[152:155], a[124:127], v[104:107]\n"
"ds_read_b64_tr_b16 a[142:143], v43 offset:59392\n"
"v_mfma_f32_16x16x32_bf16 v[108:111], a[156:159], a[124:127], v[108:111]\n"
"ds_read_b64_tr_b16 a[144:145], v40 offset:61440\n"
"v_mfma_f32_16x16x32_bf16 v[76:79], a[156:159], a[120:123], v[76:79]\n"
"ds_read_b64_tr_b16 a[146:147], v40 offset:63488\n"
"s_waitcnt lgkmcnt(6)\n"
"v_mfma_f32_16x16x32_bf16 v[64:67], a[128:131], a[168:171], v[64:67]\n"
"ds_read_b64_tr_b16 a[148:149], v41 offset:61440\n"
"v_mfma_f32_16x16x32_bf16 v[96:99], a[128:131], a[172:175], v[96:99]\n"
"ds_read_b64_tr_b16 a[150:151], v41 offset:63488\n"
"v_mfma_f32_16x16x32_bf16 v[100:103], a[132:135], a[172:175], v[100:103]\n"
"ds_read_b64_tr_b16 a[152:153], v42 offset:61440\n"
"v_mfma_f32_16x16x32_bf16 v[68:71], a[132:135], a[168:171], v[68:71]\n"
"ds_read_b64_tr_b16 a[154:155], v42 offset:63488\n"
"s_waitcnt lgkmcnt(6)\n"
"v_mfma_f32_16x16x32_bf16 v[72:75], a[136:139], a[168:171], v[72:75]\n"
"ds_read_b64_tr_b16 a[156:157], v43 offset:61440\n"
"v_mfma_f32_16x16x32_bf16 v[104:107], a[136:139], a[172:175], v[104:107]\n"
"ds_read_b64_tr_b16 a[158:159], v43 offset:63488\n"
"v_mfma_f32_16x16x32_bf16 v[108:111], a[140:143], a[172:175], v[108:111]\n"
"v_mfma_f32_16x16x32_bf16 v[76:79], a[140:143], a[168:171], v[76:79]\n"
"s_waitcnt lgkmcnt(4)\n"
"v_mfma_f32_16x16x32_bf16 v[64:67], a[144:147], v[8:11], v[64:67]\n"
"v_mfma_f32_16x16x32_bf16 v[96:99], a[144:147], v[12:15], v[96:99]\n"
"v_mfma_f32_16x16x32_bf16 v[100:103], a[148:151], v[12:15], v[100:103]\n"
"v_mfma_f32_16x16x32_bf16 v[68:71], a[148:151], v[8:11], v[68:71]\n"
"s_waitcnt lgkmcnt(0)\n"
"v_mfma_f32_16x16x32_bf16 v[72:75], a[152:155], v[8:11], v[72:75]\n"
"v_mfma_f32_16x16x32_bf16 v[104:107], a[152:155], v[12:15], v[104:107]\n"
"v_mfma_f32_16x16x32_bf16 v[108:111], a[156:159], v[12:15], v[108:111]\n"
"v_mfma_f32_16x16x32_bf16 v[76:79], a[156:159], v[8:11], v[76:79]\n"
"s_waitcnt vmcnt(11)\n"
"v_mfma_f32_16x16x32_bf16 v[64:67], v[212:215], v[112:115], v[64:67]\n"
"v_mfma_f32_16x16x32_bf16 v[68:71], v[210:213], v[112:115], v[68:71]\n"
"s_waitcnt vmcnt(10)\n"
"v_mfma_f32_16x16x32_bf16 v[72:75], v[212:215], v[116:119], v[72:75]\n"
"v_mfma_f32_16x16x32_bf16 v[76:79], v[210:213], v[116:119], v[76:79]\n"
"s_waitcnt vmcnt(9)\n"
"v_mfma_f32_16x16x32_bf16 v[96:99], v[212:215], v[120:123], v[96:99]\n"
"v_mfma_f32_16x16x32_bf16 v[100:103], v[210:213], v[120:123], v[100:103]\n"
"s_waitcnt vmcnt(8)\n"
"v_mfma_f32_16x16x32_bf16 v[104:107], v[212:215], v[124:127], v[104:107]\n"
"v_mfma_f32_16x16x32_bf16 v[108:111], v[210:213], v[124:127], v[108:111]\n"
"s_waitcnt vmcnt(7)\n"
"v_mfma_f32_16x16x32_bf16 v[64:67], v[212:215], v[80:83], v[64:67]\n"
"v_mfma_f32_16x16x32_bf16 v[68:71], v[210:213], v[80:83], v[68:71]\n"
"s_waitcnt vmcnt(6)\n"
"v_mfma_f32_16x16x32_bf16 v[72:75], v[212:215], v[84:87], v[72:75]\n"
"v_mfma_f32_16x16x32_bf16 v[76:79], v[210:213], v[84:87], v[76:79]\n"
"s_waitcnt vmcnt(5)\n"
"v_mfma_f32_16x16x32_bf16 v[96:99], v[212:215], v[88:91], v[96:99]\n"
"v_mfma_f32_16x16x32_bf16 v[100:103], v[210:213], v[88:91], v[100:103]\n"
"s_waitcnt vmcnt(4)\n"
"v_mfma_f32_16x16x32_bf16 v[104:107], v[212:215], v[92:95], v[104:107]\n"
"v_mfma_f32_16x16x32_bf16 v[108:111], v[210:213], v[92:95], v[108:111]\n"
"s_waitcnt vmcnt(3)\n"
"v_mfma_f32_16x16x32_bf16 v[64:67], v[212:215], v[128:131], v[64:67]\n"
"v_mfma_f32_16x16x32_bf16 v[68:71], v[210:213], v[128:131], v[68:71]\n"
"s_waitcnt vmcnt(2)\n"
"v_mfma_f32_16x16x32_bf16 v[72:75], v[212:215], v[132:135], v[72:75]\n"
"v_mfma_f32_16x16x32_bf16 v[76:79], v[210:213], v[132:135], v[76:79]\n"
"s_waitcnt vmcnt(1)\n"
"v_mfma_f32_16x16x32_bf16 v[96:99], v[212:215], v[136:139], v[96:99]\n"
"v_mfma_f32_16x16x32_bf16 v[100:103], v[210:213], v[136:139], v[100:103]\n"
"s_waitcnt vmcnt(0)\n"
"v_mfma_f32_16x16x32_bf16 v[104:107], v[212:215], v[140:143], v[104:107]\n"
"v_mfma_f32_16x16x32_bf16 v[108:111], v[210:213], v[140:143], v[108:111]\n"
"v_cvt_pk_bf16_f32 v144, v64, v65\n"
"v_cvt_pk_bf16_f32 v145, v66, v67\n"
"v_cvt_pk_bf16_f32 v146, v68, v69\n"
"v_cvt_pk_bf16_f32 v147, v70, v71\n"
"v_cvt_pk_bf16_f32 v148, v72, v73\n"
"v_cvt_pk_bf16_f32 v149, v74, v75\n"
"v_cvt_pk_bf16_f32 v150, v76, v77\n"
"v_cvt_pk_bf16_f32 v151, v78, v79\n"
"v_permlane16_swap_b32 v144, v146\n"
"v_permlane16_swap_b32 v145, v147\n"
"buffer_store_dwordx4 v[144:147], v62, s[72:75], s97 offen\n"
"v_cvt_pk_bf16_f32 v152, v96, v97\n"
"v_cvt_pk_bf16_f32 v153, v98, v99\n"
"v_cvt_pk_bf16_f32 v154, v100, v101\n"
"v_cvt_pk_bf16_f32 v155, v102, v103\n"
"v_permlane16_swap_b32 v148, v150\n"
"v_permlane16_swap_b32 v149, v151\n"
"buffer_store_dwordx4 v[148:151], v62, s[72:75], s97 offen offset:64\n"
"v_cvt_pk_bf16_f32 v156, v104, v105\n"
"v_cvt_pk_bf16_f32 v157, v106, v107\n"
"v_cvt_pk_bf16_f32 v158, v108, v109\n"
"v_cvt_pk_bf16_f32 v159, v110, v111\n"
"v_permlane16_swap_b32 v152, v154\n"
"v_permlane16_swap_b32 v153, v155\n"
"buffer_store_dwordx4 v[152:155], v62, s[72:75], s96 offen\n"
"v_permlane16_swap_b32 v156, v158\n"
"v_permlane16_swap_b32 v157, v159\n"
"buffer_store_dwordx4 v[156:159], v62, s[72:75], s96 offen offset:64\n"
      : "+{v255}"(tid), "+{s[36:37]}"(x), "+{s[38:39]}"(wqkv), "+{s[42:43]}"(w1), "+{s[44:45]}"(b1)
      : "{s[40:41]}"(wo), "{s[46:47]}"(w2), "{s[48:49]}"(b2), "{s[50:51]}"(out), "{s[52:53]}"(ws), "{s54}"(blockIdx.x)
      : "memory","vcc","scc","v0","v1","v2","v3","v4","v5","v6","v7","v8","v9","v10","v11","v12","v13","v14","v15","v16","v17","v18","v19","v20",
      "v21","v22","v23","v24","v25","v26","v27","v28","v29","v30","v31","v32","v33","v34","v35","v36","v37","v38","v39","v40","v41","v42","v43","v44",
      "v45","v46","v47","v48","v49","v50","v51","v52","v53","v54","v55","v56","v57","v58","v59","v60","v61","v62","v63","v64","v65","v66","v67","v68",
      "v69","v70","v71","v72","v73","v74","v75","v76","v77","v78","v79","v80","v81","v82","v83","v84","v85","v86","v87","v88","v89","v90","v91","v92",
      "v93","v94","v95","v96","v97","v98","v99","v100","v101","v102","v103","v104","v105","v106","v107","v108","v109","v110","v111","v112","v113","v114","v115","v116",
      "v117","v118","v119","v120","v121","v122","v123","v124","v125","v126","v127","v128","v129","v130","v131","v132","v133","v134","v135","v136","v137","v138","v139","v140",
      "v141","v142","v143","v144","v145","v146","v147","v148","v149","v150","v151","v152","v153","v154","v155","v156","v157","v158","v159","v160","v161","v162","v163","v164",
      "v165","v166","v167","v168","v169","v170","v171","v172","v173","v174","v175","v176","v177","v178","v179","v180","v181","v182","v183","v184","v185","v186","v187","v188",
      "v189","v190","v191","v192","v193","v194","v195","v196","v197","v198","v199","v200","v201","v202","v203","v204","v205","v206","v207","v208","v209","v210","v211","v212",
      "v213","v214","v215","v216","v217","v218","v219","v220","v221","v222","v223","v224","v225","v226","v227","v228","v229","v230","v231","v232","v233","v234","v235","v236",
      "v237","v238","v239","v240","v241","v242","v243","v244","v245","v246","v247","v248","v249","v250","v251","v252","v253","v254","a0","a1","a2","a3","a4","a5",
      "a6","a7","a8","a9","a10","a11","a12","a13","a14","a15","a16","a17","a18","a19","a20","a21","a22","a23","a24","a25","a26","a27","a28","a29",
      "a30","a31","a32","a33","a34","a35","a36","a37","a38","a39","a40","a41","a42","a43","a44","a45","a46","a47","a48","a49","a50","a51","a52","a53",
      "a54","a55","a56","a57","a58","a59","a60","a61","a62","a63","a64","a65","a66","a67","a68","a69","a70","a71","a72","a73","a74","a75","a76","a77",
      "a78","a79","a80","a81","a82","a83","a84","a85","a86","a87","a88","a89","a90","a91","a92","a93","a94","a95","a96","a97","a98","a99","a100","a101",
      "a102","a103","a104","a105","a106","a107","a108","a109","a110","a111","a112","a113","a114","a115","a116","a117","a118","a119","a120","a121","a122","a123","a124","a125",
      "a126","a127","a128","a129","a130","a131","a132","a133","a134","a135","a136","a137","a138","a139","a140","a141","a142","a143","a144","a145","a146","a147","a148","a149",
      "a150","a151","a152","a153","a154","a155","a156","a157","a158","a159","a160","a161","a162","a163","a164","a165","a166","a167","a168","a169","a170","a171","a172","a173",
      "a174","a175","a176","a177","a178","a179","a180","a181","a182","a183","a184","a185","a186","a187","a188","a189","a190","a191","a192","a193","a194","a195","a196","a197",
      "a198","a199","a200","a201","a202","a203","a204","a205","a206","a207","a208","a209","a210","a211","a212","a213","a214","a215","a216","a217","a218","a219","a220","a221",
      "a222","a223","a224","a225","a226","a227","a228","a229","a230","a231","a232","a233","a234","a235","a236","a237","a238","a239","a240","a241","a242","a243","a244","a245",
      "a246","a247","a248","a249","a250","a251","a252","a253","a254","a255","s0","s1","s2","s3","s4","s5","s6","s7","s8","s9","s10","s11","s12","s13",
      "s14","s15","s16","s17","s18","s19","s20","s21","s22","s23","s24","s25","s26","s27","s28","s29","s30","s31","s33","s34","s35","s55","s56","s57",
      "s58","s59","s60","s61","s62","s63","s64","s65","s66","s67","s68","s69","s70","s71","s72","s73","s74","s75","s76","s77","s78","s79","s80","s81",
      "s82","s83","s84","s85","s86","s87","s88","s89","s90","s91","s92","s93","s94","s95","s96","s97","s98","s99"
  );
}

extern "C" void kernel_launch(void* const* d_in, const int* in_sizes, int n_in, void* d_out, int out_size, void* d_ws, size_t ws_size,
                              hipStream_t stream) {
  (void)hipFuncSetAttribute((const void*)fused_block_kernel, hipFuncAttributeMaxDynamicSharedMemorySize, 163840);
  fused_block_kernel<<<256, 256, 163840, stream>>>(d_in[0], d_in[1], d_in[2], d_in[4], d_in[5], d_in[6], d_in[7], d_out, d_ws);
}
```
